```python
import torch

def _softmax(x, axis=-1):
    return torch.softmax(x, dim=axis)
def _gelu(x):
    return torch.nn.functional.gelu(x, approximate="tanh")
def _silu(x):
    return torch.nn.functional.silu(x)
def _sigmoid(x):
    return torch.sigmoid(x)
def _layernorm(x, axis=-1, eps=1e-5):
    m = x.mean(dim=axis, keepdim=True)
    v = x.var(dim=axis, keepdim=True, unbiased=False)
    return (x - m) / torch.sqrt(v + eps)
def _scatter_add(tgt, idx, src, axis=0):
    return tgt.clone().index_add_(axis, idx, src)
def _segment_sum(src, seg, n):
    out = torch.zeros((n,) + src.shape[1:], dtype=src.dtype)
    return out.index_add_(0, seg, src)
def _pad(x, pw):
    flat = tuple(v for pair in reversed(pw) for v in pair)
    return torch.nn.functional.pad(x, flat)

kernel_name = 'concat_heads__mha_block_cached__mha_block_cached__near_ridge'

def setup_inputs():

    _w = (lambda a: (lambda t: t.to(torch.bfloat16) if t.is_floating_point() else t)(torch.as_tensor(a)))
    def _A_setup():
        B, Sq, D = 8, 144, 640
        H, Sk, Dh = 32, 512, 128
        return {
            "x": _w((lambda *s: torch.randn(*s, dtype=torch.bfloat16) * 0.8)(B, Sq, D)),
            "k_cache": _w((lambda *s: torch.randn(*s, dtype=torch.bfloat16) * 0.8)(B, H, Sk, Dh)),
            "v_cache": _w((lambda *s: torch.randn(*s, dtype=torch.bfloat16) * 0.05)(B, H, Sk, Dh)),
            "wq": _w((lambda *s: torch.randn(*s, dtype=torch.bfloat16) * 0.8)(D, H * Dh) * (D ** -0.5)),
            "wo": _w((lambda *s: torch.randn(*s, dtype=torch.bfloat16) * 0.05)(H * Dh, D) * ((H * Dh) ** -0.5)),
        }
    _A_in = _A_setup()

    def _B_setup():
        B, Sq, D = 8, 144, 640
        H, Sk, Dh = 32, 512, 128
        return {
            "x": _w((lambda *s: torch.randn(*s, dtype=torch.bfloat16) * 0.8)(B, Sq, D)),
            "k_cache": _w((lambda *s: torch.randn(*s, dtype=torch.bfloat16) * 0.8)(B, H, Sk, Dh)),
            "v_cache": _w((lambda *s: torch.randn(*s, dtype=torch.bfloat16) * 0.05)(B, H, Sk, Dh)),
            "wq": _w((lambda *s: torch.randn(*s, dtype=torch.bfloat16) * 0.8)(D, H * Dh) * (D ** -0.5)),
            "wo": _w((lambda *s: torch.randn(*s, dtype=torch.bfloat16) * 0.05)(H * Dh, D) * ((H * Dh) ** -0.5)),
        }
    _B_in = _B_setup()

    return {
        "A_x": _A_in["x"],
        "A_k_cache": _A_in["k_cache"],
        "A_v_cache": _A_in["v_cache"],
        "A_wq": _A_in["wq"],
        "A_wo": _A_in["wo"],
        "B_x": _B_in["x"],
        "B_k_cache": _B_in["k_cache"],
        "B_v_cache": _B_in["v_cache"],
        "B_wq": _B_in["wq"],
        "B_wo": _B_in["wo"],
    }

def reference(A_x, A_k_cache, A_v_cache, A_wq, A_wo, B_x, B_k_cache, B_v_cache, B_wq, B_wo):
    def _A_ref(x, k_cache, v_cache, wq, wo):
        B, Sq, D = x.shape
        H, Dh = 32, 128
        q = (lambda x, *d: x.permute(*d))((x @ wq).reshape(B, Sq, H, Dh), 0, 2, 1, 3)
        scores = torch.einsum("bhqd,bhkd->bhqk", q, k_cache) * (Dh ** -0.5)
        p = _softmax(scores, axis=-1)
        o = torch.einsum("bhqk,bhkd->bhqd", p, v_cache)
        return (lambda x, *d: x.permute(*d))(o, 0, 2, 1, 3).reshape(B, Sq, H * Dh) @ wo

    def _B_ref(x, k_cache, v_cache, wq, wo):
        B, Sq, D = x.shape
        H, Dh = 32, 128
        q = (lambda x, *d: x.permute(*d))((x @ wq).reshape(B, Sq, H, Dh), 0, 2, 1, 3)
        scores = torch.einsum("bhqd,bhkd->bhqk", q, k_cache) * (Dh ** -0.5)
        p = _softmax(scores, axis=-1)
        o = torch.einsum("bhqk,bhkd->bhqd", p, v_cache)
        return (lambda x, *d: x.permute(*d))(o, 0, 2, 1, 3).reshape(B, Sq, H * Dh) @ wo

    a = _A_ref(A_x, A_k_cache, A_v_cache, A_wq, A_wo)
    b = _B_ref(B_x, B_k_cache, B_v_cache, B_wq, B_wo)
    return torch.concatenate([a, b], axis=-1)
```

```cpp
#include <hip/hip_runtime.h>
#include <hip/hip_bf16.h>
#include <stdint.h>

typedef unsigned short u16;
static __device__ __forceinline__ float bf2f(u16 v){ return __uint_as_float(((unsigned)v)<<16); }
static __device__ __forceinline__ u16 f2bf(float f){ unsigned u=__float_as_uint(f); u += 0x7fffu + ((u>>16)&1u); return (u16)(u>>16); }

constexpr int B_=8,SQ=144,D_=640,H_=32,SK=512,DH=128;

__global__ void k_qproj(const u16* x,const u16* wq,u16* q){
  int n = blockIdx.x*256+threadIdx.x;
  int m = blockIdx.y;
  float acc=0.f;
  for(int c=0;c<D_;++c) acc += bf2f(x[m*D_+c])*bf2f(wq[(size_t)c*4096+n]);
  q[(size_t)m*4096+n]=f2bf(acc);
}
__global__ void k_attn(const u16* q,const u16* k,const u16* v,u16* o){
  int s=blockIdx.x,h=blockIdx.y,b=blockIdx.z; int t=threadIdx.x;
  __shared__ float qs[128]; __shared__ float p[512]; __shared__ float red[128];
  qs[t]=bf2f(q[((size_t)(b*SQ+s))*4096+h*128+t]);
  __syncthreads();
  const u16* kb=k+((size_t)(b*H_+h))*SK*DH; const u16* vb=v+((size_t)(b*H_+h))*SK*DH;
  float mx=-1e30f;
  for(int j=t;j<SK;j+=128){ float a=0.f; for(int d=0;d<128;++d) a+=qs[d]*bf2f(kb[j*128+d]); a*=0.08838834764831845f; p[j]=a; mx=fmaxf(mx,a);}
  red[t]=mx; __syncthreads();
  for(int o2=64;o2>0;o2>>=1){ if(t<o2) red[t]=fmaxf(red[t],red[t+o2]); __syncthreads(); }
  mx=red[0]; __syncthreads();
  float sum=0.f;
  for(int j=t;j<SK;j+=128){ float e=__expf(p[j]-mx); p[j]=e; sum+=e; }
  red[t]=sum; __syncthreads();
  for(int o2=64;o2>0;o2>>=1){ if(t<o2) red[t]+=red[t+o2]; __syncthreads(); }
  sum=red[0];
  float acc=0.f;
  for(int j=0;j<SK;++j) acc+=p[j]*bf2f(vb[j*128+t]);
  o[((size_t)(b*SQ+s))*4096+h*128+t]=f2bf(acc/sum);
}
__global__ void k_oproj(const u16* o,const u16* wo,u16* out,int coloff){
  int n=blockIdx.x*128+threadIdx.x; int m=blockIdx.y;
  float acc=0.f;
  for(int c=0;c<4096;++c) acc+=bf2f(o[(size_t)m*4096+c])*bf2f(wo[(size_t)c*640+n]);
  out[(size_t)m*1280+coloff+n]=f2bf(acc);
}

extern "C" void kernel_launch(void* const* d_in, const int* in_sizes, int n_in,
                              void* d_out, int out_size, void* d_ws, size_t ws_size,
                              hipStream_t stream) {
  u16* q=(u16*)d_ws; u16* o=q+(size_t)1152*4096;
  for(int br=0;br<2;++br){
    const u16* x=(const u16*)d_in[br*5+0]; const u16* k=(const u16*)d_in[br*5+1]; const u16* v=(const u16*)d_in[br*5+2];
    const u16* wq=(const u16*)d_in[br*5+3]; const u16* wo=(const u16*)d_in[br*5+4];
    k_qproj<<<dim3(16,1152),256,0,stream>>>(x,wq,q);
    k_attn<<<dim3(SQ,H_,B_),128,0,stream>>>(q,k,v,o);
    k_oproj<<<dim3(5,1152),128,0,stream>>>(o,wo,(u16*)d_out,br*640);
  }
}
```

```cpp
#include <hip/hip_runtime.h>
#include <hip/hip_bf16.h>
#include <stdint.h>
typedef unsigned short u16;
typedef unsigned u32x4 __attribute__((ext_vector_type(4)));
static __device__ __forceinline__ float bf2f(u16 v){ return __uint_as_float(((unsigned)v)<<16); }
static __device__ __forceinline__ u16 f2bf(float f){ unsigned u=__float_as_uint(f); u += 0x7fffu + ((u>>16)&1u); return (u16)(u>>16); }
static __device__ __forceinline__ u32x4 mk_srd(const void* p, unsigned bytes){
  unsigned long long a=(unsigned long long)p; u32x4 r; r[0]=(unsigned)a; r[1]=(unsigned)(a>>32)&0xffffu; r[2]=bytes; r[3]=0x00020000u; return r; }
#define FUSED 1
constexpr size_t WS_X2=(size_t)32<<20, WS_FLAGS=(size_t)64<<20;
constexpr int HCH=72*4096;

__global__ __launch_bounds__(512,2) void p1_kernel(const u16* __restrict__ xA,const u16* __restrict__ kA,const u16* __restrict__ vA,const u16* __restrict__ wqA,const u16* __restrict__ woA,
                                                   const u16* __restrict__ xB,const u16* __restrict__ kB,const u16* __restrict__ vB,const u16* __restrict__ wqB,const u16* __restrict__ woB,
                                                   char* __restrict__ ws,u16* __restrict__ out){
  extern __shared__ __attribute__((aligned(16))) char lds[];
  const int bi=blockIdx.x, xcd=bi&7, j=bi>>3, br=xcd>>2, hg=xcd&3, jx=j&3, b=j>>2, h0=hg*8+jx*2, grp=br*8+b;
  const u16* x=br?xB:xA; const u16* k=br?kB:kA; const u16* v=br?vB:vA; const u16* wq=br?wqB:wqA; const u16* wo=br?woB:woA;
  const u32x4 srdx=mk_srd((const char*)x+(size_t)b*144*1280, 144*1280);
  const u32x4 srdw=mk_srd((const char*)wq+h0*256, 640*8192-h0*256);
  const u32x4 srdk=mk_srd((const char*)k+(size_t)(b*32+h0)*131072, 262144);
  const u32x4 srdv=mk_srd((const char*)v+(size_t)(b*32+h0)*131072, 262144);
  const u32x4 srdo=mk_srd(ws+(size_t)(grp*4+hg)*(4*73728), 4*73728);
  const u32x4 srdwo=mk_srd((const char*)wo+(size_t)hg*1024*1280+jx*320, 1024*1280-jx*320);
  const u32x4 srdx2=mk_srd(ws+WS_X2+(size_t)(grp*4+jx)*393216, 393216);
  const unsigned oc=br*1280+jx*320;
  const u32x4 srdout=mk_srd((char*)out+(size_t)b*144*2560+oc, 144*2560-oc);
  const u32x4 srdf=mk_srd(ws+WS_FLAGS, 34816+131072);
  const unsigned wave=__builtin_amdgcn_readfirstlane(threadIdx.x>>6);
  const unsigned ldsb=(unsigned)(size_t)lds;
  const u32x4 srdts=mk_srd(ws+((size_t)128<<20)+(size_t)bi*8192, 8192);
  asm volatile(
  "s_mov_b32 s35, 0\n"
  "v_mbcnt_lo_u32_b32 v1, -1, 0\n"
  "v_mbcnt_hi_u32_b32 v1, -1, v1\n"
  "s_lshr_b32 s36, %[wave], 2\n"
  "s_and_b32 s37, %[wave], 3\n"
  "v_lshrrev_b32 v2, 4, v1\n"
  "v_bfe_u32 v3, v1, 2, 2\n"
  "v_and_b32 v4, 3, v1\n"
  "v_and_b32 v5, 15, v1\n"
  "v_mov_b32 v6, 0\n"
  "s_lshl_b32 s38, %[grp], 7\n"
  "buffer_load_dword v33, v6, %[srdf], s38 offen offset:0 sc1\n"
  "v_bfe_u32 v6, v1, 2, 3\n"
  "v_mul_u32_u24 v6, 1280, v6\n"
  "v_lshrrev_b32 v7, 5, v1\n"
  "v_lshl_add_u32 v6, v7, 6, v6\n"
  "v_and_b32 v7, 1, v2\n"
  "v_lshlrev_b32 v7, 1, v7\n"
  "v_xor_b32 v7, v7, v4\n"
  "v_lshl_add_u32 v6, v7, 4, v6\n"
  "s_mul_i32 s38, %[wave], 20480\n"
  "v_add_u32 v30, s38, v6\n"
  "s_add_u32 s38, s38, 9216\n"
  "v_add_u32 v31, s38, v6\n"
  "s_add_u32 s38, s36, 16\n"
  "s_mul_i32 s39, s38, 10240\n"
  "v_add_u32 v32, s39, v6\n"
  "s_lshl_b32 s47, s38, 10\n"
  "s_add_u32 s47, s47, 65536\n"
  "s_add_u32 s47, s47, %[ldsb]\n"
  "s_lshl_b32 s45, %[wave], 11\n"
  "s_add_u32 s43, s45, %[ldsb]\n"
  "s_add_u32 s45, s43, 65536\n"
  "s_mov_b32 s44, 0\n"
  "s_mov_b32 s42, 0\n"
  "s_lshl_b32 s38, s37, 4\n"
  "s_lshl_b64 s[48:49], 0xffff, s38\n"
  "v_lshlrev_b32 v6, 6, v2\n"
  "v_lshl_add_u32 v6, v3, 13, v6\n"
  "s_and_b32 s38, s37, 1\n"
  "s_lshl_b32 s38, s38, 1\n"
  "v_xor_b32 v7, s38, v4\n"
  "v_lshl_add_u32 v6, v7, 4, v6\n"
  "s_lshl_b32 s38, s37, 16\n"
  "s_lshl_b32 s39, s36, 8\n"
  "s_add_u32 s38, s38, s39\n"
  "v_add_u32 v34, s38, v6\n"
  "s_add_u32 s38, s38, 31744\n"
  "v_add_u32 v35, s38, v6\n"
  "s_add_u32 m0, s45, 0\n"
  "s_nop 0\n"
  "buffer_load_dwordx4 v30, %[srdx], s44 offen offset:0 lds\n"
  "buffer_load_dwordx4 v31, %[srdx], s44 offen offset:1024 lds\n"
  "s_add_u32 m0, s47, 0\n"
  "s_mov_b64 exec, s[48:49]\n"
  "buffer_load_dwordx4 v32, %[srdx], s44 offen offset:0 lds\n"
  "s_mov_b64 exec, -1\n"
  "s_add_u32 s44, s44, 128\n"
  "s_add_u32 m0, s43, 0\n"
  "s_nop 0\n"
  "buffer_load_dwordx4 v34, %[srdw], s42 offen offset:0 lds\n"
  "buffer_load_dwordx4 v35, %[srdw], s42 offen offset:1024 lds\n"
  "s_add_u32 s42, s42, 262144\n"
  "s_add_u32 m0, s45, 18432\n"
  "s_nop 0\n"
  "buffer_load_dwordx4 v30, %[srdx], s44 offen offset:0 lds\n"
  "buffer_load_dwordx4 v31, %[srdx], s44 offen offset:1024 lds\n"
  "s_add_u32 m0, s47, 18432\n"
  "s_mov_b64 exec, s[48:49]\n"
  "buffer_load_dwordx4 v32, %[srdx], s44 offen offset:0 lds\n"
  "s_mov_b64 exec, -1\n"
  "s_add_u32 s44, s44, 128\n"
  "s_add_u32 m0, s43, 16384\n"
  "s_nop 0\n"
  "buffer_load_dwordx4 v34, %[srdw], s42 offen offset:0 lds\n"
  "buffer_load_dwordx4 v35, %[srdw], s42 offen offset:1024 lds\n"
  "s_add_u32 s42, s42, 262144\n"
  "s_add_u32 m0, s43, 32768\n"
  "s_nop 0\n"
  "buffer_load_dwordx4 v34, %[srdw], s42 offen offset:0 lds\n"
  "buffer_load_dwordx4 v35, %[srdw], s42 offen offset:1024 lds\n"
  "s_add_u32 s42, s42, 262144\n"
  "v_lshlrev_b32 v6, 6, v2\n"
  "v_lshl_add_u32 v6, v3, 8, v6\n"
  "v_lshl_add_u32 v10, v4, 4, v6\n"
  "v_xor_b32 v11, 32, v10\n"
  "v_lshlrev_b32 v6, 11, v2\n"
  "v_lshl_add_u32 v6, v3, 6, v6\n"
  "v_lshrrev_b32 v7, 1, v4\n"
  "v_lshl_add_u32 v6, v7, 4, v6\n"
  "v_and_b32 v7, 1, v4\n"
  "v_lshl_add_u32 v6, v7, 3, v6\n"
  "v_and_b32 v7, 1, v2\n"
  "s_lshl_b32 s39, s36, 13\n"
  "s_add_u32 s39, s39, 0\n"
  "s_add_u32 s39, s39, %[ldsb]\n"
  "s_xor_b32 s40, s37, 0\n"
  "s_lshl_b32 s40, s40, 8\n"
  "s_add_u32 s40, s40, s39\n"
  "v_xor_b32 v8, 0, v7\n"
  "v_lshl_add_u32 v8, v8, 5, v6\n"
  "v_add_u32 v22, s40, v8\n"
  "v_xor_b32 v8, 1, v7\n"
  "v_lshl_add_u32 v8, v8, 5, v6\n"
  "v_add_u32 v23, s40, v8\n"
  "s_xor_b32 s40, s37, 1\n"
  "s_lshl_b32 s40, s40, 8\n"
  "s_add_u32 s40, s40, s39\n"
  "v_xor_b32 v8, 0, v7\n"
  "v_lshl_add_u32 v8, v8, 5, v6\n"
  "v_add_u32 v24, s40, v8\n"
  "v_xor_b32 v8, 1, v7\n"
  "v_lshl_add_u32 v8, v8, 5, v6\n"
  "v_add_u32 v25, s40, v8\n"
  "s_xor_b32 s40, s37, 2\n"
  "s_lshl_b32 s40, s40, 8\n"
  "s_add_u32 s40, s40, s39\n"
  "v_xor_b32 v8, 0, v7\n"
  "v_lshl_add_u32 v8, v8, 5, v6\n"
  "v_add_u32 v26, s40, v8\n"
  "v_xor_b32 v8, 1, v7\n"
  "v_lshl_add_u32 v8, v8, 5, v6\n"
  "v_add_u32 v27, s40, v8\n"
  "s_xor_b32 s40, s37, 3\n"
  "s_lshl_b32 s40, s40, 8\n"
  "s_add_u32 s40, s40, s39\n"
  "v_xor_b32 v8, 0, v7\n"
  "v_lshl_add_u32 v8, v8, 5, v6\n"
  "v_add_u32 v28, s40, v8\n"
  "v_xor_b32 v8, 1, v7\n"
  "v_lshl_add_u32 v8, v8, 5, v6\n"
  "v_add_u32 v29, s40, v8\n"
  "v_lshrrev_b32 v6, 3, v5\n"
  "v_lshlrev_b32 v6, 10, v6\n"
  "v_and_b32 v7, 7, v5\n"
  "v_lshl_add_u32 v6, v7, 6, v6\n"
  "v_and_b32 v7, 1, v3\n"
  "v_lshlrev_b32 v7, 1, v7\n"
  "v_xor_b32 v7, v7, v2\n"
  "v_lshl_add_u32 v6, v7, 4, v6\n"
  "s_lshl_b32 s39, s37, 12\n"
  "s_add_u32 s39, s39, 65536\n"
  "s_add_u32 s39, s39, %[ldsb]\n"
  "v_add_u32 v20, s39, v6\n"
  "s_add_u32 s39, %[ldsb], 81920\n"
  "v_add_u32 v21, s39, v6\n"
  "v_lshlrev_b32 v17, 4, v1\n"
  "s_lshl_b32 s50, s36, 17\n"
  "s_lshl_b32 s39, s37, 12\n"
  "s_add_u32 s50, s50, s39\n"
  "s_lshl_b32 s51, s36, 15\n"
  "s_add_u32 s51, s51, s39\n"
  "s_add_u32 s51, s51, %[ldsb]\n"
  "s_lshl_b32 s38, s36, 15\n"
  "s_add_u32 s38, s38, %[ldsb]\n"
  "v_lshlrev_b32 v6, 10, v3\n"
  "v_lshl_add_u32 v6, v4, 6, v6\n"
  "v_and_b32 v7, 1, v3\n"
  "v_lshlrev_b32 v7, 1, v7\n"
  "v_xor_b32 v7, v7, v2\n"
  "v_lshl_add_u32 v6, v7, 4, v6\n"
  "v_add_u32 v12, s38, v6\n"
  "v_add_u32 v240, 65536, v12\n"
  "v_lshlrev_b32 v6, 10, v2\n"
  "v_lshl_add_u32 v6, v3, 6, v6\n"
  "v_lshrrev_b32 v7, 1, v4\n"
  "v_lshl_add_u32 v6, v7, 4, v6\n"
  "v_and_b32 v7, 1, v4\n"
  "v_lshl_add_u32 v6, v7, 3, v6\n"
  "v_and_b32 v7, 1, v2\n"
  "s_add_u32 s39, s38, 16384\n"
  "v_xor_b32 v5, 0, v7\n"
  "v_lshl_add_u32 v5, v5, 5, v6\n"
  "v_add_u32 v13, s39, v5\n"
  "v_add_u32 v241, 65536, v13\n"
  "v_xor_b32 v5, 1, v7\n"
  "v_lshl_add_u32 v5, v5, 5, v6\n"
  "v_add_u32 v14, s39, v5\n"
  "v_add_u32 v242, 65536, v14\n"
  "s_lshl_b32 s39, s36, 11\n"
  "s_add_u32 s39, s39, 159744\n"
  "s_add_u32 s39, s39, %[ldsb]\n"
  "v_lshlrev_b32 v6, 3, v1\n"
  "v_add_u32 v16, s39, v6\n"
  "s_lshl_b32 s40, s37, 9\n"
  "v_add_u32 v15, s40, v16\n"
  "s_lshl_b32 s39, s36, 12\n"
  "s_add_u32 s39, s39, 151552\n"
  "s_add_u32 s39, s39, %[ldsb]\n"
  "v_add_u32 v246, s39, v17\n"
  "v_mov_b64 v[44:45], 0\n"
  "v_mov_b64 v[46:47], 0\n"
  "v_mov_b64 v[48:49], 0\n"
  "v_mov_b64 v[50:51], 0\n"
  "v_mov_b64 v[52:53], 0\n"
  "v_mov_b64 v[54:55], 0\n"
  "v_mov_b64 v[56:57], 0\n"
  "v_mov_b64 v[58:59], 0\n"
  "v_mov_b64 v[60:61], 0\n"
  "v_mov_b64 v[62:63], 0\n"
  "v_mov_b64 v[64:65], 0\n"
  "v_mov_b64 v[66:67], 0\n"
  "v_mov_b64 v[68:69], 0\n"
  "v_mov_b64 v[70:71], 0\n"
  "v_mov_b64 v[72:73], 0\n"
  "v_mov_b64 v[74:75], 0\n"
  "v_mov_b64 v[76:77], 0\n"
  "v_mov_b64 v[78:79], 0\n"
  "v_mov_b64 v[80:81], 0\n"
  "v_mov_b64 v[82:83], 0\n"
  "v_mov_b64 v[84:85], 0\n"
  "v_mov_b64 v[86:87], 0\n"
  "v_mov_b64 v[88:89], 0\n"
  "v_mov_b64 v[90:91], 0\n"
  "v_mov_b64 v[92:93], 0\n"
  "v_mov_b64 v[94:95], 0\n"
  "v_mov_b64 v[96:97], 0\n"
  "v_mov_b64 v[98:99], 0\n"
  "v_mov_b64 v[100:101], 0\n"
  "v_mov_b64 v[102:103], 0\n"
  "v_mov_b64 v[104:105], 0\n"
  "v_mov_b64 v[106:107], 0\n"
  "v_mov_b64 v[108:109], 0\n"
  "v_mov_b64 v[110:111], 0\n"
  "v_mov_b64 v[112:113], 0\n"
  "v_mov_b64 v[114:115], 0\n"
  "s_waitcnt vmcnt(7)\n"
  "s_barrier\n"
  "ds_read_b64_tr_b16 v[128:129], v22 offset:0\n"
  "ds_read_b64_tr_b16 v[130:131], v22 offset:1024\n"
  "ds_read_b128 v[116:119], v20 offset:0\n"
  "ds_read_b128 v[120:123], v20 offset:2048\n"
  "ds_read_b128 v[124:127], v21 offset:0\n"
  "ds_read_b64_tr_b16 v[132:133], v23 offset:0\n"
  "ds_read_b64_tr_b16 v[134:135], v23 offset:1024\n"
  "ds_read_b64_tr_b16 v[136:137], v24 offset:0\n"
  "ds_read_b64_tr_b16 v[138:139], v24 offset:1024\n"
  "ds_read_b64_tr_b16 v[140:141], v25 offset:0\n"
  "ds_read_b64_tr_b16 v[142:143], v25 offset:1024\n"
  "ds_read_b64_tr_b16 v[144:145], v26 offset:0\n"
  "ds_read_b64_tr_b16 v[146:147], v26 offset:1024\n"
  "ds_read_b64_tr_b16 v[148:149], v27 offset:0\n"
  "ds_read_b64_tr_b16 v[150:151], v27 offset:1024\n"
  "ds_read_b64_tr_b16 v[152:153], v28 offset:0\n"
  "ds_read_b64_tr_b16 v[154:155], v28 offset:1024\n"
  "ds_read_b64_tr_b16 v[156:157], v29 offset:0\n"
  "ds_read_b64_tr_b16 v[158:159], v29 offset:1024\n"
  "s_add_u32 m0, s43, 49152\n"
  "s_nop 0\n"
  "buffer_load_dwordx4 v34, %[srdw], s42 offen offset:0 lds\n"
  "buffer_load_dwordx4 v35, %[srdw], s42 offen offset:1024 lds\n"
  "s_add_u32 s42, s42, 262144\n"
  "s_add_u32 m0, s45, 36864\n"
  "s_nop 0\n"
  "buffer_load_dwordx4 v30, %[srdx], s44 offen offset:0 lds\n"
  "buffer_load_dwordx4 v31, %[srdx], s44 offen offset:1024 lds\n"
  "s_add_u32 m0, s47, 36864\n"
  "s_mov_b64 exec, s[48:49]\n"
  "buffer_load_dwordx4 v32, %[srdx], s44 offen offset:0 lds\n"
  "s_mov_b64 exec, -1\n"
  "s_add_u32 s44, s44, 128\n"
  "s_waitcnt vmcnt(7) lgkmcnt(0)\n"
  "s_barrier\n"
  "v_mfma_f32_16x16x32_bf16 v[44:47], v[128:131], v[116:119], v[44:47]\n"
  "s_setprio 3\n"
  "ds_read_b64_tr_b16 v[172:173], v22 offset:16384\n"
  "ds_read_b64_tr_b16 v[174:175], v22 offset:17408\n"
  "v_mfma_f32_16x16x32_bf16 v[76:79], v[128:131], v[120:123], v[76:79]\n"
  "ds_read_b128 v[160:163], v20 offset:512\n"
  "ds_read_b128 v[164:167], v20 offset:2560\n"
  "s_add_u32 m0, s43, 0\n"
  "s_nop 0\n"
  "buffer_load_dwordx4 v34, %[srdw], s42 offen offset:0 lds\n"
  "v_mfma_f32_16x16x32_bf16 v[108:111], v[128:131], v[124:127], v[108:111]\n"
  "ds_read_b128 v[168:171], v21 offset:512\n"
  "v_mfma_f32_16x16x32_bf16 v[48:51], v[132:135], v[116:119], v[48:51]\n"
  "ds_read_b64_tr_b16 v[176:177], v23 offset:16384\n"
  "ds_read_b64_tr_b16 v[178:179], v23 offset:17408\n"
  "v_mfma_f32_16x16x32_bf16 v[80:83], v[132:135], v[120:123], v[80:83]\n"
  "s_setprio 2\n"
  "ds_read_b64_tr_b16 v[180:181], v24 offset:16384\n"
  "buffer_load_dwordx4 v35, %[srdw], s42 offen offset:1024 lds\n"
  "s_add_u32 s42, s42, 262144\n"
  "v_mfma_f32_16x16x32_bf16 v[112:115], v[132:135], v[124:127], v[112:115]\n"
  "ds_read_b64_tr_b16 v[182:183], v24 offset:17408\n"
  "ds_read_b64_tr_b16 v[184:185], v25 offset:16384\n"
  "v_mfma_f32_16x16x32_bf16 v[52:55], v[136:139], v[116:119], v[52:55]\n"
  "ds_read_b64_tr_b16 v[186:187], v25 offset:17408\n"
  "v_mfma_f32_16x16x32_bf16 v[84:87], v[136:139], v[120:123], v[84:87]\n"
  "ds_read_b64_tr_b16 v[188:189], v26 offset:16384\n"
  "ds_read_b64_tr_b16 v[190:191], v26 offset:17408\n"
  "v_mfma_f32_16x16x32_bf16 v[56:59], v[140:143], v[116:119], v[56:59]\n"
  "s_setprio 1\n"
  "ds_read_b64_tr_b16 v[192:193], v27 offset:16384\n"
  "v_mfma_f32_16x16x32_bf16 v[88:91], v[140:143], v[120:123], v[88:91]\n"
  "ds_read_b64_tr_b16 v[194:195], v27 offset:17408\n"
  "v_mfma_f32_16x16x32_bf16 v[60:63], v[144:147], v[116:119], v[60:63]\n"
  "ds_read_b64_tr_b16 v[196:197], v28 offset:16384\n"
  "v_mfma_f32_16x16x32_bf16 v[92:95], v[144:147], v[120:123], v[92:95]\n"
  "ds_read_b64_tr_b16 v[198:199], v28 offset:17408\n"
  "v_mfma_f32_16x16x32_bf16 v[64:67], v[148:151], v[116:119], v[64:67]\n"
  "s_setprio 0\n"
  "ds_read_b64_tr_b16 v[200:201], v29 offset:16384\n"
  "v_mfma_f32_16x16x32_bf16 v[96:99], v[148:151], v[120:123], v[96:99]\n"
  "ds_read_b64_tr_b16 v[202:203], v29 offset:17408\n"
  "v_mfma_f32_16x16x32_bf16 v[68:71], v[152:155], v[116:119], v[68:71]\n"
  "v_mfma_f32_16x16x32_bf16 v[100:103], v[152:155], v[120:123], v[100:103]\n"
  "v_mfma_f32_16x16x32_bf16 v[72:75], v[156:159], v[116:119], v[72:75]\n"
  "v_mfma_f32_16x16x32_bf16 v[104:107], v[156:159], v[120:123], v[104:107]\n"
  "s_waitcnt vmcnt(7) lgkmcnt(0)\n"
  "s_barrier\n"
  "v_mfma_f32_16x16x32_bf16 v[44:47], v[172:175], v[160:163], v[44:47]\n"
  "s_setprio 3\n"
  "ds_read_b64_tr_b16 v[128:129], v22 offset:32768\n"
  "ds_read_b64_tr_b16 v[130:131], v22 offset:33792\n"
  "v_mfma_f32_16x16x32_bf16 v[76:79], v[172:175], v[164:167], v[76:79]\n"
  "ds_read_b128 v[116:119], v20 offset:18432\n"
  "ds_read_b128 v[120:123], v20 offset:20480\n"
  "s_add_u32 m0, s43, 16384\n"
  "s_nop 0\n"
  "buffer_load_dwordx4 v34, %[srdw], s42 offen offset:0 lds\n"
  "v_mfma_f32_16x16x32_bf16 v[108:111], v[172:175], v[168:171], v[108:111]\n"
  "ds_read_b128 v[124:127], v21 offset:18432\n"
  "v_mfma_f32_16x16x32_bf16 v[48:51], v[176:179], v[160:163], v[48:51]\n"
  "ds_read_b64_tr_b16 v[132:133], v23 offset:32768\n"
  "ds_read_b64_tr_b16 v[134:135], v23 offset:33792\n"
  "v_mfma_f32_16x16x32_bf16 v[80:83], v[176:179], v[164:167], v[80:83]\n"
  "s_setprio 2\n"
  "ds_read_b64_tr_b16 v[136:137], v24 offset:32768\n"
  "buffer_load_dwordx4 v35, %[srdw], s42 offen offset:1024 lds\n"
  "s_add_u32 s42, s42, 262144\n"
  "v_mfma_f32_16x16x32_bf16 v[112:115], v[176:179], v[168:171], v[112:115]\n"
  "ds_read_b64_tr_b16 v[138:139], v24 offset:33792\n"
  "ds_read_b64_tr_b16 v[140:141], v25 offset:32768\n"
  "v_mfma_f32_16x16x32_bf16 v[52:55], v[180:183], v[160:163], v[52:55]\n"
  "ds_read_b64_tr_b16 v[142:143], v25 offset:33792\n"
  "v_mfma_f32_16x16x32_bf16 v[84:87], v[180:183], v[164:167], v[84:87]\n"
  "ds_read_b64_tr_b16 v[144:145], v26 offset:32768\n"
  "ds_read_b64_tr_b16 v[146:147], v26 offset:33792\n"
  "s_add_u32 m0, s45, 0\n"
  "s_nop 0\n"
  "buffer_load_dwordx4 v30, %[srdx], s44 offen offset:0 lds\n"
  "v_mfma_f32_16x16x32_bf16 v[56:59], v[184:187], v[160:163], v[56:59]\n"
  "s_setprio 1\n"
  "ds_read_b64_tr_b16 v[148:149], v27 offset:32768\n"
  "v_mfma_f32_16x16x32_bf16 v[88:91], v[184:187], v[164:167], v[88:91]\n"
  "ds_read_b64_tr_b16 v[150:151], v27 offset:33792\n"
  "v_mfma_f32_16x16x32_bf16 v[60:63], v[188:191], v[160:163], v[60:63]\n"
  "ds_read_b64_tr_b16 v[152:153], v28 offset:32768\n"
  "buffer_load_dwordx4 v31, %[srdx], s44 offen offset:1024 lds\n"
  "v_mfma_f32_16x16x32_bf16 v[92:95], v[188:191], v[164:167], v[92:95]\n"
  "ds_read_b64_tr_b16 v[154:155], v28 offset:33792\n"
  "v_mfma_f32_16x16x32_bf16 v[64:67], v[192:195], v[160:163], v[64:67]\n"
  "s_setprio 0\n"
  "ds_read_b64_tr_b16 v[156:157], v29 offset:32768\n"
  "v_mfma_f32_16x16x32_bf16 v[96:99], v[192:195], v[164:167], v[96:99]\n"
  "ds_read_b64_tr_b16 v[158:159], v29 offset:33792\n"
  "s_add_u32 m0, s47, 0\n"
  "s_mov_b64 exec, s[48:49]\n"
  "buffer_load_dwordx4 v32, %[srdx], s44 offen offset:0 lds\n"
  "s_mov_b64 exec, -1\n"
  "s_add_u32 s44, s44, 128\n"
  "v_mfma_f32_16x16x32_bf16 v[68:71], v[196:199], v[160:163], v[68:71]\n"
  "v_mfma_f32_16x16x32_bf16 v[100:103], v[196:199], v[164:167], v[100:103]\n"
  "v_mfma_f32_16x16x32_bf16 v[72:75], v[200:203], v[160:163], v[72:75]\n"
  "v_mfma_f32_16x16x32_bf16 v[104:107], v[200:203], v[164:167], v[104:107]\n"
  "s_waitcnt vmcnt(10) lgkmcnt(0)\n"
  "s_barrier\n"
  "v_mfma_f32_16x16x32_bf16 v[44:47], v[128:131], v[116:119], v[44:47]\n"
  "s_setprio 3\n"
  "ds_read_b64_tr_b16 v[172:173], v22 offset:49152\n"
  "ds_read_b64_tr_b16 v[174:175], v22 offset:50176\n"
  "v_mfma_f32_16x16x32_bf16 v[76:79], v[128:131], v[120:123], v[76:79]\n"
  "ds_read_b128 v[160:163], v20 offset:18944\n"
  "ds_read_b128 v[164:167], v20 offset:20992\n"
  "s_add_u32 m0, s43, 32768\n"
  "s_nop 0\n"
  "buffer_load_dwordx4 v34, %[srdw], s42 offen offset:0 lds\n"
  "v_mfma_f32_16x16x32_bf16 v[108:111], v[128:131], v[124:127], v[108:111]\n"
  "ds_read_b128 v[168:171], v21 offset:18944\n"
  "v_mfma_f32_16x16x32_bf16 v[48:51], v[132:135], v[116:119], v[48:51]\n"
  "ds_read_b64_tr_b16 v[176:177], v23 offset:49152\n"
  "ds_read_b64_tr_b16 v[178:179], v23 offset:50176\n"
  "v_mfma_f32_16x16x32_bf16 v[80:83], v[132:135], v[120:123], v[80:83]\n"
  "s_setprio 2\n"
  "ds_read_b64_tr_b16 v[180:181], v24 offset:49152\n"
  "buffer_load_dwordx4 v35, %[srdw], s42 offen offset:1024 lds\n"
  "s_add_u32 s42, s42, 262144\n"
  "v_mfma_f32_16x16x32_bf16 v[112:115], v[132:135], v[124:127], v[112:115]\n"
  "ds_read_b64_tr_b16 v[182:183], v24 offset:50176\n"
  "ds_read_b64_tr_b16 v[184:185], v25 offset:49152\n"
  "v_mfma_f32_16x16x32_bf16 v[52:55], v[136:139], v[116:119], v[52:55]\n"
  "ds_read_b64_tr_b16 v[186:187], v25 offset:50176\n"
  "v_mfma_f32_16x16x32_bf16 v[84:87], v[136:139], v[120:123], v[84:87]\n"
  "ds_read_b64_tr_b16 v[188:189], v26 offset:49152\n"
  "ds_read_b64_tr_b16 v[190:191], v26 offset:50176\n"
  "v_mfma_f32_16x16x32_bf16 v[56:59], v[140:143], v[116:119], v[56:59]\n"
  "s_setprio 1\n"
  "ds_read_b64_tr_b16 v[192:193], v27 offset:49152\n"
  "v_mfma_f32_16x16x32_bf16 v[88:91], v[140:143], v[120:123], v[88:91]\n"
  "ds_read_b64_tr_b16 v[194:195], v27 offset:50176\n"
  "v_mfma_f32_16x16x32_bf16 v[60:63], v[144:147], v[116:119], v[60:63]\n"
  "ds_read_b64_tr_b16 v[196:197], v28 offset:49152\n"
  "v_mfma_f32_16x16x32_bf16 v[92:95], v[144:147], v[120:123], v[92:95]\n"
  "ds_read_b64_tr_b16 v[198:199], v28 offset:50176\n"
  "v_mfma_f32_16x16x32_bf16 v[64:67], v[148:151], v[116:119], v[64:67]\n"
  "s_setprio 0\n"
  "ds_read_b64_tr_b16 v[200:201], v29 offset:49152\n"
  "v_mfma_f32_16x16x32_bf16 v[96:99], v[148:151], v[120:123], v[96:99]\n"
  "ds_read_b64_tr_b16 v[202:203], v29 offset:50176\n"
  "v_mfma_f32_16x16x32_bf16 v[68:71], v[152:155], v[116:119], v[68:71]\n"
  "v_mfma_f32_16x16x32_bf16 v[100:103], v[152:155], v[120:123], v[100:103]\n"
  "v_mfma_f32_16x16x32_bf16 v[72:75], v[156:159], v[116:119], v[72:75]\n"
  "v_mfma_f32_16x16x32_bf16 v[104:107], v[156:159], v[120:123], v[104:107]\n"
  "s_waitcnt vmcnt(7) lgkmcnt(0)\n"
  "s_barrier\n"
  "v_mfma_f32_16x16x32_bf16 v[44:47], v[172:175], v[160:163], v[44:47]\n"
  "s_setprio 3\n"
  "ds_read_b64_tr_b16 v[128:129], v22 offset:0\n"
  "ds_read_b64_tr_b16 v[130:131], v22 offset:1024\n"
  "v_mfma_f32_16x16x32_bf16 v[76:79], v[172:175], v[164:167], v[76:79]\n"
  "ds_read_b128 v[116:119], v20 offset:36864\n"
  "ds_read_b128 v[120:123], v20 offset:38912\n"
  "s_add_u32 m0, s43, 49152\n"
  "s_nop 0\n"
  "buffer_load_dwordx4 v34, %[srdw], s42 offen offset:0 lds\n"
  "v_mfma_f32_16x16x32_bf16 v[108:111], v[172:175], v[168:171], v[108:111]\n"
  "ds_read_b128 v[124:127], v21 offset:36864\n"
  "v_mfma_f32_16x16x32_bf16 v[48:51], v[176:179], v[160:163], v[48:51]\n"
  "ds_read_b64_tr_b16 v[132:133], v23 offset:0\n"
  "ds_read_b64_tr_b16 v[134:135], v23 offset:1024\n"
  "v_mfma_f32_16x16x32_bf16 v[80:83], v[176:179], v[164:167], v[80:83]\n"
  "s_setprio 2\n"
  "ds_read_b64_tr_b16 v[136:137], v24 offset:0\n"
  "buffer_load_dwordx4 v35, %[srdw], s42 offen offset:1024 lds\n"
  "s_add_u32 s42, s42, 262144\n"
  "v_mfma_f32_16x16x32_bf16 v[112:115], v[176:179], v[168:171], v[112:115]\n"
  "ds_read_b64_tr_b16 v[138:139], v24 offset:1024\n"
  "ds_read_b64_tr_b16 v[140:141], v25 offset:0\n"
  "v_mfma_f32_16x16x32_bf16 v[52:55], v[180:183], v[160:163], v[52:55]\n"
  "ds_read_b64_tr_b16 v[142:143], v25 offset:1024\n"
  "v_mfma_f32_16x16x32_bf16 v[84:87], v[180:183], v[164:167], v[84:87]\n"
  "ds_read_b64_tr_b16 v[144:145], v26 offset:0\n"
  "ds_read_b64_tr_b16 v[146:147], v26 offset:1024\n"
  "s_add_u32 m0, s45, 18432\n"
  "s_nop 0\n"
  "buffer_load_dwordx4 v30, %[srdx], s44 offen offset:0 lds\n"
  "v_mfma_f32_16x16x32_bf16 v[56:59], v[184:187], v[160:163], v[56:59]\n"
  "s_setprio 1\n"
  "ds_read_b64_tr_b16 v[148:149], v27 offset:0\n"
  "v_mfma_f32_16x16x32_bf16 v[88:91], v[184:187], v[164:167], v[88:91]\n"
  "ds_read_b64_tr_b16 v[150:151], v27 offset:1024\n"
  "v_mfma_f32_16x16x32_bf16 v[60:63], v[188:191], v[160:163], v[60:63]\n"
  "ds_read_b64_tr_b16 v[152:153], v28 offset:0\n"
  "buffer_load_dwordx4 v31, %[srdx], s44 offen offset:1024 lds\n"
  "v_mfma_f32_16x16x32_bf16 v[92:95], v[188:191], v[164:167], v[92:95]\n"
  "ds_read_b64_tr_b16 v[154:155], v28 offset:1024\n"
  "v_mfma_f32_16x16x32_bf16 v[64:67], v[192:195], v[160:163], v[64:67]\n"
  "s_setprio 0\n"
  "ds_read_b64_tr_b16 v[156:157], v29 offset:0\n"
  "v_mfma_f32_16x16x32_bf16 v[96:99], v[192:195], v[164:167], v[96:99]\n"
  "ds_read_b64_tr_b16 v[158:159], v29 offset:1024\n"
  "s_add_u32 m0, s47, 18432\n"
  "s_mov_b64 exec, s[48:49]\n"
  "buffer_load_dwordx4 v32, %[srdx], s44 offen offset:0 lds\n"
  "s_mov_b64 exec, -1\n"
  "s_add_u32 s44, s44, 128\n"
  "v_mfma_f32_16x16x32_bf16 v[68:71], v[196:199], v[160:163], v[68:71]\n"
  "v_mfma_f32_16x16x32_bf16 v[100:103], v[196:199], v[164:167], v[100:103]\n"
  "v_mfma_f32_16x16x32_bf16 v[72:75], v[200:203], v[160:163], v[72:75]\n"
  "v_mfma_f32_16x16x32_bf16 v[104:107], v[200:203], v[164:167], v[104:107]\n"
  "s_waitcnt vmcnt(10) lgkmcnt(0)\n"
  "s_barrier\n"
  "v_mfma_f32_16x16x32_bf16 v[44:47], v[128:131], v[116:119], v[44:47]\n"
  "s_setprio 3\n"
  "ds_read_b64_tr_b16 v[172:173], v22 offset:16384\n"
  "ds_read_b64_tr_b16 v[174:175], v22 offset:17408\n"
  "v_mfma_f32_16x16x32_bf16 v[76:79], v[128:131], v[120:123], v[76:79]\n"
  "ds_read_b128 v[160:163], v20 offset:37376\n"
  "ds_read_b128 v[164:167], v20 offset:39424\n"
  "s_add_u32 m0, s43, 0\n"
  "s_nop 0\n"
  "buffer_load_dwordx4 v34, %[srdw], s42 offen offset:0 lds\n"
  "v_mfma_f32_16x16x32_bf16 v[108:111], v[128:131], v[124:127], v[108:111]\n"
  "ds_read_b128 v[168:171], v21 offset:37376\n"
  "v_mfma_f32_16x16x32_bf16 v[48:51], v[132:135], v[116:119], v[48:51]\n"
  "ds_read_b64_tr_b16 v[176:177], v23 offset:16384\n"
  "ds_read_b64_tr_b16 v[178:179], v23 offset:17408\n"
  "v_mfma_f32_16x16x32_bf16 v[80:83], v[132:135], v[120:123], v[80:83]\n"
  "s_setprio 2\n"
  "ds_read_b64_tr_b16 v[180:181], v24 offset:16384\n"
  "buffer_load_dwordx4 v35, %[srdw], s42 offen offset:1024 lds\n"
  "s_add_u32 s42, s42, 262144\n"
  "v_mfma_f32_16x16x32_bf16 v[112:115], v[132:135], v[124:127], v[112:115]\n"
  "ds_read_b64_tr_b16 v[182:183], v24 offset:17408\n"
  "ds_read_b64_tr_b16 v[184:185], v25 offset:16384\n"
  "v_mfma_f32_16x16x32_bf16 v[52:55], v[136:139], v[116:119], v[52:55]\n"
  "ds_read_b64_tr_b16 v[186:187], v25 offset:17408\n"
  "v_mfma_f32_16x16x32_bf16 v[84:87], v[136:139], v[120:123], v[84:87]\n"
  "ds_read_b64_tr_b16 v[188:189], v26 offset:16384\n"
  "ds_read_b64_tr_b16 v[190:191], v26 offset:17408\n"
  "v_mfma_f32_16x16x32_bf16 v[56:59], v[140:143], v[116:119], v[56:59]\n"
  "s_setprio 1\n"
  "ds_read_b64_tr_b16 v[192:193], v27 offset:16384\n"
  "v_mfma_f32_16x16x32_bf16 v[88:91], v[140:143], v[120:123], v[88:91]\n"
  "ds_read_b64_tr_b16 v[194:195], v27 offset:17408\n"
  "v_mfma_f32_16x16x32_bf16 v[60:63], v[144:147], v[116:119], v[60:63]\n"
  "ds_read_b64_tr_b16 v[196:197], v28 offset:16384\n"
  "v_mfma_f32_16x16x32_bf16 v[92:95], v[144:147], v[120:123], v[92:95]\n"
  "ds_read_b64_tr_b16 v[198:199], v28 offset:17408\n"
  "v_mfma_f32_16x16x32_bf16 v[64:67], v[148:151], v[116:119], v[64:67]\n"
  "s_setprio 0\n"
  "ds_read_b64_tr_b16 v[200:201], v29 offset:16384\n"
  "v_mfma_f32_16x16x32_bf16 v[96:99], v[148:151], v[120:123], v[96:99]\n"
  "ds_read_b64_tr_b16 v[202:203], v29 offset:17408\n"
  "v_mfma_f32_16x16x32_bf16 v[68:71], v[152:155], v[116:119], v[68:71]\n"
  "v_mfma_f32_16x16x32_bf16 v[100:103], v[152:155], v[120:123], v[100:103]\n"
  "v_mfma_f32_16x16x32_bf16 v[72:75], v[156:159], v[116:119], v[72:75]\n"
  "v_mfma_f32_16x16x32_bf16 v[104:107], v[156:159], v[120:123], v[104:107]\n"
  "s_waitcnt vmcnt(7) lgkmcnt(0)\n"
  "s_barrier\n"
  "v_mfma_f32_16x16x32_bf16 v[44:47], v[172:175], v[160:163], v[44:47]\n"
  "s_setprio 3\n"
  "ds_read_b64_tr_b16 v[128:129], v22 offset:32768\n"
  "ds_read_b64_tr_b16 v[130:131], v22 offset:33792\n"
  "s_add_u32 m0, s43, 16384\n"
  "s_nop 0\n"
  "buffer_load_dwordx4 v34, %[srdw], s42 offen offset:0 lds\n"
  "v_mfma_f32_16x16x32_bf16 v[76:79], v[172:175], v[164:167], v[76:79]\n"
  "ds_read_b128 v[116:119], v20 offset:0\n"
  "ds_read_b128 v[120:123], v20 offset:2048\n"
  "v_mfma_f32_16x16x32_bf16 v[108:111], v[172:175], v[168:171], v[108:111]\n"
  "ds_read_b128 v[124:127], v21 offset:0\n"
  "buffer_load_dwordx4 v35, %[srdw], s42 offen offset:1024 lds\n"
  "s_add_u32 s42, s42, 262144\n"
  "v_mfma_f32_16x16x32_bf16 v[48:51], v[176:179], v[160:163], v[48:51]\n"
  "ds_read_b64_tr_b16 v[132:133], v23 offset:32768\n"
  "ds_read_b64_tr_b16 v[134:135], v23 offset:33792\n"
  "v_mfma_f32_16x16x32_bf16 v[80:83], v[176:179], v[164:167], v[80:83]\n"
  "s_setprio 2\n"
  "ds_read_b64_tr_b16 v[136:137], v24 offset:32768\n"
  "s_add_u32 m0, s45, 36864\n"
  "s_nop 0\n"
  "buffer_load_dwordx4 v30, %[srdx], s44 offen offset:0 lds\n"
  "v_mfma_f32_16x16x32_bf16 v[112:115], v[176:179], v[168:171], v[112:115]\n"
  "ds_read_b64_tr_b16 v[138:139], v24 offset:33792\n"
  "ds_read_b64_tr_b16 v[140:141], v25 offset:32768\n"
  "v_mfma_f32_16x16x32_bf16 v[52:55], v[180:183], v[160:163], v[52:55]\n"
  "ds_read_b64_tr_b16 v[142:143], v25 offset:33792\n"
  "buffer_load_dwordx4 v31, %[srdx], s44 offen offset:1024 lds\n"
  "v_mfma_f32_16x16x32_bf16 v[84:87], v[180:183], v[164:167], v[84:87]\n"
  "ds_read_b64_tr_b16 v[144:145], v26 offset:32768\n"
  "ds_read_b64_tr_b16 v[146:147], v26 offset:33792\n"
  "v_mfma_f32_16x16x32_bf16 v[56:59], v[184:187], v[160:163], v[56:59]\n"
  "s_setprio 1\n"
  "ds_read_b64_tr_b16 v[148:149], v27 offset:32768\n"
  "s_add_u32 m0, s47, 36864\n"
  "s_mov_b64 exec, s[48:49]\n"
  "buffer_load_dwordx4 v32, %[srdx], s44 offen offset:0 lds\n"
  "s_mov_b64 exec, -1\n"
  "s_add_u32 s44, s44, 128\n"
  "v_mfma_f32_16x16x32_bf16 v[88:91], v[184:187], v[164:167], v[88:91]\n"
  "ds_read_b64_tr_b16 v[150:151], v27 offset:33792\n"
  "v_mfma_f32_16x16x32_bf16 v[60:63], v[188:191], v[160:163], v[60:63]\n"
  "ds_read_b64_tr_b16 v[152:153], v28 offset:32768\n"
  "s_lshl_b32 s40, %[wave], 11\n"
  "s_add_u32 s40, s40, 0\n"
  "buffer_load_dwordx4 v[248:251], v10, %[srdk], s40 offen offset:0 nt\n"
  "v_mfma_f32_16x16x32_bf16 v[92:95], v[188:191], v[164:167], v[92:95]\n"
  "ds_read_b64_tr_b16 v[154:155], v28 offset:33792\n"
  "v_mfma_f32_16x16x32_bf16 v[64:67], v[192:195], v[160:163], v[64:67]\n"
  "s_setprio 0\n"
  "ds_read_b64_tr_b16 v[156:157], v29 offset:32768\n"
  "v_mfma_f32_16x16x32_bf16 v[96:99], v[192:195], v[164:167], v[96:99]\n"
  "ds_read_b64_tr_b16 v[158:159], v29 offset:33792\n"
  "v_mfma_f32_16x16x32_bf16 v[68:71], v[196:199], v[160:163], v[68:71]\n"
  "v_mfma_f32_16x16x32_bf16 v[100:103], v[196:199], v[164:167], v[100:103]\n"
  "v_mfma_f32_16x16x32_bf16 v[72:75], v[200:203], v[160:163], v[72:75]\n"
  "v_mfma_f32_16x16x32_bf16 v[104:107], v[200:203], v[164:167], v[104:107]\n"
  "s_waitcnt vmcnt(11) lgkmcnt(0)\n"
  "s_barrier\n"
  "v_mfma_f32_16x16x32_bf16 v[44:47], v[128:131], v[116:119], v[44:47]\n"
  "s_setprio 3\n"
  "ds_read_b64_tr_b16 v[172:173], v22 offset:49152\n"
  "ds_read_b64_tr_b16 v[174:175], v22 offset:50176\n"
  "v_mfma_f32_16x16x32_bf16 v[76:79], v[128:131], v[120:123], v[76:79]\n"
  "ds_read_b128 v[160:163], v20 offset:512\n"
  "ds_read_b128 v[164:167], v20 offset:2560\n"
  "s_add_u32 m0, s43, 32768\n"
  "s_nop 0\n"
  "buffer_load_dwordx4 v34, %[srdw], s42 offen offset:0 lds\n"
  "v_mfma_f32_16x16x32_bf16 v[108:111], v[128:131], v[124:127], v[108:111]\n"
  "ds_read_b128 v[168:171], v21 offset:512\n"
  "v_mfma_f32_16x16x32_bf16 v[48:51], v[132:135], v[116:119], v[48:51]\n"
  "ds_read_b64_tr_b16 v[176:177], v23 offset:49152\n"
  "ds_read_b64_tr_b16 v[178:179], v23 offset:50176\n"
  "v_mfma_f32_16x16x32_bf16 v[80:83], v[132:135], v[120:123], v[80:83]\n"
  "s_setprio 2\n"
  "ds_read_b64_tr_b16 v[180:181], v24 offset:49152\n"
  "buffer_load_dwordx4 v35, %[srdw], s42 offen offset:1024 lds\n"
  "s_add_u32 s42, s42, 262144\n"
  "v_mfma_f32_16x16x32_bf16 v[112:115], v[132:135], v[124:127], v[112:115]\n"
  "ds_read_b64_tr_b16 v[182:183], v24 offset:50176\n"
  "ds_read_b64_tr_b16 v[184:185], v25 offset:49152\n"
  "v_mfma_f32_16x16x32_bf16 v[52:55], v[136:139], v[116:119], v[52:55]\n"
  "ds_read_b64_tr_b16 v[186:187], v25 offset:50176\n"
  "v_mfma_f32_16x16x32_bf16 v[84:87], v[136:139], v[120:123], v[84:87]\n"
  "ds_read_b64_tr_b16 v[188:189], v26 offset:49152\n"
  "ds_read_b64_tr_b16 v[190:191], v26 offset:50176\n"
  "s_lshl_b32 s40, %[wave], 11\n"
  "s_add_u32 s40, s40, 0\n"
  "buffer_load_dwordx4 v[204:207], v11, %[srdk], s40 offen offset:1024 nt\n"
  "v_mfma_f32_16x16x32_bf16 v[56:59], v[140:143], v[116:119], v[56:59]\n"
  "s_setprio 1\n"
  "ds_read_b64_tr_b16 v[192:193], v27 offset:49152\n"
  "v_mfma_f32_16x16x32_bf16 v[88:91], v[140:143], v[120:123], v[88:91]\n"
  "ds_read_b64_tr_b16 v[194:195], v27 offset:50176\n"
  "v_mfma_f32_16x16x32_bf16 v[60:63], v[144:147], v[116:119], v[60:63]\n"
  "ds_read_b64_tr_b16 v[196:197], v28 offset:49152\n"
  "v_mfma_f32_16x16x32_bf16 v[92:95], v[144:147], v[120:123], v[92:95]\n"
  "ds_read_b64_tr_b16 v[198:199], v28 offset:50176\n"
  "v_mfma_f32_16x16x32_bf16 v[64:67], v[148:151], v[116:119], v[64:67]\n"
  "s_setprio 0\n"
  "ds_read_b64_tr_b16 v[200:201], v29 offset:49152\n"
  "v_mfma_f32_16x16x32_bf16 v[96:99], v[148:151], v[120:123], v[96:99]\n"
  "ds_read_b64_tr_b16 v[202:203], v29 offset:50176\n"
  "v_mfma_f32_16x16x32_bf16 v[68:71], v[152:155], v[116:119], v[68:71]\n"
  "v_mfma_f32_16x16x32_bf16 v[100:103], v[152:155], v[120:123], v[100:103]\n"
  "v_mfma_f32_16x16x32_bf16 v[72:75], v[156:159], v[116:119], v[72:75]\n"
  "v_mfma_f32_16x16x32_bf16 v[104:107], v[156:159], v[120:123], v[104:107]\n"
  "s_waitcnt vmcnt(9) lgkmcnt(0)\n"
  "s_barrier\n"
  "v_mfma_f32_16x16x32_bf16 v[44:47], v[172:175], v[160:163], v[44:47]\n"
  "s_setprio 3\n"
  "ds_read_b64_tr_b16 v[128:129], v22 offset:0\n"
  "ds_read_b64_tr_b16 v[130:131], v22 offset:1024\n"
  "s_add_u32 m0, s43, 49152\n"
  "s_nop 0\n"
  "buffer_load_dwordx4 v34, %[srdw], s42 offen offset:0 lds\n"
  "v_mfma_f32_16x16x32_bf16 v[76:79], v[172:175], v[164:167], v[76:79]\n"
  "ds_read_b128 v[116:119], v20 offset:18432\n"
  "ds_read_b128 v[120:123], v20 offset:20480\n"
  "v_mfma_f32_16x16x32_bf16 v[108:111], v[172:175], v[168:171], v[108:111]\n"
  "ds_read_b128 v[124:127], v21 offset:18432\n"
  "buffer_load_dwordx4 v35, %[srdw], s42 offen offset:1024 lds\n"
  "s_add_u32 s42, s42, 262144\n"
  "v_mfma_f32_16x16x32_bf16 v[48:51], v[176:179], v[160:163], v[48:51]\n"
  "ds_read_b64_tr_b16 v[132:133], v23 offset:0\n"
  "ds_read_b64_tr_b16 v[134:135], v23 offset:1024\n"
  "v_mfma_f32_16x16x32_bf16 v[80:83], v[176:179], v[164:167], v[80:83]\n"
  "s_setprio 2\n"
  "ds_read_b64_tr_b16 v[136:137], v24 offset:0\n"
  "s_add_u32 m0, s45, 0\n"
  "s_nop 0\n"
  "buffer_load_dwordx4 v30, %[srdx], s44 offen offset:0 lds\n"
  "v_mfma_f32_16x16x32_bf16 v[112:115], v[176:179], v[168:171], v[112:115]\n"
  "ds_read_b64_tr_b16 v[138:139], v24 offset:1024\n"
  "ds_read_b64_tr_b16 v[140:141], v25 offset:0\n"
  "v_mfma_f32_16x16x32_bf16 v[52:55], v[180:183], v[160:163], v[52:55]\n"
  "ds_read_b64_tr_b16 v[142:143], v25 offset:1024\n"
  "buffer_load_dwordx4 v31, %[srdx], s44 offen offset:1024 lds\n"
  "v_mfma_f32_16x16x32_bf16 v[84:87], v[180:183], v[164:167], v[84:87]\n"
  "ds_read_b64_tr_b16 v[144:145], v26 offset:0\n"
  "ds_read_b64_tr_b16 v[146:147], v26 offset:1024\n"
  "v_mfma_f32_16x16x32_bf16 v[56:59], v[184:187], v[160:163], v[56:59]\n"
  "s_setprio 1\n"
  "ds_read_b64_tr_b16 v[148:149], v27 offset:0\n"
  "s_add_u32 m0, s47, 0\n"
  "s_mov_b64 exec, s[48:49]\n"
  "buffer_load_dwordx4 v32, %[srdx], s44 offen offset:0 lds\n"
  "s_mov_b64 exec, -1\n"
  "s_add_u32 s44, s44, 128\n"
  "v_mfma_f32_16x16x32_bf16 v[88:91], v[184:187], v[164:167], v[88:91]\n"
  "ds_read_b64_tr_b16 v[150:151], v27 offset:1024\n"
  "v_mfma_f32_16x16x32_bf16 v[60:63], v[188:191], v[160:163], v[60:63]\n"
  "ds_read_b64_tr_b16 v[152:153], v28 offset:0\n"
  "s_lshl_b32 s40, %[wave], 11\n"
  "s_add_u32 s40, s40, 131072\n"
  "buffer_load_dwordx4 v[208:211], v10, %[srdk], s40 offen offset:0 nt\n"
  "v_mfma_f32_16x16x32_bf16 v[92:95], v[188:191], v[164:167], v[92:95]\n"
  "ds_read_b64_tr_b16 v[154:155], v28 offset:1024\n"
  "v_mfma_f32_16x16x32_bf16 v[64:67], v[192:195], v[160:163], v[64:67]\n"
  "s_setprio 0\n"
  "ds_read_b64_tr_b16 v[156:157], v29 offset:0\n"
  "v_mfma_f32_16x16x32_bf16 v[96:99], v[192:195], v[164:167], v[96:99]\n"
  "ds_read_b64_tr_b16 v[158:159], v29 offset:1024\n"
  "v_mfma_f32_16x16x32_bf16 v[68:71], v[196:199], v[160:163], v[68:71]\n"
  "v_mfma_f32_16x16x32_bf16 v[100:103], v[196:199], v[164:167], v[100:103]\n"
  "v_mfma_f32_16x16x32_bf16 v[72:75], v[200:203], v[160:163], v[72:75]\n"
  "v_mfma_f32_16x16x32_bf16 v[104:107], v[200:203], v[164:167], v[104:107]\n"
  "s_waitcnt vmcnt(13) lgkmcnt(0)\n"
  "s_barrier\n"
  "v_mfma_f32_16x16x32_bf16 v[44:47], v[128:131], v[116:119], v[44:47]\n"
  "s_setprio 3\n"
  "ds_read_b64_tr_b16 v[172:173], v22 offset:16384\n"
  "ds_read_b64_tr_b16 v[174:175], v22 offset:17408\n"
  "v_mfma_f32_16x16x32_bf16 v[76:79], v[128:131], v[120:123], v[76:79]\n"
  "ds_read_b128 v[160:163], v20 offset:18944\n"
  "ds_read_b128 v[164:167], v20 offset:20992\n"
  "s_add_u32 m0, s43, 0\n"
  "s_nop 0\n"
  "buffer_load_dwordx4 v34, %[srdw], s42 offen offset:0 lds\n"
  "v_mfma_f32_16x16x32_bf16 v[108:111], v[128:131], v[124:127], v[108:111]\n"
  "ds_read_b128 v[168:171], v21 offset:18944\n"
  "v_mfma_f32_16x16x32_bf16 v[48:51], v[132:135], v[116:119], v[48:51]\n"
  "ds_read_b64_tr_b16 v[176:177], v23 offset:16384\n"
  "ds_read_b64_tr_b16 v[178:179], v23 offset:17408\n"
  "v_mfma_f32_16x16x32_bf16 v[80:83], v[132:135], v[120:123], v[80:83]\n"
  "s_setprio 2\n"
  "ds_read_b64_tr_b16 v[180:181], v24 offset:16384\n"
  "buffer_load_dwordx4 v35, %[srdw], s42 offen offset:1024 lds\n"
  "s_add_u32 s42, s42, 262144\n"
  "v_mfma_f32_16x16x32_bf16 v[112:115], v[132:135], v[124:127], v[112:115]\n"
  "ds_read_b64_tr_b16 v[182:183], v24 offset:17408\n"
  "ds_read_b64_tr_b16 v[184:185], v25 offset:16384\n"
  "v_mfma_f32_16x16x32_bf16 v[52:55], v[136:139], v[116:119], v[52:55]\n"
  "ds_read_b64_tr_b16 v[186:187], v25 offset:17408\n"
  "v_mfma_f32_16x16x32_bf16 v[84:87], v[136:139], v[120:123], v[84:87]\n"
  "ds_read_b64_tr_b16 v[188:189], v26 offset:16384\n"
  "ds_read_b64_tr_b16 v[190:191], v26 offset:17408\n"
  "s_lshl_b32 s40, %[wave], 11\n"
  "s_add_u32 s40, s40, 131072\n"
  "buffer_load_dwordx4 v[212:215], v11, %[srdk], s40 offen offset:1024 nt\n"
  "v_mfma_f32_16x16x32_bf16 v[56:59], v[140:143], v[116:119], v[56:59]\n"
  "s_setprio 1\n"
  "ds_read_b64_tr_b16 v[192:193], v27 offset:16384\n"
  "v_mfma_f32_16x16x32_bf16 v[88:91], v[140:143], v[120:123], v[88:91]\n"
  "ds_read_b64_tr_b16 v[194:195], v27 offset:17408\n"
  "v_mfma_f32_16x16x32_bf16 v[60:63], v[144:147], v[116:119], v[60:63]\n"
  "ds_read_b64_tr_b16 v[196:197], v28 offset:16384\n"
  "v_mfma_f32_16x16x32_bf16 v[92:95], v[144:147], v[120:123], v[92:95]\n"
  "ds_read_b64_tr_b16 v[198:199], v28 offset:17408\n"
  "v_mfma_f32_16x16x32_bf16 v[64:67], v[148:151], v[116:119], v[64:67]\n"
  "s_setprio 0\n"
  "ds_read_b64_tr_b16 v[200:201], v29 offset:16384\n"
  "v_mfma_f32_16x16x32_bf16 v[96:99], v[148:151], v[120:123], v[96:99]\n"
  "ds_read_b64_tr_b16 v[202:203], v29 offset:17408\n"
  "v_mfma_f32_16x16x32_bf16 v[68:71], v[152:155], v[116:119], v[68:71]\n"
  "v_mfma_f32_16x16x32_bf16 v[100:103], v[152:155], v[120:123], v[100:103]\n"
  "v_mfma_f32_16x16x32_bf16 v[72:75], v[156:159], v[116:119], v[72:75]\n"
  "v_mfma_f32_16x16x32_bf16 v[104:107], v[156:159], v[120:123], v[104:107]\n"
  "s_waitcnt vmcnt(10) lgkmcnt(0)\n"
  "s_barrier\n"
  "v_mfma_f32_16x16x32_bf16 v[44:47], v[172:175], v[160:163], v[44:47]\n"
  "s_setprio 3\n"
  "ds_read_b64_tr_b16 v[128:129], v22 offset:32768\n"
  "ds_read_b64_tr_b16 v[130:131], v22 offset:33792\n"
  "s_add_u32 m0, s43, 16384\n"
  "s_nop 0\n"
  "buffer_load_dwordx4 v34, %[srdw], s42 offen offset:0 lds\n"
  "v_mfma_f32_16x16x32_bf16 v[76:79], v[172:175], v[164:167], v[76:79]\n"
  "ds_read_b128 v[116:119], v20 offset:36864\n"
  "ds_read_b128 v[120:123], v20 offset:38912\n"
  "v_mfma_f32_16x16x32_bf16 v[108:111], v[172:175], v[168:171], v[108:111]\n"
  "ds_read_b128 v[124:127], v21 offset:36864\n"
  "buffer_load_dwordx4 v35, %[srdw], s42 offen offset:1024 lds\n"
  "s_add_u32 s42, s42, 262144\n"
  "v_mfma_f32_16x16x32_bf16 v[48:51], v[176:179], v[160:163], v[48:51]\n"
  "ds_read_b64_tr_b16 v[132:133], v23 offset:32768\n"
  "ds_read_b64_tr_b16 v[134:135], v23 offset:33792\n"
  "v_mfma_f32_16x16x32_bf16 v[80:83], v[176:179], v[164:167], v[80:83]\n"
  "s_setprio 2\n"
  "ds_read_b64_tr_b16 v[136:137], v24 offset:32768\n"
  "s_add_u32 m0, s45, 18432\n"
  "s_nop 0\n"
  "buffer_load_dwordx4 v30, %[srdx], s44 offen offset:0 lds\n"
  "v_mfma_f32_16x16x32_bf16 v[112:115], v[176:179], v[168:171], v[112:115]\n"
  "ds_read_b64_tr_b16 v[138:139], v24 offset:33792\n"
  "ds_read_b64_tr_b16 v[140:141], v25 offset:32768\n"
  "v_mfma_f32_16x16x32_bf16 v[52:55], v[180:183], v[160:163], v[52:55]\n"
  "ds_read_b64_tr_b16 v[142:143], v25 offset:33792\n"
  "buffer_load_dwordx4 v31, %[srdx], s44 offen offset:1024 lds\n"
  "v_mfma_f32_16x16x32_bf16 v[84:87], v[180:183], v[164:167], v[84:87]\n"
  "ds_read_b64_tr_b16 v[144:145], v26 offset:32768\n"
  "ds_read_b64_tr_b16 v[146:147], v26 offset:33792\n"
  "v_mfma_f32_16x16x32_bf16 v[56:59], v[184:187], v[160:163], v[56:59]\n"
  "s_setprio 1\n"
  "ds_read_b64_tr_b16 v[148:149], v27 offset:32768\n"
  "s_add_u32 m0, s47, 18432\n"
  "s_mov_b64 exec, s[48:49]\n"
  "buffer_load_dwordx4 v32, %[srdx], s44 offen offset:0 lds\n"
  "s_mov_b64 exec, -1\n"
  "s_add_u32 s44, s44, 128\n"
  "v_mfma_f32_16x16x32_bf16 v[88:91], v[184:187], v[164:167], v[88:91]\n"
  "ds_read_b64_tr_b16 v[150:151], v27 offset:33792\n"
  "v_mfma_f32_16x16x32_bf16 v[60:63], v[188:191], v[160:163], v[60:63]\n"
  "ds_read_b64_tr_b16 v[152:153], v28 offset:32768\n"
  "s_lshl_b32 s40, %[wave], 11\n"
  "s_add_u32 s40, s40, 0\n"
  "buffer_load_dwordx4 v[216:219], v10, %[srdv], s40 offen offset:0 nt\n"
  "v_mfma_f32_16x16x32_bf16 v[92:95], v[188:191], v[164:167], v[92:95]\n"
  "ds_read_b64_tr_b16 v[154:155], v28 offset:33792\n"
  "v_mfma_f32_16x16x32_bf16 v[64:67], v[192:195], v[160:163], v[64:67]\n"
  "s_setprio 0\n"
  "ds_read_b64_tr_b16 v[156:157], v29 offset:32768\n"
  "v_mfma_f32_16x16x32_bf16 v[96:99], v[192:195], v[164:167], v[96:99]\n"
  "ds_read_b64_tr_b16 v[158:159], v29 offset:33792\n"
  "v_mfma_f32_16x16x32_bf16 v[68:71], v[196:199], v[160:163], v[68:71]\n"
  "v_mfma_f32_16x16x32_bf16 v[100:103], v[196:199], v[164:167], v[100:103]\n"
  "v_mfma_f32_16x16x32_bf16 v[72:75], v[200:203], v[160:163], v[72:75]\n"
  "v_mfma_f32_16x16x32_bf16 v[104:107], v[200:203], v[164:167], v[104:107]\n"
  "s_waitcnt vmcnt(13) lgkmcnt(0)\n"
  "s_barrier\n"
  "v_mfma_f32_16x16x32_bf16 v[44:47], v[128:131], v[116:119], v[44:47]\n"
  "s_setprio 3\n"
  "ds_read_b64_tr_b16 v[172:173], v22 offset:49152\n"
  "ds_read_b64_tr_b16 v[174:175], v22 offset:50176\n"
  "v_mfma_f32_16x16x32_bf16 v[76:79], v[128:131], v[120:123], v[76:79]\n"
  "ds_read_b128 v[160:163], v20 offset:37376\n"
  "ds_read_b128 v[164:167], v20 offset:39424\n"
  "s_add_u32 m0, s43, 32768\n"
  "s_nop 0\n"
  "buffer_load_dwordx4 v34, %[srdw], s42 offen offset:0 lds\n"
  "v_mfma_f32_16x16x32_bf16 v[108:111], v[128:131], v[124:127], v[108:111]\n"
  "ds_read_b128 v[168:171], v21 offset:37376\n"
  "v_mfma_f32_16x16x32_bf16 v[48:51], v[132:135], v[116:119], v[48:51]\n"
  "ds_read_b64_tr_b16 v[176:177], v23 offset:49152\n"
  "ds_read_b64_tr_b16 v[178:179], v23 offset:50176\n"
  "v_mfma_f32_16x16x32_bf16 v[80:83], v[132:135], v[120:123], v[80:83]\n"
  "s_setprio 2\n"
  "ds_read_b64_tr_b16 v[180:181], v24 offset:49152\n"
  "buffer_load_dwordx4 v35, %[srdw], s42 offen offset:1024 lds\n"
  "s_add_u32 s42, s42, 262144\n"
  "v_mfma_f32_16x16x32_bf16 v[112:115], v[132:135], v[124:127], v[112:115]\n"
  "ds_read_b64_tr_b16 v[182:183], v24 offset:50176\n"
  "ds_read_b64_tr_b16 v[184:185], v25 offset:49152\n"
  "v_mfma_f32_16x16x32_bf16 v[52:55], v[136:139], v[116:119], v[52:55]\n"
  "ds_read_b64_tr_b16 v[186:187], v25 offset:50176\n"
  "v_mfma_f32_16x16x32_bf16 v[84:87], v[136:139], v[120:123], v[84:87]\n"
  "ds_read_b64_tr_b16 v[188:189], v26 offset:49152\n"
  "ds_read_b64_tr_b16 v[190:191], v26 offset:50176\n"
  "s_lshl_b32 s40, %[wave], 11\n"
  "s_add_u32 s40, s40, 0\n"
  "buffer_load_dwordx4 v[220:223], v11, %[srdv], s40 offen offset:1024 nt\n"
  "v_mfma_f32_16x16x32_bf16 v[56:59], v[140:143], v[116:119], v[56:59]\n"
  "s_setprio 1\n"
  "ds_read_b64_tr_b16 v[192:193], v27 offset:49152\n"
  "v_mfma_f32_16x16x32_bf16 v[88:91], v[140:143], v[120:123], v[88:91]\n"
  "ds_read_b64_tr_b16 v[194:195], v27 offset:50176\n"
  "v_mfma_f32_16x16x32_bf16 v[60:63], v[144:147], v[116:119], v[60:63]\n"
  "ds_read_b64_tr_b16 v[196:197], v28 offset:49152\n"
  "v_mfma_f32_16x16x32_bf16 v[92:95], v[144:147], v[120:123], v[92:95]\n"
  "ds_read_b64_tr_b16 v[198:199], v28 offset:50176\n"
  "v_mfma_f32_16x16x32_bf16 v[64:67], v[148:151], v[116:119], v[64:67]\n"
  "s_setprio 0\n"
  "ds_read_b64_tr_b16 v[200:201], v29 offset:49152\n"
  "v_mfma_f32_16x16x32_bf16 v[96:99], v[148:151], v[120:123], v[96:99]\n"
  "ds_read_b64_tr_b16 v[202:203], v29 offset:50176\n"
  "v_mfma_f32_16x16x32_bf16 v[68:71], v[152:155], v[116:119], v[68:71]\n"
  "v_mfma_f32_16x16x32_bf16 v[100:103], v[152:155], v[120:123], v[100:103]\n"
  "v_mfma_f32_16x16x32_bf16 v[72:75], v[156:159], v[116:119], v[72:75]\n"
  "v_mfma_f32_16x16x32_bf16 v[104:107], v[156:159], v[120:123], v[104:107]\n"
  "s_waitcnt vmcnt(10) lgkmcnt(0)\n"
  "s_barrier\n"
  "v_mfma_f32_16x16x32_bf16 v[44:47], v[172:175], v[160:163], v[44:47]\n"
  "s_setprio 3\n"
  "ds_read_b64_tr_b16 v[128:129], v22 offset:0\n"
  "ds_read_b64_tr_b16 v[130:131], v22 offset:1024\n"
  "s_add_u32 m0, s43, 49152\n"
  "s_nop 0\n"
  "buffer_load_dwordx4 v34, %[srdw], s42 offen offset:0 lds\n"
  "v_mfma_f32_16x16x32_bf16 v[76:79], v[172:175], v[164:167], v[76:79]\n"
  "ds_read_b128 v[116:119], v20 offset:0\n"
  "ds_read_b128 v[120:123], v20 offset:2048\n"
  "v_mfma_f32_16x16x32_bf16 v[108:111], v[172:175], v[168:171], v[108:111]\n"
  "ds_read_b128 v[124:127], v21 offset:0\n"
  "buffer_load_dwordx4 v35, %[srdw], s42 offen offset:1024 lds\n"
  "s_add_u32 s42, s42, 262144\n"
  "v_mfma_f32_16x16x32_bf16 v[48:51], v[176:179], v[160:163], v[48:51]\n"
  "ds_read_b64_tr_b16 v[132:133], v23 offset:0\n"
  "ds_read_b64_tr_b16 v[134:135], v23 offset:1024\n"
  "v_mfma_f32_16x16x32_bf16 v[80:83], v[176:179], v[164:167], v[80:83]\n"
  "s_setprio 2\n"
  "ds_read_b64_tr_b16 v[136:137], v24 offset:0\n"
  "s_add_u32 m0, s45, 36864\n"
  "s_nop 0\n"
  "buffer_load_dwordx4 v30, %[srdx], s44 offen offset:0 lds\n"
  "v_mfma_f32_16x16x32_bf16 v[112:115], v[176:179], v[168:171], v[112:115]\n"
  "ds_read_b64_tr_b16 v[138:139], v24 offset:1024\n"
  "ds_read_b64_tr_b16 v[140:141], v25 offset:0\n"
  "v_mfma_f32_16x16x32_bf16 v[52:55], v[180:183], v[160:163], v[52:55]\n"
  "ds_read_b64_tr_b16 v[142:143], v25 offset:1024\n"
  "buffer_load_dwordx4 v31, %[srdx], s44 offen offset:1024 lds\n"
  "v_mfma_f32_16x16x32_bf16 v[84:87], v[180:183], v[164:167], v[84:87]\n"
  "ds_read_b64_tr_b16 v[144:145], v26 offset:0\n"
  "ds_read_b64_tr_b16 v[146:147], v26 offset:1024\n"
  "v_mfma_f32_16x16x32_bf16 v[56:59], v[184:187], v[160:163], v[56:59]\n"
  "s_setprio 1\n"
  "ds_read_b64_tr_b16 v[148:149], v27 offset:0\n"
  "s_add_u32 m0, s47, 36864\n"
  "s_mov_b64 exec, s[48:49]\n"
  "buffer_load_dwordx4 v32, %[srdx], s44 offen offset:0 lds\n"
  "s_mov_b64 exec, -1\n"
  "s_add_u32 s44, s44, 128\n"
  "v_mfma_f32_16x16x32_bf16 v[88:91], v[184:187], v[164:167], v[88:91]\n"
  "ds_read_b64_tr_b16 v[150:151], v27 offset:1024\n"
  "v_mfma_f32_16x16x32_bf16 v[60:63], v[188:191], v[160:163], v[60:63]\n"
  "ds_read_b64_tr_b16 v[152:153], v28 offset:0\n"
  "s_lshl_b32 s40, %[wave], 11\n"
  "s_add_u32 s40, s40, 131072\n"
  "buffer_load_dwordx4 v[224:227], v10, %[srdv], s40 offen offset:0 nt\n"
  "v_mfma_f32_16x16x32_bf16 v[92:95], v[188:191], v[164:167], v[92:95]\n"
  "ds_read_b64_tr_b16 v[154:155], v28 offset:1024\n"
  "v_mfma_f32_16x16x32_bf16 v[64:67], v[192:195], v[160:163], v[64:67]\n"
  "s_setprio 0\n"
  "ds_read_b64_tr_b16 v[156:157], v29 offset:0\n"
  "v_mfma_f32_16x16x32_bf16 v[96:99], v[192:195], v[164:167], v[96:99]\n"
  "ds_read_b64_tr_b16 v[158:159], v29 offset:1024\n"
  "v_mfma_f32_16x16x32_bf16 v[68:71], v[196:199], v[160:163], v[68:71]\n"
  "v_mfma_f32_16x16x32_bf16 v[100:103], v[196:199], v[164:167], v[100:103]\n"
  "v_mfma_f32_16x16x32_bf16 v[72:75], v[200:203], v[160:163], v[72:75]\n"
  "v_mfma_f32_16x16x32_bf16 v[104:107], v[200:203], v[164:167], v[104:107]\n"
  "s_waitcnt vmcnt(13) lgkmcnt(0)\n"
  "s_barrier\n"
  "v_mfma_f32_16x16x32_bf16 v[44:47], v[128:131], v[116:119], v[44:47]\n"
  "s_setprio 3\n"
  "ds_read_b64_tr_b16 v[172:173], v22 offset:16384\n"
  "ds_read_b64_tr_b16 v[174:175], v22 offset:17408\n"
  "v_mfma_f32_16x16x32_bf16 v[76:79], v[128:131], v[120:123], v[76:79]\n"
  "ds_read_b128 v[160:163], v20 offset:512\n"
  "ds_read_b128 v[164:167], v20 offset:2560\n"
  "s_add_u32 m0, s43, 0\n"
  "s_nop 0\n"
  "buffer_load_dwordx4 v34, %[srdw], s42 offen offset:0 lds\n"
  "v_mfma_f32_16x16x32_bf16 v[108:111], v[128:131], v[124:127], v[108:111]\n"
  "ds_read_b128 v[168:171], v21 offset:512\n"
  "v_mfma_f32_16x16x32_bf16 v[48:51], v[132:135], v[116:119], v[48:51]\n"
  "ds_read_b64_tr_b16 v[176:177], v23 offset:16384\n"
  "ds_read_b64_tr_b16 v[178:179], v23 offset:17408\n"
  "v_mfma_f32_16x16x32_bf16 v[80:83], v[132:135], v[120:123], v[80:83]\n"
  "s_setprio 2\n"
  "ds_read_b64_tr_b16 v[180:181], v24 offset:16384\n"
  "buffer_load_dwordx4 v35, %[srdw], s42 offen offset:1024 lds\n"
  "s_add_u32 s42, s42, 262144\n"
  "v_mfma_f32_16x16x32_bf16 v[112:115], v[132:135], v[124:127], v[112:115]\n"
  "ds_read_b64_tr_b16 v[182:183], v24 offset:17408\n"
  "ds_read_b64_tr_b16 v[184:185], v25 offset:16384\n"
  "v_mfma_f32_16x16x32_bf16 v[52:55], v[136:139], v[116:119], v[52:55]\n"
  "ds_read_b64_tr_b16 v[186:187], v25 offset:17408\n"
  "v_mfma_f32_16x16x32_bf16 v[84:87], v[136:139], v[120:123], v[84:87]\n"
  "ds_read_b64_tr_b16 v[188:189], v26 offset:16384\n"
  "ds_read_b64_tr_b16 v[190:191], v26 offset:17408\n"
  "s_lshl_b32 s40, %[wave], 11\n"
  "s_add_u32 s40, s40, 131072\n"
  "buffer_load_dwordx4 v[228:231], v11, %[srdv], s40 offen offset:1024 nt\n"
  "v_mfma_f32_16x16x32_bf16 v[56:59], v[140:143], v[116:119], v[56:59]\n"
  "s_setprio 1\n"
  "ds_read_b64_tr_b16 v[192:193], v27 offset:16384\n"
  "v_mfma_f32_16x16x32_bf16 v[88:91], v[140:143], v[120:123], v[88:91]\n"
  "ds_read_b64_tr_b16 v[194:195], v27 offset:17408\n"
  "v_mfma_f32_16x16x32_bf16 v[60:63], v[144:147], v[116:119], v[60:63]\n"
  "ds_read_b64_tr_b16 v[196:197], v28 offset:16384\n"
  "v_mfma_f32_16x16x32_bf16 v[92:95], v[144:147], v[120:123], v[92:95]\n"
  "ds_read_b64_tr_b16 v[198:199], v28 offset:17408\n"
  "v_mfma_f32_16x16x32_bf16 v[64:67], v[148:151], v[116:119], v[64:67]\n"
  "s_setprio 0\n"
  "ds_read_b64_tr_b16 v[200:201], v29 offset:16384\n"
  "v_mfma_f32_16x16x32_bf16 v[96:99], v[148:151], v[120:123], v[96:99]\n"
  "ds_read_b64_tr_b16 v[202:203], v29 offset:17408\n"
  "v_mfma_f32_16x16x32_bf16 v[68:71], v[152:155], v[116:119], v[68:71]\n"
  "v_mfma_f32_16x16x32_bf16 v[100:103], v[152:155], v[120:123], v[100:103]\n"
  "v_mfma_f32_16x16x32_bf16 v[72:75], v[156:159], v[116:119], v[72:75]\n"
  "v_mfma_f32_16x16x32_bf16 v[104:107], v[156:159], v[120:123], v[104:107]\n"
  "s_waitcnt vmcnt(10) lgkmcnt(0)\n"
  "s_barrier\n"
  "v_mfma_f32_16x16x32_bf16 v[44:47], v[172:175], v[160:163], v[44:47]\n"
  "s_setprio 3\n"
  "ds_read_b64_tr_b16 v[128:129], v22 offset:32768\n"
  "ds_read_b64_tr_b16 v[130:131], v22 offset:33792\n"
  "s_add_u32 m0, s43, 16384\n"
  "s_nop 0\n"
  "buffer_load_dwordx4 v34, %[srdw], s42 offen offset:0 lds\n"
  "v_mfma_f32_16x16x32_bf16 v[76:79], v[172:175], v[164:167], v[76:79]\n"
  "ds_read_b128 v[116:119], v20 offset:18432\n"
  "ds_read_b128 v[120:123], v20 offset:20480\n"
  "v_mfma_f32_16x16x32_bf16 v[108:111], v[172:175], v[168:171], v[108:111]\n"
  "ds_read_b128 v[124:127], v21 offset:18432\n"
  "buffer_load_dwordx4 v35, %[srdw], s42 offen offset:1024 lds\n"
  "s_add_u32 s42, s42, 262144\n"
  "v_mfma_f32_16x16x32_bf16 v[48:51], v[176:179], v[160:163], v[48:51]\n"
  "ds_read_b64_tr_b16 v[132:133], v23 offset:32768\n"
  "ds_read_b64_tr_b16 v[134:135], v23 offset:33792\n"
  "v_mfma_f32_16x16x32_bf16 v[80:83], v[176:179], v[164:167], v[80:83]\n"
  "s_setprio 2\n"
  "ds_read_b64_tr_b16 v[136:137], v24 offset:32768\n"
  "s_add_u32 m0, s45, 0\n"
  "s_nop 0\n"
  "buffer_load_dwordx4 v30, %[srdx], s44 offen offset:0 lds\n"
  "v_mfma_f32_16x16x32_bf16 v[112:115], v[176:179], v[168:171], v[112:115]\n"
  "ds_read_b64_tr_b16 v[138:139], v24 offset:33792\n"
  "ds_read_b64_tr_b16 v[140:141], v25 offset:32768\n"
  "v_mfma_f32_16x16x32_bf16 v[52:55], v[180:183], v[160:163], v[52:55]\n"
  "ds_read_b64_tr_b16 v[142:143], v25 offset:33792\n"
  "buffer_load_dwordx4 v31, %[srdx], s44 offen offset:1024 lds\n"
  "v_mfma_f32_16x16x32_bf16 v[84:87], v[180:183], v[164:167], v[84:87]\n"
  "ds_read_b64_tr_b16 v[144:145], v26 offset:32768\n"
  "ds_read_b64_tr_b16 v[146:147], v26 offset:33792\n"
  "v_mfma_f32_16x16x32_bf16 v[56:59], v[184:187], v[160:163], v[56:59]\n"
  "s_setprio 1\n"
  "ds_read_b64_tr_b16 v[148:149], v27 offset:32768\n"
  "s_add_u32 m0, s47, 0\n"
  "s_mov_b64 exec, s[48:49]\n"
  "buffer_load_dwordx4 v32, %[srdx], s44 offen offset:0 lds\n"
  "s_mov_b64 exec, -1\n"
  "s_add_u32 s44, s44, 128\n"
  "v_mfma_f32_16x16x32_bf16 v[88:91], v[184:187], v[164:167], v[88:91]\n"
  "ds_read_b64_tr_b16 v[150:151], v27 offset:33792\n"
  "v_mfma_f32_16x16x32_bf16 v[60:63], v[188:191], v[160:163], v[60:63]\n"
  "ds_read_b64_tr_b16 v[152:153], v28 offset:32768\n"
  "s_lshl_b32 s40, %[wave], 11\n"
  "s_add_u32 s40, s40, 16384\n"
  "buffer_load_dwordx4 v[232:235], v10, %[srdk], s40 offen offset:0 nt\n"
  "v_mfma_f32_16x16x32_bf16 v[92:95], v[188:191], v[164:167], v[92:95]\n"
  "ds_read_b64_tr_b16 v[154:155], v28 offset:33792\n"
  "v_mfma_f32_16x16x32_bf16 v[64:67], v[192:195], v[160:163], v[64:67]\n"
  "s_setprio 0\n"
  "ds_read_b64_tr_b16 v[156:157], v29 offset:32768\n"
  "v_mfma_f32_16x16x32_bf16 v[96:99], v[192:195], v[164:167], v[96:99]\n"
  "ds_read_b64_tr_b16 v[158:159], v29 offset:33792\n"
  "v_mfma_f32_16x16x32_bf16 v[68:71], v[196:199], v[160:163], v[68:71]\n"
  "v_mfma_f32_16x16x32_bf16 v[100:103], v[196:199], v[164:167], v[100:103]\n"
  "v_mfma_f32_16x16x32_bf16 v[72:75], v[200:203], v[160:163], v[72:75]\n"
  "v_mfma_f32_16x16x32_bf16 v[104:107], v[200:203], v[164:167], v[104:107]\n"
  "s_waitcnt vmcnt(13) lgkmcnt(0)\n"
  "s_barrier\n"
  "v_mfma_f32_16x16x32_bf16 v[44:47], v[128:131], v[116:119], v[44:47]\n"
  "s_setprio 3\n"
  "ds_read_b64_tr_b16 v[172:173], v22 offset:49152\n"
  "ds_read_b64_tr_b16 v[174:175], v22 offset:50176\n"
  "v_mfma_f32_16x16x32_bf16 v[76:79], v[128:131], v[120:123], v[76:79]\n"
  "ds_read_b128 v[160:163], v20 offset:18944\n"
  "ds_read_b128 v[164:167], v20 offset:20992\n"
  "s_add_u32 m0, s43, 32768\n"
  "s_nop 0\n"
  "buffer_load_dwordx4 v34, %[srdw], s42 offen offset:0 lds\n"
  "v_mfma_f32_16x16x32_bf16 v[108:111], v[128:131], v[124:127], v[108:111]\n"
  "ds_read_b128 v[168:171], v21 offset:18944\n"
  "v_mfma_f32_16x16x32_bf16 v[48:51], v[132:135], v[116:119], v[48:51]\n"
  "ds_read_b64_tr_b16 v[176:177], v23 offset:49152\n"
  "ds_read_b64_tr_b16 v[178:179], v23 offset:50176\n"
  "v_mfma_f32_16x16x32_bf16 v[80:83], v[132:135], v[120:123], v[80:83]\n"
  "s_setprio 2\n"
  "ds_read_b64_tr_b16 v[180:181], v24 offset:49152\n"
  "buffer_load_dwordx4 v35, %[srdw], s42 offen offset:1024 lds\n"
  "s_add_u32 s42, s42, 262144\n"
  "v_mfma_f32_16x16x32_bf16 v[112:115], v[132:135], v[124:127], v[112:115]\n"
  "ds_read_b64_tr_b16 v[182:183], v24 offset:50176\n"
  "ds_read_b64_tr_b16 v[184:185], v25 offset:49152\n"
  "v_mfma_f32_16x16x32_bf16 v[52:55], v[136:139], v[116:119], v[52:55]\n"
  "ds_read_b64_tr_b16 v[186:187], v25 offset:50176\n"
  "v_mfma_f32_16x16x32_bf16 v[84:87], v[136:139], v[120:123], v[84:87]\n"
  "ds_read_b64_tr_b16 v[188:189], v26 offset:49152\n"
  "ds_read_b64_tr_b16 v[190:191], v26 offset:50176\n"
  "s_lshl_b32 s40, %[wave], 11\n"
  "s_add_u32 s40, s40, 16384\n"
  "buffer_load_dwordx4 v[236:239], v11, %[srdk], s40 offen offset:1024 nt\n"
  "v_mfma_f32_16x16x32_bf16 v[56:59], v[140:143], v[116:119], v[56:59]\n"
  "s_setprio 1\n"
  "ds_read_b64_tr_b16 v[192:193], v27 offset:49152\n"
  "v_mfma_f32_16x16x32_bf16 v[88:91], v[140:143], v[120:123], v[88:91]\n"
  "ds_read_b64_tr_b16 v[194:195], v27 offset:50176\n"
  "v_mfma_f32_16x16x32_bf16 v[60:63], v[144:147], v[116:119], v[60:63]\n"
  "ds_read_b64_tr_b16 v[196:197], v28 offset:49152\n"
  "v_mfma_f32_16x16x32_bf16 v[92:95], v[144:147], v[120:123], v[92:95]\n"
  "ds_read_b64_tr_b16 v[198:199], v28 offset:50176\n"
  "v_mfma_f32_16x16x32_bf16 v[64:67], v[148:151], v[116:119], v[64:67]\n"
  "s_setprio 0\n"
  "ds_read_b64_tr_b16 v[200:201], v29 offset:49152\n"
  "v_mfma_f32_16x16x32_bf16 v[96:99], v[148:151], v[120:123], v[96:99]\n"
  "ds_read_b64_tr_b16 v[202:203], v29 offset:50176\n"
  "v_mfma_f32_16x16x32_bf16 v[68:71], v[152:155], v[116:119], v[68:71]\n"
  "v_mfma_f32_16x16x32_bf16 v[100:103], v[152:155], v[120:123], v[100:103]\n"
  "v_mfma_f32_16x16x32_bf16 v[72:75], v[156:159], v[116:119], v[72:75]\n"
  "v_mfma_f32_16x16x32_bf16 v[104:107], v[156:159], v[120:123], v[104:107]\n"
  "s_waitcnt vmcnt(10) lgkmcnt(0)\n"
  "s_barrier\n"
  "v_mfma_f32_16x16x32_bf16 v[44:47], v[172:175], v[160:163], v[44:47]\n"
  "s_setprio 3\n"
  "ds_read_b64_tr_b16 v[128:129], v22 offset:0\n"
  "ds_read_b64_tr_b16 v[130:131], v22 offset:1024\n"
  "v_mfma_f32_16x16x32_bf16 v[76:79], v[172:175], v[164:167], v[76:79]\n"
  "ds_read_b128 v[116:119], v20 offset:36864\n"
  "ds_read_b128 v[120:123], v20 offset:38912\n"
  "s_add_u32 m0, s43, 49152\n"
  "s_nop 0\n"
  "buffer_load_dwordx4 v34, %[srdw], s42 offen offset:0 lds\n"
  "v_mfma_f32_16x16x32_bf16 v[108:111], v[172:175], v[168:171], v[108:111]\n"
  "ds_read_b128 v[124:127], v21 offset:36864\n"
  "v_mfma_f32_16x16x32_bf16 v[48:51], v[176:179], v[160:163], v[48:51]\n"
  "ds_read_b64_tr_b16 v[132:133], v23 offset:0\n"
  "ds_read_b64_tr_b16 v[134:135], v23 offset:1024\n"
  "v_mfma_f32_16x16x32_bf16 v[80:83], v[176:179], v[164:167], v[80:83]\n"
  "s_setprio 2\n"
  "ds_read_b64_tr_b16 v[136:137], v24 offset:0\n"
  "buffer_load_dwordx4 v35, %[srdw], s42 offen offset:1024 lds\n"
  "s_add_u32 s42, s42, 262144\n"
  "v_mfma_f32_16x16x32_bf16 v[112:115], v[176:179], v[168:171], v[112:115]\n"
  "ds_read_b64_tr_b16 v[138:139], v24 offset:1024\n"
  "ds_read_b64_tr_b16 v[140:141], v25 offset:0\n"
  "v_mfma_f32_16x16x32_bf16 v[52:55], v[180:183], v[160:163], v[52:55]\n"
  "ds_read_b64_tr_b16 v[142:143], v25 offset:1024\n"
  "v_mfma_f32_16x16x32_bf16 v[84:87], v[180:183], v[164:167], v[84:87]\n"
  "ds_read_b64_tr_b16 v[144:145], v26 offset:0\n"
  "ds_read_b64_tr_b16 v[146:147], v26 offset:1024\n"
  "s_lshl_b32 s40, %[wave], 11\n"
  "s_add_u32 s40, s40, 147456\n"
  "buffer_load_dwordx4 v[36:39], v10, %[srdk], s40 offen offset:0 nt\n"
  "v_mfma_f32_16x16x32_bf16 v[56:59], v[184:187], v[160:163], v[56:59]\n"
  "s_setprio 1\n"
  "ds_read_b64_tr_b16 v[148:149], v27 offset:0\n"
  "v_mfma_f32_16x16x32_bf16 v[88:91], v[184:187], v[164:167], v[88:91]\n"
  "ds_read_b64_tr_b16 v[150:151], v27 offset:1024\n"
  "v_mfma_f32_16x16x32_bf16 v[60:63], v[188:191], v[160:163], v[60:63]\n"
  "ds_read_b64_tr_b16 v[152:153], v28 offset:0\n"
  "v_mfma_f32_16x16x32_bf16 v[92:95], v[188:191], v[164:167], v[92:95]\n"
  "ds_read_b64_tr_b16 v[154:155], v28 offset:1024\n"
  "v_mfma_f32_16x16x32_bf16 v[64:67], v[192:195], v[160:163], v[64:67]\n"
  "s_setprio 0\n"
  "ds_read_b64_tr_b16 v[156:157], v29 offset:0\n"
  "v_mfma_f32_16x16x32_bf16 v[96:99], v[192:195], v[164:167], v[96:99]\n"
  "ds_read_b64_tr_b16 v[158:159], v29 offset:1024\n"
  "v_mfma_f32_16x16x32_bf16 v[68:71], v[196:199], v[160:163], v[68:71]\n"
  "v_mfma_f32_16x16x32_bf16 v[100:103], v[196:199], v[164:167], v[100:103]\n"
  "v_mfma_f32_16x16x32_bf16 v[72:75], v[200:203], v[160:163], v[72:75]\n"
  "v_mfma_f32_16x16x32_bf16 v[104:107], v[200:203], v[164:167], v[104:107]\n"
  "s_waitcnt vmcnt(10) lgkmcnt(0)\n"
  "s_barrier\n"
  "v_mfma_f32_16x16x32_bf16 v[44:47], v[128:131], v[116:119], v[44:47]\n"
  "s_setprio 3\n"
  "ds_read_b64_tr_b16 v[172:173], v22 offset:16384\n"
  "ds_read_b64_tr_b16 v[174:175], v22 offset:17408\n"
  "v_mfma_f32_16x16x32_bf16 v[76:79], v[128:131], v[120:123], v[76:79]\n"
  "ds_read_b128 v[160:163], v20 offset:37376\n"
  "ds_read_b128 v[164:167], v20 offset:39424\n"
  "s_lshl_b32 s40, %[wave], 11\n"
  "s_add_u32 s40, s40, 147456\n"
  "buffer_load_dwordx4 v[40:43], v11, %[srdk], s40 offen offset:1024 nt\n"
  "v_mfma_f32_16x16x32_bf16 v[108:111], v[128:131], v[124:127], v[108:111]\n"
  "ds_read_b128 v[168:171], v21 offset:37376\n"
  "v_mfma_f32_16x16x32_bf16 v[48:51], v[132:135], v[116:119], v[48:51]\n"
  "ds_read_b64_tr_b16 v[176:177], v23 offset:16384\n"
  "ds_read_b64_tr_b16 v[178:179], v23 offset:17408\n"
  "v_mfma_f32_16x16x32_bf16 v[80:83], v[132:135], v[120:123], v[80:83]\n"
  "s_setprio 2\n"
  "ds_read_b64_tr_b16 v[180:181], v24 offset:16384\n"
  "v_mfma_f32_16x16x32_bf16 v[112:115], v[132:135], v[124:127], v[112:115]\n"
  "ds_read_b64_tr_b16 v[182:183], v24 offset:17408\n"
  "ds_read_b64_tr_b16 v[184:185], v25 offset:16384\n"
  "v_mfma_f32_16x16x32_bf16 v[52:55], v[136:139], v[116:119], v[52:55]\n"
  "ds_read_b64_tr_b16 v[186:187], v25 offset:17408\n"
  "v_mfma_f32_16x16x32_bf16 v[84:87], v[136:139], v[120:123], v[84:87]\n"
  "ds_read_b64_tr_b16 v[188:189], v26 offset:16384\n"
  "ds_read_b64_tr_b16 v[190:191], v26 offset:17408\n"
  "v_mfma_f32_16x16x32_bf16 v[56:59], v[140:143], v[116:119], v[56:59]\n"
  "s_setprio 1\n"
  "ds_read_b64_tr_b16 v[192:193], v27 offset:16384\n"
  "v_mfma_f32_16x16x32_bf16 v[88:91], v[140:143], v[120:123], v[88:91]\n"
  "ds_read_b64_tr_b16 v[194:195], v27 offset:17408\n"
  "v_mfma_f32_16x16x32_bf16 v[60:63], v[144:147], v[116:119], v[60:63]\n"
  "ds_read_b64_tr_b16 v[196:197], v28 offset:16384\n"
  "v_mfma_f32_16x16x32_bf16 v[92:95], v[144:147], v[120:123], v[92:95]\n"
  "ds_read_b64_tr_b16 v[198:199], v28 offset:17408\n"
  "v_mfma_f32_16x16x32_bf16 v[64:67], v[148:151], v[116:119], v[64:67]\n"
  "s_setprio 0\n"
  "ds_read_b64_tr_b16 v[200:201], v29 offset:16384\n"
  "v_mfma_f32_16x16x32_bf16 v[96:99], v[148:151], v[120:123], v[96:99]\n"
  "ds_read_b64_tr_b16 v[202:203], v29 offset:17408\n"
  "v_mfma_f32_16x16x32_bf16 v[68:71], v[152:155], v[116:119], v[68:71]\n"
  "v_mfma_f32_16x16x32_bf16 v[100:103], v[152:155], v[120:123], v[100:103]\n"
  "v_mfma_f32_16x16x32_bf16 v[72:75], v[156:159], v[116:119], v[72:75]\n"
  "v_mfma_f32_16x16x32_bf16 v[104:107], v[156:159], v[120:123], v[104:107]\n"
  "s_waitcnt vmcnt(5) lgkmcnt(0)\n"
  "s_barrier\n"
  "v_mfma_f32_16x16x32_bf16 v[44:47], v[172:175], v[160:163], v[44:47]\n"
  "s_setprio 3\n"
  "ds_read_b64_tr_b16 v[128:129], v22 offset:32768\n"
  "ds_read_b64_tr_b16 v[130:131], v22 offset:33792\n"
  "v_mfma_f32_16x16x32_bf16 v[76:79], v[172:175], v[164:167], v[76:79]\n"
  "ds_read_b128 v[116:119], v20 offset:0\n"
  "ds_read_b128 v[120:123], v20 offset:2048\n"
  "v_mfma_f32_16x16x32_bf16 v[108:111], v[172:175], v[168:171], v[108:111]\n"
  "ds_read_b128 v[124:127], v21 offset:0\n"
  "v_mfma_f32_16x16x32_bf16 v[48:51], v[176:179], v[160:163], v[48:51]\n"
  "ds_read_b64_tr_b16 v[132:133], v23 offset:32768\n"
  "ds_read_b64_tr_b16 v[134:135], v23 offset:33792\n"
  "v_mfma_f32_16x16x32_bf16 v[80:83], v[176:179], v[164:167], v[80:83]\n"
  "s_setprio 2\n"
  "ds_read_b64_tr_b16 v[136:137], v24 offset:32768\n"
  "v_mfma_f32_16x16x32_bf16 v[112:115], v[176:179], v[168:171], v[112:115]\n"
  "ds_read_b64_tr_b16 v[138:139], v24 offset:33792\n"
  "ds_read_b64_tr_b16 v[140:141], v25 offset:32768\n"
  "v_mfma_f32_16x16x32_bf16 v[52:55], v[180:183], v[160:163], v[52:55]\n"
  "ds_read_b64_tr_b16 v[142:143], v25 offset:33792\n"
  "v_mfma_f32_16x16x32_bf16 v[84:87], v[180:183], v[164:167], v[84:87]\n"
  "ds_read_b64_tr_b16 v[144:145], v26 offset:32768\n"
  "ds_read_b64_tr_b16 v[146:147], v26 offset:33792\n"
  "v_mfma_f32_16x16x32_bf16 v[56:59], v[184:187], v[160:163], v[56:59]\n"
  "s_setprio 1\n"
  "ds_read_b64_tr_b16 v[148:149], v27 offset:32768\n"
  "v_mfma_f32_16x16x32_bf16 v[88:91], v[184:187], v[164:167], v[88:91]\n"
  "ds_read_b64_tr_b16 v[150:151], v27 offset:33792\n"
  "v_mfma_f32_16x16x32_bf16 v[60:63], v[188:191], v[160:163], v[60:63]\n"
  "ds_read_b64_tr_b16 v[152:153], v28 offset:32768\n"
  "v_mfma_f32_16x16x32_bf16 v[92:95], v[188:191], v[164:167], v[92:95]\n"
  "ds_read_b64_tr_b16 v[154:155], v28 offset:33792\n"
  "v_mfma_f32_16x16x32_bf16 v[64:67], v[192:195], v[160:163], v[64:67]\n"
  "s_setprio 0\n"
  "ds_read_b64_tr_b16 v[156:157], v29 offset:32768\n"
  "v_mfma_f32_16x16x32_bf16 v[96:99], v[192:195], v[164:167], v[96:99]\n"
  "ds_read_b64_tr_b16 v[158:159], v29 offset:33792\n"
  "v_mfma_f32_16x16x32_bf16 v[68:71], v[196:199], v[160:163], v[68:71]\n"
  "v_mfma_f32_16x16x32_bf16 v[100:103], v[196:199], v[164:167], v[100:103]\n"
  "v_mfma_f32_16x16x32_bf16 v[72:75], v[200:203], v[160:163], v[72:75]\n"
  "v_mfma_f32_16x16x32_bf16 v[104:107], v[200:203], v[164:167], v[104:107]\n"
  "s_waitcnt vmcnt(2) lgkmcnt(0)\n"
  "s_barrier\n"
  "v_mfma_f32_16x16x32_bf16 v[44:47], v[128:131], v[116:119], v[44:47]\n"
  "s_setprio 3\n"
  "ds_read_b64_tr_b16 v[172:173], v22 offset:49152\n"
  "ds_read_b64_tr_b16 v[174:175], v22 offset:50176\n"
  "v_mfma_f32_16x16x32_bf16 v[76:79], v[128:131], v[120:123], v[76:79]\n"
  "ds_read_b128 v[160:163], v20 offset:512\n"
  "ds_read_b128 v[164:167], v20 offset:2560\n"
  "v_mfma_f32_16x16x32_bf16 v[108:111], v[128:131], v[124:127], v[108:111]\n"
  "ds_read_b128 v[168:171], v21 offset:512\n"
  "v_mfma_f32_16x16x32_bf16 v[48:51], v[132:135], v[116:119], v[48:51]\n"
  "ds_read_b64_tr_b16 v[176:177], v23 offset:49152\n"
  "ds_read_b64_tr_b16 v[178:179], v23 offset:50176\n"
  "v_mfma_f32_16x16x32_bf16 v[80:83], v[132:135], v[120:123], v[80:83]\n"
  "s_setprio 2\n"
  "ds_read_b64_tr_b16 v[180:181], v24 offset:49152\n"
  "v_mfma_f32_16x16x32_bf16 v[112:115], v[132:135], v[124:127], v[112:115]\n"
  "ds_read_b64_tr_b16 v[182:183], v24 offset:50176\n"
  "ds_read_b64_tr_b16 v[184:185], v25 offset:49152\n"
  "v_mfma_f32_16x16x32_bf16 v[52:55], v[136:139], v[116:119], v[52:55]\n"
  "ds_read_b64_tr_b16 v[186:187], v25 offset:50176\n"
  "v_mfma_f32_16x16x32_bf16 v[84:87], v[136:139], v[120:123], v[84:87]\n"
  "ds_read_b64_tr_b16 v[188:189], v26 offset:49152\n"
  "ds_read_b64_tr_b16 v[190:191], v26 offset:50176\n"
  "v_mfma_f32_16x16x32_bf16 v[56:59], v[140:143], v[116:119], v[56:59]\n"
  "s_setprio 1\n"
  "ds_read_b64_tr_b16 v[192:193], v27 offset:49152\n"
  "v_mfma_f32_16x16x32_bf16 v[88:91], v[140:143], v[120:123], v[88:91]\n"
  "ds_read_b64_tr_b16 v[194:195], v27 offset:50176\n"
  "v_mfma_f32_16x16x32_bf16 v[60:63], v[144:147], v[116:119], v[60:63]\n"
  "ds_read_b64_tr_b16 v[196:197], v28 offset:49152\n"
  "v_mfma_f32_16x16x32_bf16 v[92:95], v[144:147], v[120:123], v[92:95]\n"
  "ds_read_b64_tr_b16 v[198:199], v28 offset:50176\n"
  "v_mfma_f32_16x16x32_bf16 v[64:67], v[148:151], v[116:119], v[64:67]\n"
  "s_setprio 0\n"
  "ds_read_b64_tr_b16 v[200:201], v29 offset:49152\n"
  "v_mfma_f32_16x16x32_bf16 v[96:99], v[148:151], v[120:123], v[96:99]\n"
  "ds_read_b64_tr_b16 v[202:203], v29 offset:50176\n"
  "v_mfma_f32_16x16x32_bf16 v[68:71], v[152:155], v[116:119], v[68:71]\n"
  "v_mfma_f32_16x16x32_bf16 v[100:103], v[152:155], v[120:123], v[100:103]\n"
  "v_mfma_f32_16x16x32_bf16 v[72:75], v[156:159], v[116:119], v[72:75]\n"
  "v_mfma_f32_16x16x32_bf16 v[104:107], v[156:159], v[120:123], v[104:107]\n"
  "s_waitcnt lgkmcnt(0)\n"
  "s_barrier\n"
  "v_mfma_f32_16x16x32_bf16 v[44:47], v[172:175], v[160:163], v[44:47]\n"
  "s_setprio 3\n"
  "v_mfma_f32_16x16x32_bf16 v[76:79], v[172:175], v[164:167], v[76:79]\n"
  "s_add_u32 m0, s51, 81920\n"
  "s_add_u32 s40, s50, 16384\n"
  "buffer_load_dwordx4 v10, %[srdv], s40 offen offset:0 nt lds\n"
  "v_mfma_f32_16x16x32_bf16 v[108:111], v[172:175], v[168:171], v[108:111]\n"
  "v_mfma_f32_16x16x32_bf16 v[48:51], v[176:179], v[160:163], v[48:51]\n"
  "v_mfma_f32_16x16x32_bf16 v[80:83], v[176:179], v[164:167], v[80:83]\n"
  "s_setprio 2\n"
  "buffer_load_dwordx4 v11, %[srdv], s40 offen offset:1024 nt lds\n"
  "v_mfma_f32_16x16x32_bf16 v[112:115], v[176:179], v[168:171], v[112:115]\n"
  "v_mfma_f32_16x16x32_bf16 v[52:55], v[180:183], v[160:163], v[52:55]\n"
  "v_mfma_f32_16x16x32_bf16 v[84:87], v[180:183], v[164:167], v[84:87]\n"
  "buffer_load_dwordx4 v10, %[srdv], s40 offen offset:2048 nt lds\n"
  "v_mfma_f32_16x16x32_bf16 v[56:59], v[184:187], v[160:163], v[56:59]\n"
  "s_setprio 1\n"
  "v_mfma_f32_16x16x32_bf16 v[88:91], v[184:187], v[164:167], v[88:91]\n"
  "v_mfma_f32_16x16x32_bf16 v[60:63], v[188:191], v[160:163], v[60:63]\n"
  "buffer_load_dwordx4 v11, %[srdv], s40 offen offset:3072 nt lds\n"
  "v_mfma_f32_16x16x32_bf16 v[92:95], v[188:191], v[164:167], v[92:95]\n"
  "v_mfma_f32_16x16x32_bf16 v[64:67], v[192:195], v[160:163], v[64:67]\n"
  "s_setprio 0\n"
  "v_mfma_f32_16x16x32_bf16 v[96:99], v[192:195], v[164:167], v[96:99]\n"
  "v_mfma_f32_16x16x32_bf16 v[68:71], v[196:199], v[160:163], v[68:71]\n"
  "v_mfma_f32_16x16x32_bf16 v[100:103], v[196:199], v[164:167], v[100:103]\n"
  "v_mfma_f32_16x16x32_bf16 v[72:75], v[200:203], v[160:163], v[72:75]\n"
  "v_mfma_f32_16x16x32_bf16 v[104:107], v[200:203], v[164:167], v[104:107]\n"
  "s_mov_b32 s58, 0x3e0293ee\n"
  "s_mov_b32 s59, 0x3e0293ee\n"
  "s_add_u32 s38, s43, 0\n"
  "v_add_u32 v8, s38, v17\n"
  "v_add_u32 v9, 65536, v8\n"
  "s_waitcnt vmcnt(4)\n"
  "ds_write_b128 v8, v[248:251] offset:0\n"
  "v_pk_mul_f32 v[108:109], v[108:109], s[58:59]\n"
  "v_pk_mul_f32 v[110:111], v[110:111], s[58:59]\n"
  "v_pk_mul_f32 v[112:113], v[112:113], s[58:59]\n"
  "v_pk_mul_f32 v[114:115], v[114:115], s[58:59]\n"
  "ds_write_b128 v8, v[204:207] offset:1024\n"
  "ds_write_b128 v8, v[208:211] offset:32768\n"
  "v_pk_mul_f32 v[44:45], v[44:45], s[58:59]\n"
  "v_pk_mul_f32 v[46:47], v[46:47], s[58:59]\n"
  "v_pk_mul_f32 v[48:49], v[48:49], s[58:59]\n"
  "v_pk_mul_f32 v[50:51], v[50:51], s[58:59]\n"
  "ds_write_b128 v8, v[212:215] offset:33792\n"
  "v_cvt_pk_bf16_f32 v248, v108, v109\n"
  "v_cvt_pk_bf16_f32 v249, v110, v111\n"
  "v_cvt_pk_bf16_f32 v250, v112, v113\n"
  "v_cvt_pk_bf16_f32 v251, v114, v115\n"
  "ds_write_b128 v8, v[216:219] offset:16384\n"
  "v_pk_mul_f32 v[52:53], v[52:53], s[58:59]\n"
  "v_pk_mul_f32 v[54:55], v[54:55], s[58:59]\n"
  "v_pk_mul_f32 v[56:57], v[56:57], s[58:59]\n"
  "v_pk_mul_f32 v[58:59], v[58:59], s[58:59]\n"
  "v_cvt_pk_bf16_f32 v116, v44, v45\n"
  "v_cvt_pk_bf16_f32 v117, v46, v47\n"
  "v_cvt_pk_bf16_f32 v118, v48, v49\n"
  "v_cvt_pk_bf16_f32 v119, v50, v51\n"
  "v_permlane32_swap_b32 v248, v250\n"
  "v_permlane32_swap_b32 v249, v251\n"
  "v_mov_b64 v[44:45], 0\n"
  "v_mov_b64 v[46:47], 0\n"
  "v_permlane16_swap_b32 v248, v250\n"
  "v_permlane16_swap_b32 v249, v251\n"
  "v_mov_b64 v[48:49], 0\n"
  "v_mov_b64 v[50:51], 0\n"
  "s_lshl_b32 s38, s37, 10\n"
  "v_add_u32 v6, s38, v246\n"
  "ds_write_b128 v6, v[248:251] offset:0\n"
  "ds_write_b128 v8, v[220:223] offset:17408\n"
  "v_pk_mul_f32 v[60:61], v[60:61], s[58:59]\n"
  "v_pk_mul_f32 v[62:63], v[62:63], s[58:59]\n"
  "v_pk_mul_f32 v[64:65], v[64:65], s[58:59]\n"
  "v_pk_mul_f32 v[66:67], v[66:67], s[58:59]\n"
  "v_cvt_pk_bf16_f32 v120, v52, v53\n"
  "v_cvt_pk_bf16_f32 v121, v54, v55\n"
  "v_cvt_pk_bf16_f32 v122, v56, v57\n"
  "v_cvt_pk_bf16_f32 v123, v58, v59\n"
  "v_permlane32_swap_b32 v116, v118\n"
  "v_permlane32_swap_b32 v117, v119\n"
  "v_mov_b64 v[52:53], 0\n"
  "v_mov_b64 v[54:55], 0\n"
  "v_permlane16_swap_b32 v116, v118\n"
  "v_permlane16_swap_b32 v117, v119\n"
  "v_mov_b64 v[56:57], 0\n"
  "v_mov_b64 v[58:59], 0\n"
  "ds_write_b128 v8, v[224:227] offset:49152\n"
  "v_pk_mul_f32 v[68:69], v[68:69], s[58:59]\n"
  "v_pk_mul_f32 v[70:71], v[70:71], s[58:59]\n"
  "v_pk_mul_f32 v[72:73], v[72:73], s[58:59]\n"
  "v_pk_mul_f32 v[74:75], v[74:75], s[58:59]\n"
  "v_cvt_pk_bf16_f32 v124, v60, v61\n"
  "v_cvt_pk_bf16_f32 v125, v62, v63\n"
  "v_cvt_pk_bf16_f32 v126, v64, v65\n"
  "v_cvt_pk_bf16_f32 v127, v66, v67\n"
  "v_permlane32_swap_b32 v120, v122\n"
  "v_permlane32_swap_b32 v121, v123\n"
  "v_mov_b64 v[60:61], 0\n"
  "v_mov_b64 v[62:63], 0\n"
  "v_permlane16_swap_b32 v120, v122\n"
  "v_permlane16_swap_b32 v121, v123\n"
  "v_mov_b64 v[64:65], 0\n"
  "v_mov_b64 v[66:67], 0\n"
  "ds_write_b128 v8, v[228:231] offset:50176\n"
  "v_pk_mul_f32 v[76:77], v[76:77], s[58:59]\n"
  "v_pk_mul_f32 v[78:79], v[78:79], s[58:59]\n"
  "v_pk_mul_f32 v[80:81], v[80:81], s[58:59]\n"
  "v_pk_mul_f32 v[82:83], v[82:83], s[58:59]\n"
  "v_cvt_pk_bf16_f32 v128, v68, v69\n"
  "v_cvt_pk_bf16_f32 v129, v70, v71\n"
  "v_cvt_pk_bf16_f32 v130, v72, v73\n"
  "v_cvt_pk_bf16_f32 v131, v74, v75\n"
  "v_permlane32_swap_b32 v124, v126\n"
  "v_permlane32_swap_b32 v125, v127\n"
  "v_mov_b64 v[68:69], 0\n"
  "v_mov_b64 v[70:71], 0\n"
  "v_permlane16_swap_b32 v124, v126\n"
  "v_permlane16_swap_b32 v125, v127\n"
  "v_mov_b64 v[72:73], 0\n"
  "v_mov_b64 v[74:75], 0\n"
  "ds_write_b128 v9, v[232:235] offset:0\n"
  "v_pk_mul_f32 v[84:85], v[84:85], s[58:59]\n"
  "v_pk_mul_f32 v[86:87], v[86:87], s[58:59]\n"
  "v_pk_mul_f32 v[88:89], v[88:89], s[58:59]\n"
  "v_pk_mul_f32 v[90:91], v[90:91], s[58:59]\n"
  "v_cvt_pk_bf16_f32 v132, v76, v77\n"
  "v_cvt_pk_bf16_f32 v133, v78, v79\n"
  "v_cvt_pk_bf16_f32 v134, v80, v81\n"
  "v_cvt_pk_bf16_f32 v135, v82, v83\n"
  "v_permlane32_swap_b32 v128, v130\n"
  "v_permlane32_swap_b32 v129, v131\n"
  "v_mov_b64 v[76:77], 0\n"
  "v_mov_b64 v[78:79], 0\n"
  "v_permlane16_swap_b32 v128, v130\n"
  "v_permlane16_swap_b32 v129, v131\n"
  "v_mov_b64 v[80:81], 0\n"
  "v_mov_b64 v[82:83], 0\n"
  "ds_write_b128 v9, v[236:239] offset:1024\n"
  "v_pk_mul_f32 v[92:93], v[92:93], s[58:59]\n"
  "v_pk_mul_f32 v[94:95], v[94:95], s[58:59]\n"
  "v_pk_mul_f32 v[96:97], v[96:97], s[58:59]\n"
  "v_pk_mul_f32 v[98:99], v[98:99], s[58:59]\n"
  "v_cvt_pk_bf16_f32 v136, v84, v85\n"
  "v_cvt_pk_bf16_f32 v137, v86, v87\n"
  "v_cvt_pk_bf16_f32 v138, v88, v89\n"
  "v_cvt_pk_bf16_f32 v139, v90, v91\n"
  "v_permlane32_swap_b32 v132, v134\n"
  "v_permlane32_swap_b32 v133, v135\n"
  "v_mov_b64 v[84:85], 0\n"
  "v_mov_b64 v[86:87], 0\n"
  "v_permlane16_swap_b32 v132, v134\n"
  "v_permlane16_swap_b32 v133, v135\n"
  "v_mov_b64 v[88:89], 0\n"
  "v_mov_b64 v[90:91], 0\n"
  "ds_write_b128 v9, v[36:39] offset:32768\n"
  "v_pk_mul_f32 v[100:101], v[100:101], s[58:59]\n"
  "v_pk_mul_f32 v[102:103], v[102:103], s[58:59]\n"
  "v_pk_mul_f32 v[104:105], v[104:105], s[58:59]\n"
  "v_pk_mul_f32 v[106:107], v[106:107], s[58:59]\n"
  "v_cvt_pk_bf16_f32 v140, v92, v93\n"
  "v_cvt_pk_bf16_f32 v141, v94, v95\n"
  "v_cvt_pk_bf16_f32 v142, v96, v97\n"
  "v_cvt_pk_bf16_f32 v143, v98, v99\n"
  "v_permlane32_swap_b32 v136, v138\n"
  "v_permlane32_swap_b32 v137, v139\n"
  "v_mov_b64 v[92:93], 0\n"
  "v_mov_b64 v[94:95], 0\n"
  "v_permlane16_swap_b32 v136, v138\n"
  "v_permlane16_swap_b32 v137, v139\n"
  "v_mov_b64 v[96:97], 0\n"
  "v_mov_b64 v[98:99], 0\n"
  "ds_write_b128 v9, v[40:43] offset:33792\n"
  "v_cvt_pk_bf16_f32 v144, v100, v101\n"
  "v_cvt_pk_bf16_f32 v145, v102, v103\n"
  "v_cvt_pk_bf16_f32 v146, v104, v105\n"
  "v_cvt_pk_bf16_f32 v147, v106, v107\n"
  "v_permlane32_swap_b32 v140, v142\n"
  "v_permlane32_swap_b32 v141, v143\n"
  "v_mov_b64 v[100:101], 0\n"
  "v_mov_b64 v[102:103], 0\n"
  "v_permlane16_swap_b32 v140, v142\n"
  "v_permlane16_swap_b32 v141, v143\n"
  "v_mov_b64 v[104:105], 0\n"
  "v_mov_b64 v[106:107], 0\n"
  "v_permlane32_swap_b32 v144, v146\n"
  "v_permlane32_swap_b32 v145, v147\n"
  "s_nop 0\n"
  "v_permlane16_swap_b32 v144, v146\n"
  "v_permlane16_swap_b32 v145, v147\n"
  "v_mov_b64 v[108:109], 0\n"
  "v_mov_b64 v[110:111], 0\n"
  "v_mov_b64 v[112:113], 0\n"
  "v_mov_b64 v[114:115], 0\n"
  "s_waitcnt lgkmcnt(0)\n"
  "v_readfirstlane_b32 s34, v33\n"
  "s_nop 4\n"
  "s_add_u32 s34, s34, 1\n"
  "s_barrier\n"
  "ds_read_b128 v[148:151], v246 offset:0\n"
  "ds_read_b128 v[152:155], v246 offset:1024\n"
  "ds_read_b128 v[156:159], v246 offset:2048\n"
  "ds_read_b128 v[160:163], v246 offset:3072\n"
  "v_mov_b32 v32, 0x3f803f80\n"
  "v_mov_b32 v33, 0x3f803f80\n"
  "v_mov_b32 v34, 0x3f803f80\n"
  "v_mov_b32 v35, 0x3f803f80\n"
  "v_mov_b64 v[36:37], 0\n"
  "v_mov_b64 v[38:39], 0\n"
  "v_mov_b64 v[40:41], 0\n"
  "v_mov_b64 v[42:43], 0\n"
  "v_mov_b64 v[18:19], 0\n"
  "P1_ATT:\n"
  "s_nop 7\n"
  "s_nop 3\n"
  "s_cmp_eq_u32 s37, 1\n"
  "s_cbranch_scc1 P1_W1\n"
  "s_cmp_eq_u32 s37, 2\n"
  "s_cbranch_scc1 P1_W2\n"
  "s_cmp_eq_u32 s37, 3\n"
  "s_cbranch_scc1 P1_W3\n"
  "ds_read_b128 v[216:219], v12 offset:0\n"
  "ds_read_b128 v[220:223], v12 offset:256\n"
  "ds_read_b128 v[224:227], v12 offset:512\n"
  "ds_read_b128 v[228:231], v12 offset:768\n"
  "s_waitcnt lgkmcnt(3)\n"
  "v_mfma_f32_16x16x32_bf16 v[196:199], v[216:219], v[148:151], 0\n"
  "v_mfma_f32_16x16x32_bf16 v[164:167], v[216:219], v[116:119], 0\n"
  "v_mfma_f32_16x16x32_bf16 v[180:183], v[216:219], v[132:135], 0\n"
  "s_waitcnt lgkmcnt(2)\n"
  "v_mfma_f32_16x16x32_bf16 v[196:199], v[220:223], v[152:155], v[196:199]\n"
  "v_mfma_f32_16x16x32_bf16 v[164:167], v[220:223], v[120:123], v[164:167]\n"
  "v_mfma_f32_16x16x32_bf16 v[180:183], v[220:223], v[136:139], v[180:183]\n"
  "s_waitcnt lgkmcnt(1)\n"
  "v_mfma_f32_16x16x32_bf16 v[196:199], v[224:227], v[156:159], v[196:199]\n"
  "v_mfma_f32_16x16x32_bf16 v[164:167], v[224:227], v[124:127], v[164:167]\n"
  "v_mfma_f32_16x16x32_bf16 v[180:183], v[224:227], v[140:143], v[180:183]\n"
  "s_waitcnt lgkmcnt(0)\n"
  "v_mfma_f32_16x16x32_bf16 v[196:199], v[228:231], v[160:163], v[196:199]\n"
  "v_mfma_f32_16x16x32_bf16 v[164:167], v[228:231], v[128:131], v[164:167]\n"
  "v_mfma_f32_16x16x32_bf16 v[180:183], v[228:231], v[144:147], v[180:183]\n"
  "s_nop 5\n"
  "v_max3_f32 v248, v196, v197, v198\n"
  "v_max_f32 v248, v248, v199\n"
  "v_mov_b32 v249, v248\n"
  "s_nop 1\n"
  "v_permlane32_swap_b32 v248, v249\n"
  "v_max_f32 v248, v248, v249\n"
  "v_mov_b32 v249, v248\n"
  "s_nop 1\n"
  "v_permlane16_swap_b32 v248, v249\n"
  "v_max_f32 v248, v248, v249\n"
  "v_sub_f32 v28, 0, v248\n"
  "v_mov_b32 v29, v28\n"
  "v_mov_b32 v30, v28\n"
  "v_mov_b32 v31, v28\n"
  "v_max3_f32 v248, v164, v165, v166\n"
  "v_max_f32 v248, v248, v167\n"
  "v_mov_b32 v249, v248\n"
  "s_nop 1\n"
  "v_permlane32_swap_b32 v248, v249\n"
  "v_max_f32 v248, v248, v249\n"
  "v_mov_b32 v249, v248\n"
  "s_nop 1\n"
  "v_permlane16_swap_b32 v248, v249\n"
  "v_max_f32 v248, v248, v249\n"
  "v_sub_f32 v20, 0, v248\n"
  "v_mov_b32 v21, v20\n"
  "v_mov_b32 v22, v20\n"
  "v_mov_b32 v23, v20\n"
  "v_max3_f32 v248, v180, v181, v182\n"
  "v_max_f32 v248, v248, v183\n"
  "v_mov_b32 v249, v248\n"
  "s_nop 1\n"
  "v_permlane32_swap_b32 v248, v249\n"
  "v_max_f32 v248, v248, v249\n"
  "v_mov_b32 v249, v248\n"
  "s_nop 1\n"
  "v_permlane16_swap_b32 v248, v249\n"
  "v_max_f32 v248, v248, v249\n"
  "v_sub_f32 v24, 0, v248\n"
  "v_mov_b32 v25, v24\n"
  "v_mov_b32 v26, v24\n"
  "v_mov_b32 v27, v24\n"
  "v_lshrrev_b32 v7, 2, v1\n"
  "v_mul_u32_u24 v7, 1280, v7\n"
  "v_and_b32 v8, 3, v1\n"
  "v_lshl_add_u32 v7, v8, 7, v7\n"
  "s_lshl_b32 s38, %[bb], 3\n"
  "s_add_u32 s38, s38, %[wave]\n"
  "s_mul_i32 s38, s38, 20480\n"
  "buffer_load_dword v254, v7, %[srdwo], s38 offen offset:0\n"
  "s_waitcnt lgkmcnt(0)\n"
  "s_nop 7\n"
  "s_nop 3\n"
  "P1_RST0:\n"
  "ds_read_b128 v[200:203], v12 offset:0\n"
  "ds_read_b128 v[204:207], v12 offset:256\n"
  "ds_read_b128 v[208:211], v12 offset:512\n"
  "ds_read_b128 v[212:215], v12 offset:768\n"
  "s_nop 7\n"
  "s_nop 3\n"
  "s_mov_b32 s41, 3\n"
  "P1_LOOP0:\n"
  "s_waitcnt lgkmcnt(3)\n"
  "v_mfma_f32_16x16x32_bf16 v[196:199], v[200:203], v[148:151], v[28:31]\n"
  "ds_read_b128 v[216:219], v12 offset:4096\n"
  "v_mfma_f32_16x16x32_bf16 v[164:167], v[200:203], v[116:119], v[20:23]\n"
  "v_mfma_f32_16x16x32_bf16 v[180:183], v[200:203], v[132:135], v[24:27]\n"
  "ds_read_b128 v[220:223], v12 offset:4352\n"
  "s_waitcnt lgkmcnt(4)\n"
  "v_mfma_f32_16x16x32_bf16 v[196:199], v[204:207], v[152:155], v[196:199]\n"
  "s_setprio 2\n"
  "v_mfma_f32_16x16x32_bf16 v[164:167], v[204:207], v[120:123], v[164:167]\n"
  "ds_read_b128 v[224:227], v12 offset:4608\n"
  "v_mfma_f32_16x16x32_bf16 v[180:183], v[204:207], v[136:139], v[180:183]\n"
  "s_waitcnt lgkmcnt(4)\n"
  "v_mfma_f32_16x16x32_bf16 v[196:199], v[208:211], v[156:159], v[196:199]\n"
  "ds_read_b128 v[228:231], v12 offset:4864\n"
  "v_mfma_f32_16x16x32_bf16 v[164:167], v[208:211], v[124:127], v[164:167]\n"
  "v_mfma_f32_16x16x32_bf16 v[180:183], v[208:211], v[140:143], v[180:183]\n"
  "s_waitcnt lgkmcnt(4)\n"
  "v_mfma_f32_16x16x32_bf16 v[196:199], v[212:215], v[160:163], v[196:199]\n"
  "v_mfma_f32_16x16x32_bf16 v[164:167], v[212:215], v[128:131], v[164:167]\n"
  "v_mfma_f32_16x16x32_bf16 v[180:183], v[212:215], v[144:147], v[180:183]\n"
  "s_setprio 1\n"
  "s_waitcnt lgkmcnt(3)\n"
  "v_mfma_f32_16x16x32_bf16 v[168:171], v[216:219], v[116:119], v[20:23]\n"
  "ds_read_b128 v[200:203], v12 offset:8192\n"
  "v_mfma_f32_16x16x32_bf16 v[184:187], v[216:219], v[132:135], v[24:27]\n"
  "s_nop 0\n"
  "v_exp_f32 v196, v196\n"
  "ds_read_b128 v[204:207], v12 offset:8448\n"
  "s_waitcnt lgkmcnt(4)\n"
  "v_mfma_f32_16x16x32_bf16 v[168:171], v[220:223], v[120:123], v[168:171]\n"
  "v_exp_f32 v197, v197\n"
  "ds_read_b128 v[208:211], v12 offset:8704\n"
  "v_mfma_f32_16x16x32_bf16 v[184:187], v[220:223], v[136:139], v[184:187]\n"
  "v_exp_f32 v198, v198\n"
  "ds_read_b128 v[212:215], v12 offset:8960\n"
  "s_waitcnt lgkmcnt(5)\n"
  "v_mfma_f32_16x16x32_bf16 v[168:171], v[224:227], v[124:127], v[168:171]\n"
  "v_exp_f32 v199, v199\n"
  "v_mfma_f32_16x16x32_bf16 v[184:187], v[224:227], v[140:143], v[184:187]\n"
  "v_add_f32 v18, v18, v196\n"
  "v_add_f32 v19, v19, v197\n"
  "s_waitcnt lgkmcnt(4)\n"
  "v_mfma_f32_16x16x32_bf16 v[168:171], v[228:231], v[128:131], v[168:171]\n"
  "v_cvt_pk_bf16_f32 v244, v196, v197\n"
  "v_cvt_pk_bf16_f32 v245, v198, v199\n"
  "ds_write_b64 v15, v[244:245] offset:0\n"
  "v_mfma_f32_16x16x32_bf16 v[184:187], v[228:231], v[144:147], v[184:187]\n"
  "v_add_f32 v18, v18, v198\n"
  "v_add_f32 v19, v19, v199\n"
  "s_setprio 0\n"
  "s_waitcnt lgkmcnt(4)\n"
  "v_mfma_f32_16x16x32_bf16 v[172:175], v[200:203], v[116:119], v[20:23]\n"
  "v_exp_f32 v164, v164\n"
  "ds_read_b128 v[216:219], v12 offset:12288\n"
  "v_mfma_f32_16x16x32_bf16 v[188:191], v[200:203], v[132:135], v[24:27]\n"
  "v_exp_f32 v165, v165\n"
  "ds_read_b128 v[220:223], v12 offset:12544\n"
  "s_waitcnt lgkmcnt(5)\n"
  "v_mfma_f32_16x16x32_bf16 v[172:175], v[204:207], v[120:123], v[172:175]\n"
  "v_exp_f32 v166, v166\n"
  "ds_read_b128 v[224:227], v12 offset:12800\n"
  "v_mfma_f32_16x16x32_bf16 v[188:191], v[204:207], v[136:139], v[188:191]\n"
  "v_exp_f32 v167, v167\n"
  "ds_read_b128 v[228:231], v12 offset:13056\n"
  "s_waitcnt lgkmcnt(6)\n"
  "v_mfma_f32_16x16x32_bf16 v[172:175], v[208:211], v[124:127], v[172:175]\n"
  "v_exp_f32 v180, v180\n"
  "v_mfma_f32_16x16x32_bf16 v[188:191], v[208:211], v[140:143], v[188:191]\n"
  "v_exp_f32 v181, v181\n"
  "s_waitcnt lgkmcnt(5)\n"
  "v_mfma_f32_16x16x32_bf16 v[172:175], v[212:215], v[128:131], v[172:175]\n"
  "v_exp_f32 v182, v182\n"
  "v_mfma_f32_16x16x32_bf16 v[188:191], v[212:215], v[144:147], v[188:191]\n"
  "v_exp_f32 v183, v183\n"
  "s_waitcnt vmcnt(8) lgkmcnt(0)\n"
  "s_barrier\n"
  "ds_read_b64_tr_b16 v[200:201], v13 offset:0\n"
  "ds_read_b64_tr_b16 v[202:203], v13 offset:4096\n"
  "s_setprio 3\n"
  "v_mfma_f32_16x16x32_bf16 v[176:179], v[216:219], v[116:119], v[20:23]\n"
  "v_exp_f32 v168, v168\n"
  "ds_read_b64_tr_b16 v[204:205], v14 offset:0\n"
  "ds_read_b64_tr_b16 v[206:207], v14 offset:4096\n"
  "s_add_u32 m0, s51, 0\n"
  "s_add_u32 s40, s50, 32768\n"
  "buffer_load_dwordx4 v10, %[srdk], s40 offen offset:0 nt lds\n"
  "v_mfma_f32_16x16x32_bf16 v[192:195], v[216:219], v[132:135], v[24:27]\n"
  "v_exp_f32 v169, v169\n"
  "ds_read_b64_tr_b16 v[208:209], v13 offset:256\n"
  "ds_read_b64_tr_b16 v[210:211], v13 offset:4352\n"
  "v_mfma_f32_16x16x32_bf16 v[176:179], v[220:223], v[120:123], v[176:179]\n"
  "v_exp_f32 v170, v170\n"
  "ds_read_b64_tr_b16 v[212:213], v14 offset:256\n"
  "ds_read_b64_tr_b16 v[214:215], v14 offset:4352\n"
  "buffer_load_dwordx4 v11, %[srdk], s40 offen offset:1024 nt lds\n"
  "v_mfma_f32_16x16x32_bf16 v[192:195], v[220:223], v[136:139], v[192:195]\n"
  "v_exp_f32 v171, v171\n"
  "ds_read_b64 v[232:233], v16 offset:0\n"
  "ds_read_b64 v[234:235], v16 offset:512\n"
  "v_mfma_f32_16x16x32_bf16 v[176:179], v[224:227], v[124:127], v[176:179]\n"
  "v_cvt_pk_bf16_f32 v164, v164, v165\n"
  "v_cvt_pk_bf16_f32 v165, v166, v167\n"
  "ds_read_b64 v[236:237], v16 offset:1024\n"
  "ds_read_b64 v[238:239], v16 offset:1536\n"
  "buffer_load_dwordx4 v10, %[srdk], s40 offen offset:2048 nt lds\n"
  "v_mfma_f32_16x16x32_bf16 v[192:195], v[224:227], v[140:143], v[192:195]\n"
  "v_cvt_pk_bf16_f32 v166, v168, v169\n"
  "v_cvt_pk_bf16_f32 v167, v170, v171\n"
  "v_mfma_f32_16x16x32_bf16 v[176:179], v[228:231], v[128:131], v[176:179]\n"
  "v_exp_f32 v184, v184\n"
  "buffer_load_dwordx4 v11, %[srdk], s40 offen offset:3072 nt lds\n"
  "v_mfma_f32_16x16x32_bf16 v[192:195], v[228:231], v[144:147], v[192:195]\n"
  "v_exp_f32 v185, v185\n"
  "s_waitcnt lgkmcnt(10)\n"
  "v_mfma_f32_16x16x32_bf16 v[44:47], v[200:203], v[164:167], v[44:47]\n"
  "v_exp_f32 v186, v186\n"
  "v_mfma_f32_16x16x32_bf16 v[36:39], v[32:35], v[164:167], v[36:39]\n"
  "v_exp_f32 v187, v187\n"
  "v_cvt_pk_bf16_f32 v180, v180, v181\n"
  "v_cvt_pk_bf16_f32 v181, v182, v183\n"
  "v_cvt_pk_bf16_f32 v182, v184, v185\n"
  "v_cvt_pk_bf16_f32 v183, v186, v187\n"
  "s_nop 1\n"
  "v_mfma_f32_16x16x32_bf16 v[76:79], v[200:203], v[180:183], v[76:79]\n"
  "v_exp_f32 v172, v172\n"
  "s_setprio 2\n"
  "v_mfma_f32_16x16x32_bf16 v[40:43], v[32:35], v[180:183], v[40:43]\n"
  "v_exp_f32 v173, v173\n"
  "s_waitcnt lgkmcnt(2)\n"
  "v_mfma_f32_16x16x32_bf16 v[108:111], v[200:203], v[232:235], v[108:111]\n"
  "v_exp_f32 v174, v174\n"
  "ds_read_b64_tr_b16 v[216:217], v13 offset:512\n"
  "ds_read_b64_tr_b16 v[218:219], v13 offset:4608\n"
  "v_mfma_f32_16x16x32_bf16 v[48:51], v[204:207], v[164:167], v[48:51]\n"
  "v_exp_f32 v175, v175\n"
  "v_mfma_f32_16x16x32_bf16 v[80:83], v[204:207], v[180:183], v[80:83]\n"
  "v_exp_f32 v188, v188\n"
  "v_mfma_f32_16x16x32_bf16 v[112:115], v[204:207], v[232:235], v[112:115]\n"
  "v_exp_f32 v189, v189\n"
  "ds_read_b64_tr_b16 v[220:221], v14 offset:512\n"
  "ds_read_b64_tr_b16 v[222:223], v14 offset:4608\n"
  "v_mfma_f32_16x16x32_bf16 v[52:55], v[208:211], v[164:167], v[52:55]\n"
  "v_exp_f32 v190, v190\n"
  "v_mfma_f32_16x16x32_bf16 v[84:87], v[208:211], v[180:183], v[84:87]\n"
  "v_exp_f32 v191, v191\n"
  "ds_read_b64_tr_b16 v[224:225], v13 offset:768\n"
  "ds_read_b64_tr_b16 v[226:227], v13 offset:4864\n"
  "v_mfma_f32_16x16x32_bf16 v[56:59], v[212:215], v[164:167], v[56:59]\n"
  "v_exp_f32 v176, v176\n"
  "v_mfma_f32_16x16x32_bf16 v[88:91], v[212:215], v[180:183], v[88:91]\n"
  "v_exp_f32 v177, v177\n"
  "ds_read_b64_tr_b16 v[228:229], v14 offset:768\n"
  "ds_read_b64_tr_b16 v[230:231], v14 offset:4864\n"
  "s_waitcnt lgkmcnt(6)\n"
  "v_mfma_f32_16x16x32_bf16 v[60:63], v[216:219], v[164:167], v[60:63]\n"
  "v_exp_f32 v178, v178\n"
  "v_mfma_f32_16x16x32_bf16 v[92:95], v[216:219], v[180:183], v[92:95]\n"
  "v_exp_f32 v179, v179\n"
  "ds_read_b64_tr_b16 v[200:201], v13 offset:8192\n"
  "ds_read_b64_tr_b16 v[202:203], v13 offset:12288\n"
  "s_setprio 1\n"
  "s_waitcnt lgkmcnt(6)\n"
  "v_mfma_f32_16x16x32_bf16 v[64:67], v[220:223], v[164:167], v[64:67]\n"
  "v_cvt_pk_bf16_f32 v172, v172, v173\n"
  "v_cvt_pk_bf16_f32 v173, v174, v175\n"
  "v_mfma_f32_16x16x32_bf16 v[96:99], v[220:223], v[180:183], v[96:99]\n"
  "v_cvt_pk_bf16_f32 v174, v176, v177\n"
  "v_cvt_pk_bf16_f32 v175, v178, v179\n"
  "ds_read_b64_tr_b16 v[204:205], v14 offset:8192\n"
  "ds_read_b64_tr_b16 v[206:207], v14 offset:12288\n"
  "s_waitcnt lgkmcnt(6)\n"
  "v_mfma_f32_16x16x32_bf16 v[68:71], v[224:227], v[164:167], v[68:71]\n"
  "v_exp_f32 v192, v192\n"
  "v_mfma_f32_16x16x32_bf16 v[100:103], v[224:227], v[180:183], v[100:103]\n"
  "v_exp_f32 v193, v193\n"
  "ds_read_b64_tr_b16 v[208:209], v13 offset:8448\n"
  "ds_read_b64_tr_b16 v[210:211], v13 offset:12544\n"
  "s_waitcnt lgkmcnt(6)\n"
  "v_mfma_f32_16x16x32_bf16 v[72:75], v[228:231], v[164:167], v[72:75]\n"
  "v_exp_f32 v194, v194\n"
  "v_mfma_f32_16x16x32_bf16 v[104:107], v[228:231], v[180:183], v[104:107]\n"
  "v_exp_f32 v195, v195\n"
  "ds_read_b64_tr_b16 v[212:213], v14 offset:8448\n"
  "ds_read_b64_tr_b16 v[214:215], v14 offset:12544\n"
  "s_waitcnt lgkmcnt(6)\n"
  "v_mfma_f32_16x16x32_bf16 v[44:47], v[200:203], v[172:175], v[44:47]\n"
  "v_cvt_pk_bf16_f32 v188, v188, v189\n"
  "v_cvt_pk_bf16_f32 v189, v190, v191\n"
  "v_mfma_f32_16x16x32_bf16 v[36:39], v[32:35], v[172:175], v[36:39]\n"
  "v_cvt_pk_bf16_f32 v190, v192, v193\n"
  "v_cvt_pk_bf16_f32 v191, v194, v195\n"
  "s_nop 1\n"
  "v_mfma_f32_16x16x32_bf16 v[76:79], v[200:203], v[188:191], v[76:79]\n"
  "v_mfma_f32_16x16x32_bf16 v[40:43], v[32:35], v[188:191], v[40:43]\n"
  "v_mfma_f32_16x16x32_bf16 v[108:111], v[200:203], v[236:239], v[108:111]\n"
  "ds_read_b64_tr_b16 v[216:217], v13 offset:8704\n"
  "ds_read_b64_tr_b16 v[218:219], v13 offset:12800\n"
  "s_setprio 0\n"
  "s_waitcnt lgkmcnt(6)\n"
  "v_mfma_f32_16x16x32_bf16 v[48:51], v[204:207], v[172:175], v[48:51]\n"
  "v_mfma_f32_16x16x32_bf16 v[80:83], v[204:207], v[188:191], v[80:83]\n"
  "v_mfma_f32_16x16x32_bf16 v[112:115], v[204:207], v[236:239], v[112:115]\n"
  "ds_read_b64_tr_b16 v[220:221], v14 offset:8704\n"
  "ds_read_b64_tr_b16 v[222:223], v14 offset:12800\n"
  "s_waitcnt lgkmcnt(6)\n"
  "v_mfma_f32_16x16x32_bf16 v[52:55], v[208:211], v[172:175], v[52:55]\n"
  "v_mfma_f32_16x16x32_bf16 v[84:87], v[208:211], v[188:191], v[84:87]\n"
  "ds_read_b64_tr_b16 v[224:225], v13 offset:8960\n"
  "ds_read_b64_tr_b16 v[226:227], v13 offset:13056\n"
  "s_waitcnt lgkmcnt(6)\n"
  "v_mfma_f32_16x16x32_bf16 v[56:59], v[212:215], v[172:175], v[56:59]\n"
  "v_mfma_f32_16x16x32_bf16 v[88:91], v[212:215], v[188:191], v[88:91]\n"
  "ds_read_b64_tr_b16 v[228:229], v14 offset:8960\n"
  "ds_read_b64_tr_b16 v[230:231], v14 offset:13056\n"
  "s_waitcnt lgkmcnt(6)\n"
  "v_mfma_f32_16x16x32_bf16 v[60:63], v[216:219], v[172:175], v[60:63]\n"
  "v_mfma_f32_16x16x32_bf16 v[92:95], v[216:219], v[188:191], v[92:95]\n"
  "s_waitcnt lgkmcnt(4)\n"
  "v_mfma_f32_16x16x32_bf16 v[64:67], v[220:223], v[172:175], v[64:67]\n"
  "v_mfma_f32_16x16x32_bf16 v[96:99], v[220:223], v[188:191], v[96:99]\n"
  "s_setprio 3\n"
  "s_waitcnt vmcnt(8) lgkmcnt(0)\n"
  "s_barrier\n"
  "ds_read_b128 v[200:203], v240 offset:0\n"
  "v_mfma_f32_16x16x32_bf16 v[68:71], v[224:227], v[172:175], v[68:71]\n"
  "ds_read_b128 v[204:207], v240 offset:256\n"
  "s_add_u32 m0, s51, 16384\n"
  "s_add_u32 s40, s50, 32768\n"
  "buffer_load_dwordx4 v10, %[srdv], s40 offen offset:0 nt lds\n"
  "v_mfma_f32_16x16x32_bf16 v[100:103], v[224:227], v[188:191], v[100:103]\n"
  "ds_read_b128 v[208:211], v240 offset:512\n"
  "buffer_load_dwordx4 v11, %[srdv], s40 offen offset:1024 nt lds\n"
  "v_mfma_f32_16x16x32_bf16 v[72:75], v[228:231], v[172:175], v[72:75]\n"
  "ds_read_b128 v[212:215], v240 offset:768\n"
  "buffer_load_dwordx4 v10, %[srdv], s40 offen offset:2048 nt lds\n"
  "v_mfma_f32_16x16x32_bf16 v[104:107], v[228:231], v[188:191], v[104:107]\n"
  "buffer_load_dwordx4 v11, %[srdv], s40 offen offset:3072 nt lds\n"
  "s_waitcnt lgkmcnt(3)\n"
  "v_mfma_f32_16x16x32_bf16 v[196:199], v[200:203], v[148:151], v[28:31]\n"
  "ds_read_b128 v[216:219], v240 offset:4096\n"
  "v_mfma_f32_16x16x32_bf16 v[164:167], v[200:203], v[116:119], v[20:23]\n"
  "v_mfma_f32_16x16x32_bf16 v[180:183], v[200:203], v[132:135], v[24:27]\n"
  "ds_read_b128 v[220:223], v240 offset:4352\n"
  "s_waitcnt lgkmcnt(4)\n"
  "v_mfma_f32_16x16x32_bf16 v[196:199], v[204:207], v[152:155], v[196:199]\n"
  "s_setprio 2\n"
  "v_mfma_f32_16x16x32_bf16 v[164:167], v[204:207], v[120:123], v[164:167]\n"
  "ds_read_b128 v[224:227], v240 offset:4608\n"
  "v_mfma_f32_16x16x32_bf16 v[180:183], v[204:207], v[136:139], v[180:183]\n"
  "s_waitcnt lgkmcnt(4)\n"
  "v_mfma_f32_16x16x32_bf16 v[196:199], v[208:211], v[156:159], v[196:199]\n"
  "ds_read_b128 v[228:231], v240 offset:4864\n"
  "v_mfma_f32_16x16x32_bf16 v[164:167], v[208:211], v[124:127], v[164:167]\n"
  "v_mfma_f32_16x16x32_bf16 v[180:183], v[208:211], v[140:143], v[180:183]\n"
  "s_waitcnt lgkmcnt(4)\n"
  "v_mfma_f32_16x16x32_bf16 v[196:199], v[212:215], v[160:163], v[196:199]\n"
  "v_mfma_f32_16x16x32_bf16 v[164:167], v[212:215], v[128:131], v[164:167]\n"
  "v_mfma_f32_16x16x32_bf16 v[180:183], v[212:215], v[144:147], v[180:183]\n"
  "s_setprio 1\n"
  "s_waitcnt lgkmcnt(3)\n"
  "v_mfma_f32_16x16x32_bf16 v[168:171], v[216:219], v[116:119], v[20:23]\n"
  "ds_read_b128 v[200:203], v240 offset:8192\n"
  "v_mfma_f32_16x16x32_bf16 v[184:187], v[216:219], v[132:135], v[24:27]\n"
  "s_nop 0\n"
  "v_exp_f32 v196, v196\n"
  "ds_read_b128 v[204:207], v240 offset:8448\n"
  "s_waitcnt lgkmcnt(4)\n"
  "v_mfma_f32_16x16x32_bf16 v[168:171], v[220:223], v[120:123], v[168:171]\n"
  "v_exp_f32 v197, v197\n"
  "ds_read_b128 v[208:211], v240 offset:8704\n"
  "v_mfma_f32_16x16x32_bf16 v[184:187], v[220:223], v[136:139], v[184:187]\n"
  "v_exp_f32 v198, v198\n"
  "ds_read_b128 v[212:215], v240 offset:8960\n"
  "s_waitcnt lgkmcnt(5)\n"
  "v_mfma_f32_16x16x32_bf16 v[168:171], v[224:227], v[124:127], v[168:171]\n"
  "v_exp_f32 v199, v199\n"
  "v_mfma_f32_16x16x32_bf16 v[184:187], v[224:227], v[140:143], v[184:187]\n"
  "v_add_f32 v18, v18, v196\n"
  "v_add_f32 v19, v19, v197\n"
  "s_waitcnt lgkmcnt(4)\n"
  "v_mfma_f32_16x16x32_bf16 v[168:171], v[228:231], v[128:131], v[168:171]\n"
  "v_cvt_pk_bf16_f32 v244, v196, v197\n"
  "v_cvt_pk_bf16_f32 v245, v198, v199\n"
  "ds_write_b64 v15, v[244:245] offset:0\n"
  "v_mfma_f32_16x16x32_bf16 v[184:187], v[228:231], v[144:147], v[184:187]\n"
  "v_add_f32 v18, v18, v198\n"
  "v_add_f32 v19, v19, v199\n"
  "s_setprio 0\n"
  "s_waitcnt lgkmcnt(4)\n"
  "v_mfma_f32_16x16x32_bf16 v[172:175], v[200:203], v[116:119], v[20:23]\n"
  "v_exp_f32 v164, v164\n"
  "ds_read_b128 v[216:219], v240 offset:12288\n"
  "v_mfma_f32_16x16x32_bf16 v[188:191], v[200:203], v[132:135], v[24:27]\n"
  "v_exp_f32 v165, v165\n"
  "ds_read_b128 v[220:223], v240 offset:12544\n"
  "s_waitcnt lgkmcnt(5)\n"
  "v_mfma_f32_16x16x32_bf16 v[172:175], v[204:207], v[120:123], v[172:175]\n"
  "v_exp_f32 v166, v166\n"
  "ds_read_b128 v[224:227], v240 offset:12800\n"
  "v_mfma_f32_16x16x32_bf16 v[188:191], v[204:207], v[136:139], v[188:191]\n"
  "v_exp_f32 v167, v167\n"
  "ds_read_b128 v[228:231], v240 offset:13056\n"
  "s_waitcnt lgkmcnt(6)\n"
  "v_mfma_f32_16x16x32_bf16 v[172:175], v[208:211], v[124:127], v[172:175]\n"
  "v_exp_f32 v180, v180\n"
  "v_mfma_f32_16x16x32_bf16 v[188:191], v[208:211], v[140:143], v[188:191]\n"
  "v_exp_f32 v181, v181\n"
  "s_waitcnt lgkmcnt(5)\n"
  "v_mfma_f32_16x16x32_bf16 v[172:175], v[212:215], v[128:131], v[172:175]\n"
  "v_exp_f32 v182, v182\n"
  "v_mfma_f32_16x16x32_bf16 v[188:191], v[212:215], v[144:147], v[188:191]\n"
  "v_exp_f32 v183, v183\n"
  "s_waitcnt vmcnt(8) lgkmcnt(0)\n"
  "s_barrier\n"
  "ds_read_b64_tr_b16 v[200:201], v241 offset:0\n"
  "ds_read_b64_tr_b16 v[202:203], v241 offset:4096\n"
  "s_setprio 3\n"
  "v_mfma_f32_16x16x32_bf16 v[176:179], v[216:219], v[116:119], v[20:23]\n"
  "v_exp_f32 v168, v168\n"
  "ds_read_b64_tr_b16 v[204:205], v242 offset:0\n"
  "ds_read_b64_tr_b16 v[206:207], v242 offset:4096\n"
  "s_add_u32 m0, s51, 65536\n"
  "s_add_u32 s40, s50, 49152\n"
  "buffer_load_dwordx4 v10, %[srdk], s40 offen offset:0 nt lds\n"
  "v_mfma_f32_16x16x32_bf16 v[192:195], v[216:219], v[132:135], v[24:27]\n"
  "v_exp_f32 v169, v169\n"
  "ds_read_b64_tr_b16 v[208:209], v241 offset:256\n"
  "ds_read_b64_tr_b16 v[210:211], v241 offset:4352\n"
  "v_mfma_f32_16x16x32_bf16 v[176:179], v[220:223], v[120:123], v[176:179]\n"
  "v_exp_f32 v170, v170\n"
  "ds_read_b64_tr_b16 v[212:213], v242 offset:256\n"
  "ds_read_b64_tr_b16 v[214:215], v242 offset:4352\n"
  "buffer_load_dwordx4 v11, %[srdk], s40 offen offset:1024 nt lds\n"
  "v_mfma_f32_16x16x32_bf16 v[192:195], v[220:223], v[136:139], v[192:195]\n"
  "v_exp_f32 v171, v171\n"
  "ds_read_b64 v[232:233], v16 offset:0\n"
  "ds_read_b64 v[234:235], v16 offset:512\n"
  "v_mfma_f32_16x16x32_bf16 v[176:179], v[224:227], v[124:127], v[176:179]\n"
  "v_cvt_pk_bf16_f32 v164, v164, v165\n"
  "v_cvt_pk_bf16_f32 v165, v166, v167\n"
  "ds_read_b64 v[236:237], v16 offset:1024\n"
  "ds_read_b64 v[238:239], v16 offset:1536\n"
  "buffer_load_dwordx4 v10, %[srdk], s40 offen offset:2048 nt lds\n"
  "v_mfma_f32_16x16x32_bf16 v[192:195], v[224:227], v[140:143], v[192:195]\n"
  "v_cvt_pk_bf16_f32 v166, v168, v169\n"
  "v_cvt_pk_bf16_f32 v167, v170, v171\n"
  "v_mfma_f32_16x16x32_bf16 v[176:179], v[228:231], v[128:131], v[176:179]\n"
  "v_exp_f32 v184, v184\n"
  "buffer_load_dwordx4 v11, %[srdk], s40 offen offset:3072 nt lds\n"
  "v_mfma_f32_16x16x32_bf16 v[192:195], v[228:231], v[144:147], v[192:195]\n"
  "v_exp_f32 v185, v185\n"
  "s_waitcnt lgkmcnt(10)\n"
  "v_mfma_f32_16x16x32_bf16 v[44:47], v[200:203], v[164:167], v[44:47]\n"
  "v_exp_f32 v186, v186\n"
  "v_mfma_f32_16x16x32_bf16 v[36:39], v[32:35], v[164:167], v[36:39]\n"
  "v_exp_f32 v187, v187\n"
  "v_cvt_pk_bf16_f32 v180, v180, v181\n"
  "v_cvt_pk_bf16_f32 v181, v182, v183\n"
  "v_cvt_pk_bf16_f32 v182, v184, v185\n"
  "v_cvt_pk_bf16_f32 v183, v186, v187\n"
  "s_nop 1\n"
  "v_mfma_f32_16x16x32_bf16 v[76:79], v[200:203], v[180:183], v[76:79]\n"
  "v_exp_f32 v172, v172\n"
  "s_setprio 2\n"
  "v_mfma_f32_16x16x32_bf16 v[40:43], v[32:35], v[180:183], v[40:43]\n"
  "v_exp_f32 v173, v173\n"
  "s_waitcnt lgkmcnt(2)\n"
  "v_mfma_f32_16x16x32_bf16 v[108:111], v[200:203], v[232:235], v[108:111]\n"
  "v_exp_f32 v174, v174\n"
  "ds_read_b64_tr_b16 v[216:217], v241 offset:512\n"
  "ds_read_b64_tr_b16 v[218:219], v241 offset:4608\n"
  "v_mfma_f32_16x16x32_bf16 v[48:51], v[204:207], v[164:167], v[48:51]\n"
  "v_exp_f32 v175, v175\n"
  "v_mfma_f32_16x16x32_bf16 v[80:83], v[204:207], v[180:183], v[80:83]\n"
  "v_exp_f32 v188, v188\n"
  "v_mfma_f32_16x16x32_bf16 v[112:115], v[204:207], v[232:235], v[112:115]\n"
  "v_exp_f32 v189, v189\n"
  "ds_read_b64_tr_b16 v[220:221], v242 offset:512\n"
  "ds_read_b64_tr_b16 v[222:223], v242 offset:4608\n"
  "v_mfma_f32_16x16x32_bf16 v[52:55], v[208:211], v[164:167], v[52:55]\n"
  "v_exp_f32 v190, v190\n"
  "v_mfma_f32_16x16x32_bf16 v[84:87], v[208:211], v[180:183], v[84:87]\n"
  "v_exp_f32 v191, v191\n"
  "ds_read_b64_tr_b16 v[224:225], v241 offset:768\n"
  "ds_read_b64_tr_b16 v[226:227], v241 offset:4864\n"
  "v_mfma_f32_16x16x32_bf16 v[56:59], v[212:215], v[164:167], v[56:59]\n"
  "v_exp_f32 v176, v176\n"
  "v_mfma_f32_16x16x32_bf16 v[88:91], v[212:215], v[180:183], v[88:91]\n"
  "v_exp_f32 v177, v177\n"
  "ds_read_b64_tr_b16 v[228:229], v242 offset:768\n"
  "ds_read_b64_tr_b16 v[230:231], v242 offset:4864\n"
  "s_waitcnt lgkmcnt(6)\n"
  "v_mfma_f32_16x16x32_bf16 v[60:63], v[216:219], v[164:167], v[60:63]\n"
  "v_exp_f32 v178, v178\n"
  "v_mfma_f32_16x16x32_bf16 v[92:95], v[216:219], v[180:183], v[92:95]\n"
  "v_exp_f32 v179, v179\n"
  "ds_read_b64_tr_b16 v[200:201], v241 offset:8192\n"
  "ds_read_b64_tr_b16 v[202:203], v241 offset:12288\n"
  "s_setprio 1\n"
  "s_waitcnt lgkmcnt(6)\n"
  "v_mfma_f32_16x16x32_bf16 v[64:67], v[220:223], v[164:167], v[64:67]\n"
  "v_cvt_pk_bf16_f32 v172, v172, v173\n"
  "v_cvt_pk_bf16_f32 v173, v174, v175\n"
  "v_mfma_f32_16x16x32_bf16 v[96:99], v[220:223], v[180:183], v[96:99]\n"
  "v_cvt_pk_bf16_f32 v174, v176, v177\n"
  "v_cvt_pk_bf16_f32 v175, v178, v179\n"
  "ds_read_b64_tr_b16 v[204:205], v242 offset:8192\n"
  "ds_read_b64_tr_b16 v[206:207], v242 offset:12288\n"
  "s_waitcnt lgkmcnt(6)\n"
  "v_mfma_f32_16x16x32_bf16 v[68:71], v[224:227], v[164:167], v[68:71]\n"
  "v_exp_f32 v192, v192\n"
  "v_mfma_f32_16x16x32_bf16 v[100:103], v[224:227], v[180:183], v[100:103]\n"
  "v_exp_f32 v193, v193\n"
  "ds_read_b64_tr_b16 v[208:209], v241 offset:8448\n"
  "ds_read_b64_tr_b16 v[210:211], v241 offset:12544\n"
  "s_waitcnt lgkmcnt(6)\n"
  "v_mfma_f32_16x16x32_bf16 v[72:75], v[228:231], v[164:167], v[72:75]\n"
  "v_exp_f32 v194, v194\n"
  "v_mfma_f32_16x16x32_bf16 v[104:107], v[228:231], v[180:183], v[104:107]\n"
  "v_exp_f32 v195, v195\n"
  "ds_read_b64_tr_b16 v[212:213], v242 offset:8448\n"
  "ds_read_b64_tr_b16 v[214:215], v242 offset:12544\n"
  "s_waitcnt lgkmcnt(6)\n"
  "v_mfma_f32_16x16x32_bf16 v[44:47], v[200:203], v[172:175], v[44:47]\n"
  "v_cvt_pk_bf16_f32 v188, v188, v189\n"
  "v_cvt_pk_bf16_f32 v189, v190, v191\n"
  "v_mfma_f32_16x16x32_bf16 v[36:39], v[32:35], v[172:175], v[36:39]\n"
  "v_cvt_pk_bf16_f32 v190, v192, v193\n"
  "v_cvt_pk_bf16_f32 v191, v194, v195\n"
  "s_nop 1\n"
  "v_mfma_f32_16x16x32_bf16 v[76:79], v[200:203], v[188:191], v[76:79]\n"
  "v_mfma_f32_16x16x32_bf16 v[40:43], v[32:35], v[188:191], v[40:43]\n"
  "v_mfma_f32_16x16x32_bf16 v[108:111], v[200:203], v[236:239], v[108:111]\n"
  "ds_read_b64_tr_b16 v[216:217], v241 offset:8704\n"
  "ds_read_b64_tr_b16 v[218:219], v241 offset:12800\n"
  "s_setprio 0\n"
  "s_waitcnt lgkmcnt(6)\n"
  "v_mfma_f32_16x16x32_bf16 v[48:51], v[204:207], v[172:175], v[48:51]\n"
  "v_mfma_f32_16x16x32_bf16 v[80:83], v[204:207], v[188:191], v[80:83]\n"
  "v_mfma_f32_16x16x32_bf16 v[112:115], v[204:207], v[236:239], v[112:115]\n"
  "ds_read_b64_tr_b16 v[220:221], v242 offset:8704\n"
  "ds_read_b64_tr_b16 v[222:223], v242 offset:12800\n"
  "s_waitcnt lgkmcnt(6)\n"
  "v_mfma_f32_16x16x32_bf16 v[52:55], v[208:211], v[172:175], v[52:55]\n"
  "v_mfma_f32_16x16x32_bf16 v[84:87], v[208:211], v[188:191], v[84:87]\n"
  "ds_read_b64_tr_b16 v[224:225], v241 offset:8960\n"
  "ds_read_b64_tr_b16 v[226:227], v241 offset:13056\n"
  "s_waitcnt lgkmcnt(6)\n"
  "v_mfma_f32_16x16x32_bf16 v[56:59], v[212:215], v[172:175], v[56:59]\n"
  "v_mfma_f32_16x16x32_bf16 v[88:91], v[212:215], v[188:191], v[88:91]\n"
  "ds_read_b64_tr_b16 v[228:229], v242 offset:8960\n"
  "ds_read_b64_tr_b16 v[230:231], v242 offset:13056\n"
  "s_waitcnt lgkmcnt(6)\n"
  "v_mfma_f32_16x16x32_bf16 v[60:63], v[216:219], v[172:175], v[60:63]\n"
  "v_mfma_f32_16x16x32_bf16 v[92:95], v[216:219], v[188:191], v[92:95]\n"
  "s_waitcnt lgkmcnt(4)\n"
  "v_mfma_f32_16x16x32_bf16 v[64:67], v[220:223], v[172:175], v[64:67]\n"
  "v_mfma_f32_16x16x32_bf16 v[96:99], v[220:223], v[188:191], v[96:99]\n"
  "s_setprio 3\n"
  "s_waitcnt vmcnt(8) lgkmcnt(0)\n"
  "s_barrier\n"
  "ds_read_b128 v[200:203], v12 offset:0\n"
  "v_mfma_f32_16x16x32_bf16 v[68:71], v[224:227], v[172:175], v[68:71]\n"
  "ds_read_b128 v[204:207], v12 offset:256\n"
  "s_add_u32 m0, s51, 81920\n"
  "s_add_u32 s40, s50, 49152\n"
  "buffer_load_dwordx4 v10, %[srdv], s40 offen offset:0 nt lds\n"
  "v_mfma_f32_16x16x32_bf16 v[100:103], v[224:227], v[188:191], v[100:103]\n"
  "ds_read_b128 v[208:211], v12 offset:512\n"
  "buffer_load_dwordx4 v11, %[srdv], s40 offen offset:1024 nt lds\n"
  "v_mfma_f32_16x16x32_bf16 v[72:75], v[228:231], v[172:175], v[72:75]\n"
  "ds_read_b128 v[212:215], v12 offset:768\n"
  "buffer_load_dwordx4 v10, %[srdv], s40 offen offset:2048 nt lds\n"
  "v_mfma_f32_16x16x32_bf16 v[104:107], v[228:231], v[188:191], v[104:107]\n"
  "buffer_load_dwordx4 v11, %[srdv], s40 offen offset:3072 nt lds\n"
  "s_add_u32 s50, s50, 32768\n"
  "s_sub_u32 s41, s41, 1\n"
  "s_cmp_lg_u32 s41, 0\n"
  "s_cbranch_scc1 P1_LOOP0\n"
  "v_lshrrev_b32 v3, 2, v1\n"
  "v_mul_u32_u24 v5, 1280, v3\n"
  "v_bfe_u32 v3, v1, 4, 1\n"
  "v_lshlrev_b32 v3, 1, v3\n"
  "v_and_b32 v7, 3, v1\n"
  "v_xor_b32 v3, v3, v7\n"
  "v_lshl_add_u32 v252, v3, 4, v5\n"
  "s_lshl_b32 s38, %[wave], 1\n"
  "s_and_b32 s39, s38, 3\n"
  "s_mul_i32 s39, s39, 20480\n"
  "s_lshr_b32 s40, s38, 2\n"
  "s_lshl_b32 s40, s40, 6\n"
  "s_add_u32 s52, s39, s40\n"
  "s_add_u32 s38, s38, 1\n"
  "s_and_b32 s39, s38, 3\n"
  "s_mul_i32 s39, s39, 20480\n"
  "s_lshr_b32 s40, s38, 2\n"
  "s_lshl_b32 s40, s40, 6\n"
  "s_add_u32 s53, s39, s40\n"
  "s_lshr_b32 s38, %[wave], 1\n"
  "s_add_u32 s38, s38, 16\n"
  "s_and_b32 s39, s38, 3\n"
  "s_mul_i32 s39, s39, 20480\n"
  "s_lshr_b32 s40, s38, 2\n"
  "s_lshl_b32 s40, s40, 6\n"
  "s_add_u32 s54, s39, s40\n"
  "s_lshl_b32 s44, s38, 10\n"
  "s_add_u32 s44, s44, %[ldsb]\n"
  "s_lshl_b32 s43, %[wave], 11\n"
  "s_add_u32 s43, s43, %[ldsb]\n"
  "s_and_b32 s38, %[wave], 1\n"
  "s_cmp_eq_u32 s38, 0\n"
  "s_cselect_b32 s48, -1, 0\n"
  "s_cselect_b32 s49, 0, -1\n"
  "s_mul_i32 s42, %[jx], 327680\n"
  "s_waitcnt lgkmcnt(3)\n"
  "v_mfma_f32_16x16x32_bf16 v[196:199], v[200:203], v[148:151], v[28:31]\n"
  "ds_read_b128 v[216:219], v12 offset:4096\n"
  "v_mfma_f32_16x16x32_bf16 v[164:167], v[200:203], v[116:119], v[20:23]\n"
  "v_mfma_f32_16x16x32_bf16 v[180:183], v[200:203], v[132:135], v[24:27]\n"
  "ds_read_b128 v[220:223], v12 offset:4352\n"
  "s_waitcnt lgkmcnt(4)\n"
  "v_mfma_f32_16x16x32_bf16 v[196:199], v[204:207], v[152:155], v[196:199]\n"
  "s_setprio 2\n"
  "v_mfma_f32_16x16x32_bf16 v[164:167], v[204:207], v[120:123], v[164:167]\n"
  "ds_read_b128 v[224:227], v12 offset:4608\n"
  "v_mfma_f32_16x16x32_bf16 v[180:183], v[204:207], v[136:139], v[180:183]\n"
  "s_waitcnt lgkmcnt(4)\n"
  "v_mfma_f32_16x16x32_bf16 v[196:199], v[208:211], v[156:159], v[196:199]\n"
  "ds_read_b128 v[228:231], v12 offset:4864\n"
  "v_mfma_f32_16x16x32_bf16 v[164:167], v[208:211], v[124:127], v[164:167]\n"
  "v_mfma_f32_16x16x32_bf16 v[180:183], v[208:211], v[140:143], v[180:183]\n"
  "s_waitcnt lgkmcnt(4)\n"
  "v_mfma_f32_16x16x32_bf16 v[196:199], v[212:215], v[160:163], v[196:199]\n"
  "v_mfma_f32_16x16x32_bf16 v[164:167], v[212:215], v[128:131], v[164:167]\n"
  "v_mfma_f32_16x16x32_bf16 v[180:183], v[212:215], v[144:147], v[180:183]\n"
  "s_setprio 1\n"
  "s_waitcnt lgkmcnt(3)\n"
  "v_mfma_f32_16x16x32_bf16 v[168:171], v[216:219], v[116:119], v[20:23]\n"
  "ds_read_b128 v[200:203], v12 offset:8192\n"
  "v_mfma_f32_16x16x32_bf16 v[184:187], v[216:219], v[132:135], v[24:27]\n"
  "s_nop 0\n"
  "v_exp_f32 v196, v196\n"
  "ds_read_b128 v[204:207], v12 offset:8448\n"
  "s_waitcnt lgkmcnt(4)\n"
  "v_mfma_f32_16x16x32_bf16 v[168:171], v[220:223], v[120:123], v[168:171]\n"
  "v_exp_f32 v197, v197\n"
  "ds_read_b128 v[208:211], v12 offset:8704\n"
  "v_mfma_f32_16x16x32_bf16 v[184:187], v[220:223], v[136:139], v[184:187]\n"
  "v_exp_f32 v198, v198\n"
  "ds_read_b128 v[212:215], v12 offset:8960\n"
  "s_waitcnt lgkmcnt(5)\n"
  "v_mfma_f32_16x16x32_bf16 v[168:171], v[224:227], v[124:127], v[168:171]\n"
  "v_exp_f32 v199, v199\n"
  "v_mfma_f32_16x16x32_bf16 v[184:187], v[224:227], v[140:143], v[184:187]\n"
  "v_add_f32 v18, v18, v196\n"
  "v_add_f32 v19, v19, v197\n"
  "s_waitcnt lgkmcnt(4)\n"
  "v_mfma_f32_16x16x32_bf16 v[168:171], v[228:231], v[128:131], v[168:171]\n"
  "v_cvt_pk_bf16_f32 v244, v196, v197\n"
  "v_cvt_pk_bf16_f32 v245, v198, v199\n"
  "ds_write_b64 v15, v[244:245] offset:0\n"
  "v_mfma_f32_16x16x32_bf16 v[184:187], v[228:231], v[144:147], v[184:187]\n"
  "v_add_f32 v18, v18, v198\n"
  "v_add_f32 v19, v19, v199\n"
  "s_setprio 0\n"
  "s_waitcnt lgkmcnt(4)\n"
  "v_mfma_f32_16x16x32_bf16 v[172:175], v[200:203], v[116:119], v[20:23]\n"
  "v_exp_f32 v164, v164\n"
  "ds_read_b128 v[216:219], v12 offset:12288\n"
  "v_mfma_f32_16x16x32_bf16 v[188:191], v[200:203], v[132:135], v[24:27]\n"
  "v_exp_f32 v165, v165\n"
  "ds_read_b128 v[220:223], v12 offset:12544\n"
  "s_waitcnt lgkmcnt(5)\n"
  "v_mfma_f32_16x16x32_bf16 v[172:175], v[204:207], v[120:123], v[172:175]\n"
  "v_exp_f32 v166, v166\n"
  "ds_read_b128 v[224:227], v12 offset:12800\n"
  "v_mfma_f32_16x16x32_bf16 v[188:191], v[204:207], v[136:139], v[188:191]\n"
  "v_exp_f32 v167, v167\n"
  "ds_read_b128 v[228:231], v12 offset:13056\n"
  "s_waitcnt lgkmcnt(6)\n"
  "v_mfma_f32_16x16x32_bf16 v[172:175], v[208:211], v[124:127], v[172:175]\n"
  "v_exp_f32 v180, v180\n"
  "v_mfma_f32_16x16x32_bf16 v[188:191], v[208:211], v[140:143], v[188:191]\n"
  "v_exp_f32 v181, v181\n"
  "s_waitcnt lgkmcnt(5)\n"
  "v_mfma_f32_16x16x32_bf16 v[172:175], v[212:215], v[128:131], v[172:175]\n"
  "v_exp_f32 v182, v182\n"
  "v_mfma_f32_16x16x32_bf16 v[188:191], v[212:215], v[144:147], v[188:191]\n"
  "v_exp_f32 v183, v183\n"
  "s_waitcnt vmcnt(8) lgkmcnt(0)\n"
  "s_barrier\n"
  "ds_read_b64_tr_b16 v[200:201], v13 offset:0\n"
  "ds_read_b64_tr_b16 v[202:203], v13 offset:4096\n"
  "s_setprio 3\n"
  "v_mfma_f32_16x16x32_bf16 v[176:179], v[216:219], v[116:119], v[20:23]\n"
  "v_exp_f32 v168, v168\n"
  "ds_read_b64_tr_b16 v[204:205], v14 offset:0\n"
  "ds_read_b64_tr_b16 v[206:207], v14 offset:4096\n"
  "v_mfma_f32_16x16x32_bf16 v[192:195], v[216:219], v[132:135], v[24:27]\n"
  "v_exp_f32 v169, v169\n"
  "ds_read_b64_tr_b16 v[208:209], v13 offset:256\n"
  "ds_read_b64_tr_b16 v[210:211], v13 offset:4352\n"
  "v_mfma_f32_16x16x32_bf16 v[176:179], v[220:223], v[120:123], v[176:179]\n"
  "v_exp_f32 v170, v170\n"
  "ds_read_b64_tr_b16 v[212:213], v14 offset:256\n"
  "ds_read_b64_tr_b16 v[214:215], v14 offset:4352\n"
  "v_mfma_f32_16x16x32_bf16 v[192:195], v[220:223], v[136:139], v[192:195]\n"
  "v_exp_f32 v171, v171\n"
  "ds_read_b64 v[232:233], v16 offset:0\n"
  "ds_read_b64 v[234:235], v16 offset:512\n"
  "v_mfma_f32_16x16x32_bf16 v[176:179], v[224:227], v[124:127], v[176:179]\n"
  "v_cvt_pk_bf16_f32 v164, v164, v165\n"
  "v_cvt_pk_bf16_f32 v165, v166, v167\n"
  "ds_read_b64 v[236:237], v16 offset:1024\n"
  "ds_read_b64 v[238:239], v16 offset:1536\n"
  "v_mfma_f32_16x16x32_bf16 v[192:195], v[224:227], v[140:143], v[192:195]\n"
  "v_cvt_pk_bf16_f32 v166, v168, v169\n"
  "v_cvt_pk_bf16_f32 v167, v170, v171\n"
  "v_mfma_f32_16x16x32_bf16 v[176:179], v[228:231], v[128:131], v[176:179]\n"
  "v_exp_f32 v184, v184\n"
  "v_mfma_f32_16x16x32_bf16 v[192:195], v[228:231], v[144:147], v[192:195]\n"
  "v_exp_f32 v185, v185\n"
  "s_waitcnt lgkmcnt(10)\n"
  "v_mfma_f32_16x16x32_bf16 v[44:47], v[200:203], v[164:167], v[44:47]\n"
  "v_exp_f32 v186, v186\n"
  "v_mfma_f32_16x16x32_bf16 v[36:39], v[32:35], v[164:167], v[36:39]\n"
  "v_exp_f32 v187, v187\n"
  "v_cvt_pk_bf16_f32 v180, v180, v181\n"
  "v_cvt_pk_bf16_f32 v181, v182, v183\n"
  "v_cvt_pk_bf16_f32 v182, v184, v185\n"
  "v_cvt_pk_bf16_f32 v183, v186, v187\n"
  "s_nop 1\n"
  "v_mfma_f32_16x16x32_bf16 v[76:79], v[200:203], v[180:183], v[76:79]\n"
  "v_exp_f32 v172, v172\n"
  "s_setprio 2\n"
  "v_mfma_f32_16x16x32_bf16 v[40:43], v[32:35], v[180:183], v[40:43]\n"
  "v_exp_f32 v173, v173\n"
  "s_waitcnt lgkmcnt(2)\n"
  "v_mfma_f32_16x16x32_bf16 v[108:111], v[200:203], v[232:235], v[108:111]\n"
  "v_exp_f32 v174, v174\n"
  "ds_read_b64_tr_b16 v[216:217], v13 offset:512\n"
  "ds_read_b64_tr_b16 v[218:219], v13 offset:4608\n"
  "v_mfma_f32_16x16x32_bf16 v[48:51], v[204:207], v[164:167], v[48:51]\n"
  "v_exp_f32 v175, v175\n"
  "v_mfma_f32_16x16x32_bf16 v[80:83], v[204:207], v[180:183], v[80:83]\n"
  "v_exp_f32 v188, v188\n"
  "v_mfma_f32_16x16x32_bf16 v[112:115], v[204:207], v[232:235], v[112:115]\n"
  "v_exp_f32 v189, v189\n"
  "ds_read_b64_tr_b16 v[220:221], v14 offset:512\n"
  "ds_read_b64_tr_b16 v[222:223], v14 offset:4608\n"
  "v_mfma_f32_16x16x32_bf16 v[52:55], v[208:211], v[164:167], v[52:55]\n"
  "v_exp_f32 v190, v190\n"
  "v_mfma_f32_16x16x32_bf16 v[84:87], v[208:211], v[180:183], v[84:87]\n"
  "v_exp_f32 v191, v191\n"
  "ds_read_b64_tr_b16 v[224:225], v13 offset:768\n"
  "ds_read_b64_tr_b16 v[226:227], v13 offset:4864\n"
  "v_mfma_f32_16x16x32_bf16 v[56:59], v[212:215], v[164:167], v[56:59]\n"
  "v_exp_f32 v176, v176\n"
  "v_mfma_f32_16x16x32_bf16 v[88:91], v[212:215], v[180:183], v[88:91]\n"
  "v_exp_f32 v177, v177\n"
  "ds_read_b64_tr_b16 v[228:229], v14 offset:768\n"
  "ds_read_b64_tr_b16 v[230:231], v14 offset:4864\n"
  "s_waitcnt lgkmcnt(6)\n"
  "v_mfma_f32_16x16x32_bf16 v[60:63], v[216:219], v[164:167], v[60:63]\n"
  "v_exp_f32 v178, v178\n"
  "v_mfma_f32_16x16x32_bf16 v[92:95], v[216:219], v[180:183], v[92:95]\n"
  "v_exp_f32 v179, v179\n"
  "ds_read_b64_tr_b16 v[200:201], v13 offset:8192\n"
  "ds_read_b64_tr_b16 v[202:203], v13 offset:12288\n"
  "s_setprio 1\n"
  "s_waitcnt lgkmcnt(6)\n"
  "v_mfma_f32_16x16x32_bf16 v[64:67], v[220:223], v[164:167], v[64:67]\n"
  "v_cvt_pk_bf16_f32 v172, v172, v173\n"
  "v_cvt_pk_bf16_f32 v173, v174, v175\n"
  "v_mfma_f32_16x16x32_bf16 v[96:99], v[220:223], v[180:183], v[96:99]\n"
  "v_cvt_pk_bf16_f32 v174, v176, v177\n"
  "v_cvt_pk_bf16_f32 v175, v178, v179\n"
  "ds_read_b64_tr_b16 v[204:205], v14 offset:8192\n"
  "ds_read_b64_tr_b16 v[206:207], v14 offset:12288\n"
  "s_waitcnt lgkmcnt(6)\n"
  "v_mfma_f32_16x16x32_bf16 v[68:71], v[224:227], v[164:167], v[68:71]\n"
  "v_exp_f32 v192, v192\n"
  "v_mfma_f32_16x16x32_bf16 v[100:103], v[224:227], v[180:183], v[100:103]\n"
  "v_exp_f32 v193, v193\n"
  "ds_read_b64_tr_b16 v[208:209], v13 offset:8448\n"
  "ds_read_b64_tr_b16 v[210:211], v13 offset:12544\n"
  "s_waitcnt lgkmcnt(6)\n"
  "v_mfma_f32_16x16x32_bf16 v[72:75], v[228:231], v[164:167], v[72:75]\n"
  "v_exp_f32 v194, v194\n"
  "v_mfma_f32_16x16x32_bf16 v[104:107], v[228:231], v[180:183], v[104:107]\n"
  "v_exp_f32 v195, v195\n"
  "ds_read_b64_tr_b16 v[212:213], v14 offset:8448\n"
  "ds_read_b64_tr_b16 v[214:215], v14 offset:12544\n"
  "s_waitcnt lgkmcnt(6)\n"
  "v_mfma_f32_16x16x32_bf16 v[44:47], v[200:203], v[172:175], v[44:47]\n"
  "v_cvt_pk_bf16_f32 v188, v188, v189\n"
  "v_cvt_pk_bf16_f32 v189, v190, v191\n"
  "v_mfma_f32_16x16x32_bf16 v[36:39], v[32:35], v[172:175], v[36:39]\n"
  "v_cvt_pk_bf16_f32 v190, v192, v193\n"
  "v_cvt_pk_bf16_f32 v191, v194, v195\n"
  "s_nop 1\n"
  "v_mfma_f32_16x16x32_bf16 v[76:79], v[200:203], v[188:191], v[76:79]\n"
  "v_mfma_f32_16x16x32_bf16 v[40:43], v[32:35], v[188:191], v[40:43]\n"
  "v_mfma_f32_16x16x32_bf16 v[108:111], v[200:203], v[236:239], v[108:111]\n"
  "ds_read_b64_tr_b16 v[216:217], v13 offset:8704\n"
  "ds_read_b64_tr_b16 v[218:219], v13 offset:12800\n"
  "s_setprio 0\n"
  "s_waitcnt lgkmcnt(6)\n"
  "v_mfma_f32_16x16x32_bf16 v[48:51], v[204:207], v[172:175], v[48:51]\n"
  "v_mfma_f32_16x16x32_bf16 v[80:83], v[204:207], v[188:191], v[80:83]\n"
  "v_mfma_f32_16x16x32_bf16 v[112:115], v[204:207], v[236:239], v[112:115]\n"
  "ds_read_b64_tr_b16 v[220:221], v14 offset:8704\n"
  "ds_read_b64_tr_b16 v[222:223], v14 offset:12800\n"
  "s_waitcnt lgkmcnt(6)\n"
  "v_mfma_f32_16x16x32_bf16 v[52:55], v[208:211], v[172:175], v[52:55]\n"
  "v_mfma_f32_16x16x32_bf16 v[84:87], v[208:211], v[188:191], v[84:87]\n"
  "ds_read_b64_tr_b16 v[224:225], v13 offset:8960\n"
  "ds_read_b64_tr_b16 v[226:227], v13 offset:13056\n"
  "s_waitcnt lgkmcnt(6)\n"
  "v_mfma_f32_16x16x32_bf16 v[56:59], v[212:215], v[172:175], v[56:59]\n"
  "v_mfma_f32_16x16x32_bf16 v[88:91], v[212:215], v[188:191], v[88:91]\n"
  "ds_read_b64_tr_b16 v[228:229], v14 offset:8960\n"
  "ds_read_b64_tr_b16 v[230:231], v14 offset:13056\n"
  "s_waitcnt lgkmcnt(6)\n"
  "v_mfma_f32_16x16x32_bf16 v[60:63], v[216:219], v[172:175], v[60:63]\n"
  "v_mfma_f32_16x16x32_bf16 v[92:95], v[216:219], v[188:191], v[92:95]\n"
  "s_waitcnt lgkmcnt(4)\n"
  "v_mfma_f32_16x16x32_bf16 v[64:67], v[220:223], v[172:175], v[64:67]\n"
  "v_mfma_f32_16x16x32_bf16 v[96:99], v[220:223], v[188:191], v[96:99]\n"
  "s_setprio 3\n"
  "s_waitcnt vmcnt(4) lgkmcnt(0)\n"
  "s_barrier\n"
  "ds_read_b128 v[200:203], v240 offset:0\n"
  "v_mfma_f32_16x16x32_bf16 v[68:71], v[224:227], v[172:175], v[68:71]\n"
  "ds_read_b128 v[204:207], v240 offset:256\n"
  "s_add_u32 m0, s43, 0\n"
  "s_add_u32 s40, s42, s52\n"
  "buffer_load_dwordx4 v252, %[srdwo], s40 offen offset:0 lds\n"
  "v_mfma_f32_16x16x32_bf16 v[100:103], v[224:227], v[188:191], v[100:103]\n"
  "ds_read_b128 v[208:211], v240 offset:512\n"
  "s_add_u32 m0, s43, 1024\n"
  "s_add_u32 s40, s42, s53\n"
  "buffer_load_dwordx4 v252, %[srdwo], s40 offen offset:0 lds\n"
  "v_mfma_f32_16x16x32_bf16 v[72:75], v[228:231], v[172:175], v[72:75]\n"
  "ds_read_b128 v[212:215], v240 offset:768\n"
  "s_add_u32 m0, s44, 0\n"
  "s_add_u32 s40, s42, s54\n"
  "s_mov_b64 exec, s[48:49]\n"
  "buffer_load_dwordx4 v252, %[srdwo], s40 offen offset:0 lds\n"
  "s_mov_b64 exec, -1\n"
  "s_add_u32 s42, s42, 81920\n"
  "s_cmp_ge_u32 s42, 1310720\n"
  "s_cselect_b32 s40, 1310720, 0\n"
  "s_sub_u32 s42, s42, s40\n"
  "v_mfma_f32_16x16x32_bf16 v[104:107], v[228:231], v[188:191], v[104:107]\n"
  "s_add_u32 m0, s43, 38912\n"
  "s_add_u32 s40, s42, s52\n"
  "buffer_load_dwordx4 v252, %[srdwo], s40 offen offset:0 lds\n"
  "s_add_u32 m0, s43, 39936\n"
  "s_add_u32 s40, s42, s53\n"
  "buffer_load_dwordx4 v252, %[srdwo], s40 offen offset:0 lds\n"
  "s_add_u32 m0, s44, 38912\n"
  "s_add_u32 s40, s42, s54\n"
  "s_mov_b64 exec, s[48:49]\n"
  "buffer_load_dwordx4 v252, %[srdwo], s40 offen offset:0 lds\n"
  "s_mov_b64 exec, -1\n"
  "s_add_u32 s42, s42, 81920\n"
  "s_cmp_ge_u32 s42, 1310720\n"
  "s_cselect_b32 s40, 1310720, 0\n"
  "s_sub_u32 s42, s42, s40\n"
  "s_waitcnt lgkmcnt(3)\n"
  "v_mfma_f32_16x16x32_bf16 v[196:199], v[200:203], v[148:151], v[28:31]\n"
  "ds_read_b128 v[216:219], v240 offset:4096\n"
  "v_mfma_f32_16x16x32_bf16 v[164:167], v[200:203], v[116:119], v[20:23]\n"
  "v_mfma_f32_16x16x32_bf16 v[180:183], v[200:203], v[132:135], v[24:27]\n"
  "ds_read_b128 v[220:223], v240 offset:4352\n"
  "s_waitcnt lgkmcnt(4)\n"
  "v_mfma_f32_16x16x32_bf16 v[196:199], v[204:207], v[152:155], v[196:199]\n"
  "s_setprio 2\n"
  "v_mfma_f32_16x16x32_bf16 v[164:167], v[204:207], v[120:123], v[164:167]\n"
  "ds_read_b128 v[224:227], v240 offset:4608\n"
  "v_mfma_f32_16x16x32_bf16 v[180:183], v[204:207], v[136:139], v[180:183]\n"
  "s_waitcnt lgkmcnt(4)\n"
  "v_mfma_f32_16x16x32_bf16 v[196:199], v[208:211], v[156:159], v[196:199]\n"
  "ds_read_b128 v[228:231], v240 offset:4864\n"
  "v_mfma_f32_16x16x32_bf16 v[164:167], v[208:211], v[124:127], v[164:167]\n"
  "v_mfma_f32_16x16x32_bf16 v[180:183], v[208:211], v[140:143], v[180:183]\n"
  "s_waitcnt lgkmcnt(4)\n"
  "v_mfma_f32_16x16x32_bf16 v[196:199], v[212:215], v[160:163], v[196:199]\n"
  "v_mfma_f32_16x16x32_bf16 v[164:167], v[212:215], v[128:131], v[164:167]\n"
  "v_mfma_f32_16x16x32_bf16 v[180:183], v[212:215], v[144:147], v[180:183]\n"
  "s_setprio 1\n"
  "s_waitcnt lgkmcnt(3)\n"
  "v_mfma_f32_16x16x32_bf16 v[168:171], v[216:219], v[116:119], v[20:23]\n"
  "ds_read_b128 v[200:203], v240 offset:8192\n"
  "v_mfma_f32_16x16x32_bf16 v[184:187], v[216:219], v[132:135], v[24:27]\n"
  "s_nop 0\n"
  "v_exp_f32 v196, v196\n"
  "ds_read_b128 v[204:207], v240 offset:8448\n"
  "s_waitcnt lgkmcnt(4)\n"
  "v_mfma_f32_16x16x32_bf16 v[168:171], v[220:223], v[120:123], v[168:171]\n"
  "v_exp_f32 v197, v197\n"
  "ds_read_b128 v[208:211], v240 offset:8704\n"
  "v_mfma_f32_16x16x32_bf16 v[184:187], v[220:223], v[136:139], v[184:187]\n"
  "v_exp_f32 v198, v198\n"
  "ds_read_b128 v[212:215], v240 offset:8960\n"
  "s_waitcnt lgkmcnt(5)\n"
  "v_mfma_f32_16x16x32_bf16 v[168:171], v[224:227], v[124:127], v[168:171]\n"
  "v_exp_f32 v199, v199\n"
  "v_mfma_f32_16x16x32_bf16 v[184:187], v[224:227], v[140:143], v[184:187]\n"
  "v_add_f32 v18, v18, v196\n"
  "v_add_f32 v19, v19, v197\n"
  "s_waitcnt lgkmcnt(4)\n"
  "v_mfma_f32_16x16x32_bf16 v[168:171], v[228:231], v[128:131], v[168:171]\n"
  "v_cvt_pk_bf16_f32 v244, v196, v197\n"
  "v_cvt_pk_bf16_f32 v245, v198, v199\n"
  "ds_write_b64 v15, v[244:245] offset:0\n"
  "v_mfma_f32_16x16x32_bf16 v[184:187], v[228:231], v[144:147], v[184:187]\n"
  "v_add_f32 v18, v18, v198\n"
  "v_add_f32 v19, v19, v199\n"
  "s_setprio 0\n"
  "s_waitcnt lgkmcnt(4)\n"
  "v_mfma_f32_16x16x32_bf16 v[172:175], v[200:203], v[116:119], v[20:23]\n"
  "v_exp_f32 v164, v164\n"
  "ds_read_b128 v[216:219], v240 offset:12288\n"
  "v_mfma_f32_16x16x32_bf16 v[188:191], v[200:203], v[132:135], v[24:27]\n"
  "v_exp_f32 v165, v165\n"
  "ds_read_b128 v[220:223], v240 offset:12544\n"
  "s_waitcnt lgkmcnt(5)\n"
  "v_mfma_f32_16x16x32_bf16 v[172:175], v[204:207], v[120:123], v[172:175]\n"
  "v_exp_f32 v166, v166\n"
  "ds_read_b128 v[224:227], v240 offset:12800\n"
  "v_mfma_f32_16x16x32_bf16 v[188:191], v[204:207], v[136:139], v[188:191]\n"
  "v_exp_f32 v167, v167\n"
  "ds_read_b128 v[228:231], v240 offset:13056\n"
  "s_waitcnt lgkmcnt(6)\n"
  "v_mfma_f32_16x16x32_bf16 v[172:175], v[208:211], v[124:127], v[172:175]\n"
  "v_exp_f32 v180, v180\n"
  "v_mfma_f32_16x16x32_bf16 v[188:191], v[208:211], v[140:143], v[188:191]\n"
  "v_exp_f32 v181, v181\n"
  "s_waitcnt lgkmcnt(5)\n"
  "v_mfma_f32_16x16x32_bf16 v[172:175], v[212:215], v[128:131], v[172:175]\n"
  "v_exp_f32 v182, v182\n"
  "v_mfma_f32_16x16x32_bf16 v[188:191], v[212:215], v[144:147], v[188:191]\n"
  "v_exp_f32 v183, v183\n"
  "s_waitcnt vmcnt(6) lgkmcnt(0)\n"
  "s_barrier\n"
  "ds_read_b64_tr_b16 v[200:201], v241 offset:0\n"
  "ds_read_b64_tr_b16 v[202:203], v241 offset:4096\n"
  "s_setprio 3\n"
  "v_mfma_f32_16x16x32_bf16 v[176:179], v[216:219], v[116:119], v[20:23]\n"
  "v_exp_f32 v168, v168\n"
  "ds_read_b64_tr_b16 v[204:205], v242 offset:0\n"
  "ds_read_b64_tr_b16 v[206:207], v242 offset:4096\n"
  "v_mfma_f32_16x16x32_bf16 v[192:195], v[216:219], v[132:135], v[24:27]\n"
  "v_exp_f32 v169, v169\n"
  "ds_read_b64_tr_b16 v[208:209], v241 offset:256\n"
  "ds_read_b64_tr_b16 v[210:211], v241 offset:4352\n"
  "v_mfma_f32_16x16x32_bf16 v[176:179], v[220:223], v[120:123], v[176:179]\n"
  "v_exp_f32 v170, v170\n"
  "ds_read_b64_tr_b16 v[212:213], v242 offset:256\n"
  "ds_read_b64_tr_b16 v[214:215], v242 offset:4352\n"
  "v_mfma_f32_16x16x32_bf16 v[192:195], v[220:223], v[136:139], v[192:195]\n"
  "v_exp_f32 v171, v171\n"
  "ds_read_b64 v[232:233], v16 offset:0\n"
  "ds_read_b64 v[234:235], v16 offset:512\n"
  "v_mfma_f32_16x16x32_bf16 v[176:179], v[224:227], v[124:127], v[176:179]\n"
  "v_cvt_pk_bf16_f32 v164, v164, v165\n"
  "v_cvt_pk_bf16_f32 v165, v166, v167\n"
  "ds_read_b64 v[236:237], v16 offset:1024\n"
  "ds_read_b64 v[238:239], v16 offset:1536\n"
  "v_mfma_f32_16x16x32_bf16 v[192:195], v[224:227], v[140:143], v[192:195]\n"
  "v_cvt_pk_bf16_f32 v166, v168, v169\n"
  "v_cvt_pk_bf16_f32 v167, v170, v171\n"
  "v_mfma_f32_16x16x32_bf16 v[176:179], v[228:231], v[128:131], v[176:179]\n"
  "v_exp_f32 v184, v184\n"
  "v_mfma_f32_16x16x32_bf16 v[192:195], v[228:231], v[144:147], v[192:195]\n"
  "v_exp_f32 v185, v185\n"
  "s_waitcnt lgkmcnt(10)\n"
  "v_mfma_f32_16x16x32_bf16 v[44:47], v[200:203], v[164:167], v[44:47]\n"
  "v_exp_f32 v186, v186\n"
  "v_mfma_f32_16x16x32_bf16 v[36:39], v[32:35], v[164:167], v[36:39]\n"
  "v_exp_f32 v187, v187\n"
  "v_cvt_pk_bf16_f32 v180, v180, v181\n"
  "v_cvt_pk_bf16_f32 v181, v182, v183\n"
  "v_cvt_pk_bf16_f32 v182, v184, v185\n"
  "v_cvt_pk_bf16_f32 v183, v186, v187\n"
  "s_nop 1\n"
  "v_mfma_f32_16x16x32_bf16 v[76:79], v[200:203], v[180:183], v[76:79]\n"
  "v_exp_f32 v172, v172\n"
  "s_setprio 2\n"
  "v_mfma_f32_16x16x32_bf16 v[40:43], v[32:35], v[180:183], v[40:43]\n"
  "v_exp_f32 v173, v173\n"
  "s_waitcnt lgkmcnt(2)\n"
  "v_mfma_f32_16x16x32_bf16 v[108:111], v[200:203], v[232:235], v[108:111]\n"
  "v_exp_f32 v174, v174\n"
  "ds_read_b64_tr_b16 v[216:217], v241 offset:512\n"
  "ds_read_b64_tr_b16 v[218:219], v241 offset:4608\n"
  "v_mfma_f32_16x16x32_bf16 v[48:51], v[204:207], v[164:167], v[48:51]\n"
  "v_exp_f32 v175, v175\n"
  "v_mfma_f32_16x16x32_bf16 v[80:83], v[204:207], v[180:183], v[80:83]\n"
  "v_exp_f32 v188, v188\n"
  "v_mfma_f32_16x16x32_bf16 v[112:115], v[204:207], v[232:235], v[112:115]\n"
  "v_exp_f32 v189, v189\n"
  "ds_read_b64_tr_b16 v[220:221], v242 offset:512\n"
  "ds_read_b64_tr_b16 v[222:223], v242 offset:4608\n"
  "v_mfma_f32_16x16x32_bf16 v[52:55], v[208:211], v[164:167], v[52:55]\n"
  "v_exp_f32 v190, v190\n"
  "v_mfma_f32_16x16x32_bf16 v[84:87], v[208:211], v[180:183], v[84:87]\n"
  "v_exp_f32 v191, v191\n"
  "ds_read_b64_tr_b16 v[224:225], v241 offset:768\n"
  "ds_read_b64_tr_b16 v[226:227], v241 offset:4864\n"
  "v_mfma_f32_16x16x32_bf16 v[56:59], v[212:215], v[164:167], v[56:59]\n"
  "v_exp_f32 v176, v176\n"
  "v_mfma_f32_16x16x32_bf16 v[88:91], v[212:215], v[180:183], v[88:91]\n"
  "v_exp_f32 v177, v177\n"
  "ds_read_b64_tr_b16 v[228:229], v242 offset:768\n"
  "ds_read_b64_tr_b16 v[230:231], v242 offset:4864\n"
  "s_waitcnt lgkmcnt(6)\n"
  "v_mfma_f32_16x16x32_bf16 v[60:63], v[216:219], v[164:167], v[60:63]\n"
  "v_exp_f32 v178, v178\n"
  "v_mfma_f32_16x16x32_bf16 v[92:95], v[216:219], v[180:183], v[92:95]\n"
  "v_exp_f32 v179, v179\n"
  "ds_read_b64_tr_b16 v[200:201], v241 offset:8192\n"
  "ds_read_b64_tr_b16 v[202:203], v241 offset:12288\n"
  "s_setprio 1\n"
  "s_waitcnt lgkmcnt(6)\n"
  "v_mfma_f32_16x16x32_bf16 v[64:67], v[220:223], v[164:167], v[64:67]\n"
  "v_cvt_pk_bf16_f32 v172, v172, v173\n"
  "v_cvt_pk_bf16_f32 v173, v174, v175\n"
  "v_mfma_f32_16x16x32_bf16 v[96:99], v[220:223], v[180:183], v[96:99]\n"
  "v_cvt_pk_bf16_f32 v174, v176, v177\n"
  "v_cvt_pk_bf16_f32 v175, v178, v179\n"
  "ds_read_b64_tr_b16 v[204:205], v242 offset:8192\n"
  "ds_read_b64_tr_b16 v[206:207], v242 offset:12288\n"
  "s_waitcnt lgkmcnt(6)\n"
  "v_mfma_f32_16x16x32_bf16 v[68:71], v[224:227], v[164:167], v[68:71]\n"
  "v_exp_f32 v192, v192\n"
  "v_mfma_f32_16x16x32_bf16 v[100:103], v[224:227], v[180:183], v[100:103]\n"
  "v_exp_f32 v193, v193\n"
  "ds_read_b64_tr_b16 v[208:209], v241 offset:8448\n"
  "ds_read_b64_tr_b16 v[210:211], v241 offset:12544\n"
  "s_waitcnt lgkmcnt(6)\n"
  "v_mfma_f32_16x16x32_bf16 v[72:75], v[228:231], v[164:167], v[72:75]\n"
  "v_exp_f32 v194, v194\n"
  "v_mfma_f32_16x16x32_bf16 v[104:107], v[228:231], v[180:183], v[104:107]\n"
  "v_exp_f32 v195, v195\n"
  "ds_read_b64_tr_b16 v[212:213], v242 offset:8448\n"
  "ds_read_b64_tr_b16 v[214:215], v242 offset:12544\n"
  "s_waitcnt lgkmcnt(6)\n"
  "v_mfma_f32_16x16x32_bf16 v[44:47], v[200:203], v[172:175], v[44:47]\n"
  "v_cvt_pk_bf16_f32 v188, v188, v189\n"
  "v_cvt_pk_bf16_f32 v189, v190, v191\n"
  "v_mfma_f32_16x16x32_bf16 v[36:39], v[32:35], v[172:175], v[36:39]\n"
  "v_cvt_pk_bf16_f32 v190, v192, v193\n"
  "v_cvt_pk_bf16_f32 v191, v194, v195\n"
  "s_nop 1\n"
  "v_mfma_f32_16x16x32_bf16 v[76:79], v[200:203], v[188:191], v[76:79]\n"
  "v_lshrrev_b32 v3, 2, v1\n"
  "v_lshlrev_b32 v3, 6, v3\n"
  "v_mfma_f32_16x16x32_bf16 v[40:43], v[32:35], v[188:191], v[40:43]\n"
  "v_bfe_u32 v5, v1, 1, 1\n"
  "v_lshl_add_u32 v3, v5, 4, v3\n"
  "v_mfma_f32_16x16x32_bf16 v[108:111], v[200:203], v[236:239], v[108:111]\n"
  "v_and_b32 v5, 1, v1\n"
  "v_lshl_add_u32 v3, v5, 3, v3\n"
  "ds_read_b64_tr_b16 v[216:217], v241 offset:8704\n"
  "ds_read_b64_tr_b16 v[218:219], v241 offset:12800\n"
  "s_setprio 0\n"
  "s_waitcnt lgkmcnt(6)\n"
  "v_mfma_f32_16x16x32_bf16 v[48:51], v[204:207], v[172:175], v[48:51]\n"
  "v_bfe_u32 v5, v1, 4, 1\n"
  "v_add_u32 v3, %[ldsb], v3\n"
  "v_mfma_f32_16x16x32_bf16 v[80:83], v[204:207], v[188:191], v[80:83]\n"
  "s_add_u32 s38, s37, 0\n"
  "s_cmp_ge_u32 s38, 5\n"
  "s_cselect_b32 s39, 5, 0\n"
  "s_sub_u32 s38, s38, s39\n"
  "s_mul_i32 s39, s36, 5\n"
  "s_add_u32 s39, s39, s38\n"
  "s_and_b32 s38, s39, 1\n"
  "s_lshr_b32 s39, s39, 1\n"
  "s_lshl_b32 s39, s39, 12\n"
  "v_xor_b32 v7, s38, v5\n"
  "v_lshl_add_u32 v7, v7, 5, v3\n"
  "v_mfma_f32_16x16x32_bf16 v[112:115], v[204:207], v[236:239], v[112:115]\n"
  "v_add_u32 v20, s39, v7\n"
  "v_add_u32 v25, 77824, v20\n"
  "ds_read_b64_tr_b16 v[220:221], v242 offset:8704\n"
  "ds_read_b64_tr_b16 v[222:223], v242 offset:12800\n"
  "s_waitcnt lgkmcnt(6)\n"
  "v_mfma_f32_16x16x32_bf16 v[52:55], v[208:211], v[172:175], v[52:55]\n"
  "s_add_u32 s38, s37, 1\n"
  "s_cmp_ge_u32 s38, 5\n"
  "s_cselect_b32 s39, 5, 0\n"
  "s_sub_u32 s38, s38, s39\n"
  "s_mul_i32 s39, s36, 5\n"
  "s_add_u32 s39, s39, s38\n"
  "s_and_b32 s38, s39, 1\n"
  "s_lshr_b32 s39, s39, 1\n"
  "s_lshl_b32 s39, s39, 12\n"
  "v_xor_b32 v7, s38, v5\n"
  "v_lshl_add_u32 v7, v7, 5, v3\n"
  "v_mfma_f32_16x16x32_bf16 v[84:87], v[208:211], v[188:191], v[84:87]\n"
  "v_add_u32 v21, s39, v7\n"
  "v_add_u32 v26, 77824, v21\n"
  "ds_read_b64_tr_b16 v[224:225], v241 offset:8960\n"
  "ds_read_b64_tr_b16 v[226:227], v241 offset:13056\n"
  "s_waitcnt lgkmcnt(6)\n"
  "v_mfma_f32_16x16x32_bf16 v[56:59], v[212:215], v[172:175], v[56:59]\n"
  "s_add_u32 s38, s37, 2\n"
  "s_cmp_ge_u32 s38, 5\n"
  "s_cselect_b32 s39, 5, 0\n"
  "s_sub_u32 s38, s38, s39\n"
  "s_mul_i32 s39, s36, 5\n"
  "s_add_u32 s39, s39, s38\n"
  "s_and_b32 s38, s39, 1\n"
  "s_lshr_b32 s39, s39, 1\n"
  "s_lshl_b32 s39, s39, 12\n"
  "v_xor_b32 v7, s38, v5\n"
  "v_lshl_add_u32 v7, v7, 5, v3\n"
  "v_mfma_f32_16x16x32_bf16 v[88:91], v[212:215], v[188:191], v[88:91]\n"
  "v_add_u32 v22, s39, v7\n"
  "v_add_u32 v27, 77824, v22\n"
  "ds_read_b64_tr_b16 v[228:229], v242 offset:8960\n"
  "ds_read_b64_tr_b16 v[230:231], v242 offset:13056\n"
  "s_waitcnt lgkmcnt(6)\n"
  "v_mfma_f32_16x16x32_bf16 v[60:63], v[216:219], v[172:175], v[60:63]\n"
  "s_add_u32 s38, s37, 3\n"
  "s_cmp_ge_u32 s38, 5\n"
  "s_cselect_b32 s39, 5, 0\n"
  "s_sub_u32 s38, s38, s39\n"
  "s_mul_i32 s39, s36, 5\n"
  "s_add_u32 s39, s39, s38\n"
  "s_and_b32 s38, s39, 1\n"
  "s_lshr_b32 s39, s39, 1\n"
  "s_lshl_b32 s39, s39, 12\n"
  "v_xor_b32 v7, s38, v5\n"
  "v_lshl_add_u32 v7, v7, 5, v3\n"
  "v_mfma_f32_16x16x32_bf16 v[92:95], v[216:219], v[188:191], v[92:95]\n"
  "v_add_u32 v23, s39, v7\n"
  "v_add_u32 v28, 77824, v23\n"
  "s_waitcnt lgkmcnt(4)\n"
  "v_mfma_f32_16x16x32_bf16 v[64:67], v[220:223], v[172:175], v[64:67]\n"
  "s_add_u32 s38, s37, 4\n"
  "s_cmp_ge_u32 s38, 5\n"
  "s_cselect_b32 s39, 5, 0\n"
  "s_sub_u32 s38, s38, s39\n"
  "s_mul_i32 s39, s36, 5\n"
  "s_add_u32 s39, s39, s38\n"
  "s_and_b32 s38, s39, 1\n"
  "s_lshr_b32 s39, s39, 1\n"
  "s_lshl_b32 s39, s39, 12\n"
  "v_xor_b32 v7, s38, v5\n"
  "v_lshl_add_u32 v7, v7, 5, v3\n"
  "v_mfma_f32_16x16x32_bf16 v[96:99], v[220:223], v[188:191], v[96:99]\n"
  "v_add_u32 v24, s39, v7\n"
  "v_add_u32 v29, 77824, v24\n"
  "s_setprio 3\n"
  "s_waitcnt lgkmcnt(2)\n"
  "v_mfma_f32_16x16x32_bf16 v[68:71], v[224:227], v[172:175], v[68:71]\n"
  "s_mov_b32 s38, 4\n"
  "s_mul_i32 s39, s36, 5\n"
  "s_add_u32 s39, s39, s38\n"
  "s_and_b32 s38, s39, 1\n"
  "s_lshr_b32 s39, s39, 1\n"
  "s_lshl_b32 s39, s39, 12\n"
  "v_xor_b32 v7, s38, v5\n"
  "v_lshl_add_u32 v7, v7, 5, v3\n"
  "v_mfma_f32_16x16x32_bf16 v[100:103], v[224:227], v[188:191], v[100:103]\n"
  "v_add_u32 v30, s39, v7\n"
  "v_add_u32 v31, 77824, v30\n"
  "s_waitcnt lgkmcnt(0)\n"
  "v_mfma_f32_16x16x32_bf16 v[72:75], v[228:231], v[172:175], v[72:75]\n"
  "s_lshl_b32 s38, s37, 12\n"
  "s_add_u32 s38, s38, 20480\n"
  "s_add_u32 s38, s38, %[ldsb]\n"
  "v_add_u32 v244, s38, v17\n"
  "v_add_u32 v245, 77824, v244\n"
  "v_mfma_f32_16x16x32_bf16 v[104:107], v[228:231], v[188:191], v[104:107]\n"
  "s_add_u32 s38, %[ldsb], 36864\n"
  "v_add_u32 v246, s38, v17\n"
  "v_add_u32 v247, 77824, v246\n"
  "s_mul_i32 s38, s36, 77824\n"
  "s_add_u32 s38, s38, 20480\n"
  "s_add_u32 s38, s38, %[ldsb]\n"
  "s_lshl_b32 s39, s37, 12\n"
  "s_add_u32 s39, s39, s38\n"
  "v_add_u32 v13, s39, v17\n"
  "s_mul_i32 s38, s36, 77824\n"
  "s_add_u32 s38, s38, 36864\n"
  "s_add_u32 s38, s38, %[ldsb]\n"
  "s_lshr_b32 s39, s37, 1\n"
  "s_mul_i32 s39, s39, 38912\n"
  "s_add_u32 s38, s38, s39\n"
  "s_and_b32 s39, s37, 1\n"
  "s_lshl_b32 s39, s39, 10\n"
  "s_add_u32 s38, s38, s39\n"
  "v_add_u32 v14, s38, v17\n"
  "v_and_b32 v7, 15, v1\n"
  "v_mul_u32_u24 v7, 2560, v7\n"
  "v_lshrrev_b32 v5, 4, v1\n"
  "v_lshl_add_u32 v15, v5, 3, v7\n"
  "v_mov_b64 v[248:249], 0\n"
  "v_max3_f32 v248, abs(v44), abs(v45), v248\n"
  "v_max3_f32 v249, abs(v46), abs(v47), v249\n"
  "v_max3_f32 v248, abs(v76), abs(v77), v248\n"
  "v_max3_f32 v249, abs(v78), abs(v79), v249\n"
  "v_max3_f32 v248, abs(v48), abs(v49), v248\n"
  "v_max3_f32 v249, abs(v50), abs(v51), v249\n"
  "v_max3_f32 v248, abs(v80), abs(v81), v248\n"
  "v_max3_f32 v249, abs(v82), abs(v83), v249\n"
  "v_max3_f32 v248, abs(v52), abs(v53), v248\n"
  "v_max3_f32 v249, abs(v54), abs(v55), v249\n"
  "v_max3_f32 v248, abs(v84), abs(v85), v248\n"
  "v_max3_f32 v249, abs(v86), abs(v87), v249\n"
  "v_max3_f32 v248, abs(v56), abs(v57), v248\n"
  "v_max3_f32 v249, abs(v58), abs(v59), v249\n"
  "v_max3_f32 v248, abs(v88), abs(v89), v248\n"
  "v_max3_f32 v249, abs(v90), abs(v91), v249\n"
  "v_max3_f32 v248, abs(v60), abs(v61), v248\n"
  "v_max3_f32 v249, abs(v62), abs(v63), v249\n"
  "v_max3_f32 v248, abs(v92), abs(v93), v248\n"
  "v_max3_f32 v249, abs(v94), abs(v95), v249\n"
  "v_max3_f32 v248, abs(v64), abs(v65), v248\n"
  "v_max3_f32 v249, abs(v66), abs(v67), v249\n"
  "v_max3_f32 v248, abs(v96), abs(v97), v248\n"
  "v_max3_f32 v249, abs(v98), abs(v99), v249\n"
  "v_max3_f32 v248, abs(v68), abs(v69), v248\n"
  "v_max3_f32 v249, abs(v70), abs(v71), v249\n"
  "v_max3_f32 v248, abs(v100), abs(v101), v248\n"
  "v_max3_f32 v249, abs(v102), abs(v103), v249\n"
  "v_max3_f32 v248, abs(v72), abs(v73), v248\n"
  "v_max3_f32 v249, abs(v74), abs(v75), v249\n"
  "v_max3_f32 v248, abs(v104), abs(v105), v248\n"
  "v_max3_f32 v249, abs(v106), abs(v107), v249\n"
  "v_max3_f32 v248, abs(v108), abs(v109), v248\n"
  "v_max3_f32 v249, abs(v110), abs(v111), v249\n"
  "v_max3_f32 v248, abs(v112), abs(v113), v248\n"
  "v_max3_f32 v249, abs(v114), abs(v115), v249\n"
  "s_branch P1_EPI\n"
  "P1_W1:\n"
  "ds_read_b128 v[216:219], v12 offset:0\n"
  "ds_read_b128 v[220:223], v12 offset:256\n"
  "ds_read_b128 v[224:227], v12 offset:512\n"
  "ds_read_b128 v[228:231], v12 offset:768\n"
  "s_waitcnt lgkmcnt(3)\n"
  "v_mfma_f32_16x16x32_bf16 v[196:199], v[216:219], v[148:151], 0\n"
  "v_mfma_f32_16x16x32_bf16 v[164:167], v[216:219], v[120:123], 0\n"
  "v_mfma_f32_16x16x32_bf16 v[180:183], v[216:219], v[136:139], 0\n"
  "s_waitcnt lgkmcnt(2)\n"
  "v_mfma_f32_16x16x32_bf16 v[196:199], v[220:223], v[152:155], v[196:199]\n"
  "v_mfma_f32_16x16x32_bf16 v[164:167], v[220:223], v[116:119], v[164:167]\n"
  "v_mfma_f32_16x16x32_bf16 v[180:183], v[220:223], v[132:135], v[180:183]\n"
  "s_waitcnt lgkmcnt(1)\n"
  "v_mfma_f32_16x16x32_bf16 v[196:199], v[224:227], v[156:159], v[196:199]\n"
  "v_mfma_f32_16x16x32_bf16 v[164:167], v[224:227], v[128:131], v[164:167]\n"
  "v_mfma_f32_16x16x32_bf16 v[180:183], v[224:227], v[144:147], v[180:183]\n"
  "s_waitcnt lgkmcnt(0)\n"
  "v_mfma_f32_16x16x32_bf16 v[196:199], v[228:231], v[160:163], v[196:199]\n"
  "v_mfma_f32_16x16x32_bf16 v[164:167], v[228:231], v[124:127], v[164:167]\n"
  "v_mfma_f32_16x16x32_bf16 v[180:183], v[228:231], v[140:143], v[180:183]\n"
  "s_nop 5\n"
  "v_max3_f32 v248, v196, v197, v198\n"
  "v_max_f32 v248, v248, v199\n"
  "v_mov_b32 v249, v248\n"
  "s_nop 1\n"
  "v_permlane32_swap_b32 v248, v249\n"
  "v_max_f32 v248, v248, v249\n"
  "v_mov_b32 v249, v248\n"
  "s_nop 1\n"
  "v_permlane16_swap_b32 v248, v249\n"
  "v_max_f32 v248, v248, v249\n"
  "v_sub_f32 v28, 0, v248\n"
  "v_mov_b32 v29, v28\n"
  "v_mov_b32 v30, v28\n"
  "v_mov_b32 v31, v28\n"
  "v_max3_f32 v248, v164, v165, v166\n"
  "v_max_f32 v248, v248, v167\n"
  "v_mov_b32 v249, v248\n"
  "s_nop 1\n"
  "v_permlane32_swap_b32 v248, v249\n"
  "v_max_f32 v248, v248, v249\n"
  "v_mov_b32 v249, v248\n"
  "s_nop 1\n"
  "v_permlane16_swap_b32 v248, v249\n"
  "v_max_f32 v248, v248, v249\n"
  "v_sub_f32 v20, 0, v248\n"
  "v_mov_b32 v21, v20\n"
  "v_mov_b32 v22, v20\n"
  "v_mov_b32 v23, v20\n"
  "v_max3_f32 v248, v180, v181, v182\n"
  "v_max_f32 v248, v248, v183\n"
  "v_mov_b32 v249, v248\n"
  "s_nop 1\n"
  "v_permlane32_swap_b32 v248, v249\n"
  "v_max_f32 v248, v248, v249\n"
  "v_mov_b32 v249, v248\n"
  "s_nop 1\n"
  "v_permlane16_swap_b32 v248, v249\n"
  "v_max_f32 v248, v248, v249\n"
  "v_sub_f32 v24, 0, v248\n"
  "v_mov_b32 v25, v24\n"
  "v_mov_b32 v26, v24\n"
  "v_mov_b32 v27, v24\n"
  "v_lshrrev_b32 v7, 2, v1\n"
  "v_mul_u32_u24 v7, 1280, v7\n"
  "v_and_b32 v8, 3, v1\n"
  "v_lshl_add_u32 v7, v8, 7, v7\n"
  "s_lshl_b32 s38, %[bb], 3\n"
  "s_add_u32 s38, s38, %[wave]\n"
  "s_mul_i32 s38, s38, 20480\n"
  "buffer_load_dword v254, v7, %[srdwo], s38 offen offset:0\n"
  "s_waitcnt lgkmcnt(0)\n"
  "s_nop 7\n"
  "s_nop 3\n"
  "P1_RST1:\n"
  "ds_read_b128 v[200:203], v12 offset:4096\n"
  "ds_read_b128 v[204:207], v12 offset:4352\n"
  "ds_read_b128 v[208:211], v12 offset:4608\n"
  "ds_read_b128 v[212:215], v12 offset:4864\n"
  "s_nop 7\n"
  "s_nop 3\n"
  "s_mov_b32 s41, 3\n"
  "P1_LOOP1:\n"
  "s_waitcnt lgkmcnt(3)\n"
  "v_mfma_f32_16x16x32_bf16 v[196:199], v[200:203], v[148:151], v[28:31]\n"
  "ds_read_b128 v[216:219], v12 offset:0\n"
  "v_mfma_f32_16x16x32_bf16 v[164:167], v[200:203], v[120:123], v[20:23]\n"
  "v_mfma_f32_16x16x32_bf16 v[180:183], v[200:203], v[136:139], v[24:27]\n"
  "ds_read_b128 v[220:223], v12 offset:256\n"
  "s_waitcnt lgkmcnt(4)\n"
  "v_mfma_f32_16x16x32_bf16 v[196:199], v[204:207], v[152:155], v[196:199]\n"
  "s_setprio 2\n"
  "v_mfma_f32_16x16x32_bf16 v[164:167], v[204:207], v[116:119], v[164:167]\n"
  "ds_read_b128 v[224:227], v12 offset:512\n"
  "v_mfma_f32_16x16x32_bf16 v[180:183], v[204:207], v[132:135], v[180:183]\n"
  "s_waitcnt lgkmcnt(4)\n"
  "v_mfma_f32_16x16x32_bf16 v[196:199], v[208:211], v[156:159], v[196:199]\n"
  "ds_read_b128 v[228:231], v12 offset:768\n"
  "v_mfma_f32_16x16x32_bf16 v[164:167], v[208:211], v[128:131], v[164:167]\n"
  "v_mfma_f32_16x16x32_bf16 v[180:183], v[208:211], v[144:147], v[180:183]\n"
  "s_waitcnt lgkmcnt(4)\n"
  "v_mfma_f32_16x16x32_bf16 v[196:199], v[212:215], v[160:163], v[196:199]\n"
  "v_mfma_f32_16x16x32_bf16 v[164:167], v[212:215], v[124:127], v[164:167]\n"
  "v_mfma_f32_16x16x32_bf16 v[180:183], v[212:215], v[140:143], v[180:183]\n"
  "s_setprio 1\n"
  "s_waitcnt lgkmcnt(3)\n"
  "v_mfma_f32_16x16x32_bf16 v[168:171], v[216:219], v[120:123], v[20:23]\n"
  "ds_read_b128 v[200:203], v12 offset:12288\n"
  "v_mfma_f32_16x16x32_bf16 v[184:187], v[216:219], v[136:139], v[24:27]\n"
  "s_nop 0\n"
  "v_exp_f32 v196, v196\n"
  "ds_read_b128 v[204:207], v12 offset:12544\n"
  "s_waitcnt lgkmcnt(4)\n"
  "v_mfma_f32_16x16x32_bf16 v[168:171], v[220:223], v[116:119], v[168:171]\n"
  "v_exp_f32 v197, v197\n"
  "ds_read_b128 v[208:211], v12 offset:12800\n"
  "v_mfma_f32_16x16x32_bf16 v[184:187], v[220:223], v[132:135], v[184:187]\n"
  "v_exp_f32 v198, v198\n"
  "ds_read_b128 v[212:215], v12 offset:13056\n"
  "s_waitcnt lgkmcnt(5)\n"
  "v_mfma_f32_16x16x32_bf16 v[168:171], v[224:227], v[128:131], v[168:171]\n"
  "v_exp_f32 v199, v199\n"
  "v_mfma_f32_16x16x32_bf16 v[184:187], v[224:227], v[144:147], v[184:187]\n"
  "v_add_f32 v18, v18, v196\n"
  "v_add_f32 v19, v19, v197\n"
  "s_waitcnt lgkmcnt(4)\n"
  "v_mfma_f32_16x16x32_bf16 v[168:171], v[228:231], v[124:127], v[168:171]\n"
  "v_cvt_pk_bf16_f32 v244, v196, v197\n"
  "v_cvt_pk_bf16_f32 v245, v198, v199\n"
  "ds_write_b64 v15, v[244:245] offset:0\n"
  "v_mfma_f32_16x16x32_bf16 v[184:187], v[228:231], v[140:143], v[184:187]\n"
  "v_add_f32 v18, v18, v198\n"
  "v_add_f32 v19, v19, v199\n"
  "s_setprio 0\n"
  "s_waitcnt lgkmcnt(4)\n"
  "v_mfma_f32_16x16x32_bf16 v[172:175], v[200:203], v[120:123], v[20:23]\n"
  "v_exp_f32 v164, v164\n"
  "ds_read_b128 v[216:219], v12 offset:8192\n"
  "v_mfma_f32_16x16x32_bf16 v[188:191], v[200:203], v[136:139], v[24:27]\n"
  "v_exp_f32 v165, v165\n"
  "ds_read_b128 v[220:223], v12 offset:8448\n"
  "s_waitcnt lgkmcnt(5)\n"
  "v_mfma_f32_16x16x32_bf16 v[172:175], v[204:207], v[116:119], v[172:175]\n"
  "v_exp_f32 v166, v166\n"
  "ds_read_b128 v[224:227], v12 offset:8704\n"
  "v_mfma_f32_16x16x32_bf16 v[188:191], v[204:207], v[132:135], v[188:191]\n"
  "v_exp_f32 v167, v167\n"
  "ds_read_b128 v[228:231], v12 offset:8960\n"
  "s_waitcnt lgkmcnt(6)\n"
  "v_mfma_f32_16x16x32_bf16 v[172:175], v[208:211], v[128:131], v[172:175]\n"
  "v_exp_f32 v180, v180\n"
  "v_mfma_f32_16x16x32_bf16 v[188:191], v[208:211], v[144:147], v[188:191]\n"
  "v_exp_f32 v181, v181\n"
  "s_waitcnt lgkmcnt(5)\n"
  "v_mfma_f32_16x16x32_bf16 v[172:175], v[212:215], v[124:127], v[172:175]\n"
  "v_exp_f32 v182, v182\n"
  "v_mfma_f32_16x16x32_bf16 v[188:191], v[212:215], v[140:143], v[188:191]\n"
  "v_exp_f32 v183, v183\n"
  "s_waitcnt vmcnt(8) lgkmcnt(0)\n"
  "s_barrier\n"
  "ds_read_b64_tr_b16 v[200:201], v13 offset:4096\n"
  "ds_read_b64_tr_b16 v[202:203], v13 offset:0\n"
  "s_setprio 3\n"
  "v_mfma_f32_16x16x32_bf16 v[176:179], v[216:219], v[120:123], v[20:23]\n"
  "v_exp_f32 v168, v168\n"
  "ds_read_b64_tr_b16 v[204:205], v14 offset:4096\n"
  "ds_read_b64_tr_b16 v[206:207], v14 offset:0\n"
  "s_add_u32 m0, s51, 0\n"
  "s_add_u32 s40, s50, 32768\n"
  "buffer_load_dwordx4 v10, %[srdk], s40 offen offset:0 nt lds\n"
  "v_mfma_f32_16x16x32_bf16 v[192:195], v[216:219], v[136:139], v[24:27]\n"
  "v_exp_f32 v169, v169\n"
  "ds_read_b64_tr_b16 v[208:209], v13 offset:4352\n"
  "ds_read_b64_tr_b16 v[210:211], v13 offset:256\n"
  "v_mfma_f32_16x16x32_bf16 v[176:179], v[220:223], v[116:119], v[176:179]\n"
  "v_exp_f32 v170, v170\n"
  "ds_read_b64_tr_b16 v[212:213], v14 offset:4352\n"
  "ds_read_b64_tr_b16 v[214:215], v14 offset:256\n"
  "buffer_load_dwordx4 v11, %[srdk], s40 offen offset:1024 nt lds\n"
  "v_mfma_f32_16x16x32_bf16 v[192:195], v[220:223], v[132:135], v[192:195]\n"
  "v_exp_f32 v171, v171\n"
  "ds_read_b64 v[232:233], v16 offset:512\n"
  "ds_read_b64 v[234:235], v16 offset:0\n"
  "v_mfma_f32_16x16x32_bf16 v[176:179], v[224:227], v[128:131], v[176:179]\n"
  "v_cvt_pk_bf16_f32 v164, v164, v165\n"
  "v_cvt_pk_bf16_f32 v165, v166, v167\n"
  "ds_read_b64 v[236:237], v16 offset:1536\n"
  "ds_read_b64 v[238:239], v16 offset:1024\n"
  "buffer_load_dwordx4 v10, %[srdk], s40 offen offset:2048 nt lds\n"
  "v_mfma_f32_16x16x32_bf16 v[192:195], v[224:227], v[144:147], v[192:195]\n"
  "v_cvt_pk_bf16_f32 v166, v168, v169\n"
  "v_cvt_pk_bf16_f32 v167, v170, v171\n"
  "v_mfma_f32_16x16x32_bf16 v[176:179], v[228:231], v[124:127], v[176:179]\n"
  "v_exp_f32 v184, v184\n"
  "buffer_load_dwordx4 v11, %[srdk], s40 offen offset:3072 nt lds\n"
  "v_mfma_f32_16x16x32_bf16 v[192:195], v[228:231], v[140:143], v[192:195]\n"
  "v_exp_f32 v185, v185\n"
  "s_waitcnt lgkmcnt(10)\n"
  "v_mfma_f32_16x16x32_bf16 v[44:47], v[200:203], v[164:167], v[44:47]\n"
  "v_exp_f32 v186, v186\n"
  "v_mfma_f32_16x16x32_bf16 v[36:39], v[32:35], v[164:167], v[36:39]\n"
  "v_exp_f32 v187, v187\n"
  "v_cvt_pk_bf16_f32 v180, v180, v181\n"
  "v_cvt_pk_bf16_f32 v181, v182, v183\n"
  "v_cvt_pk_bf16_f32 v182, v184, v185\n"
  "v_cvt_pk_bf16_f32 v183, v186, v187\n"
  "s_nop 1\n"
  "v_mfma_f32_16x16x32_bf16 v[76:79], v[200:203], v[180:183], v[76:79]\n"
  "v_exp_f32 v172, v172\n"
  "s_setprio 2\n"
  "v_mfma_f32_16x16x32_bf16 v[40:43], v[32:35], v[180:183], v[40:43]\n"
  "v_exp_f32 v173, v173\n"
  "ds_read_b64_tr_b16 v[216:217], v13 offset:4608\n"
  "ds_read_b64_tr_b16 v[218:219], v13 offset:512\n"
  "s_waitcnt lgkmcnt(10)\n"
  "v_mfma_f32_16x16x32_bf16 v[48:51], v[204:207], v[164:167], v[48:51]\n"
  "v_exp_f32 v174, v174\n"
  "v_mfma_f32_16x16x32_bf16 v[80:83], v[204:207], v[180:183], v[80:83]\n"
  "v_exp_f32 v175, v175\n"
  "ds_read_b64_tr_b16 v[220:221], v14 offset:4608\n"
  "ds_read_b64_tr_b16 v[222:223], v14 offset:512\n"
  "s_waitcnt lgkmcnt(10)\n"
  "v_mfma_f32_16x16x32_bf16 v[52:55], v[208:211], v[164:167], v[52:55]\n"
  "v_exp_f32 v188, v188\n"
  "v_mfma_f32_16x16x32_bf16 v[84:87], v[208:211], v[180:183], v[84:87]\n"
  "v_exp_f32 v189, v189\n"
  "s_waitcnt lgkmcnt(6)\n"
  "v_mfma_f32_16x16x32_bf16 v[108:111], v[208:211], v[232:235], v[108:111]\n"
  "v_exp_f32 v190, v190\n"
  "ds_read_b64_tr_b16 v[224:225], v13 offset:4864\n"
  "ds_read_b64_tr_b16 v[226:227], v13 offset:768\n"
  "v_mfma_f32_16x16x32_bf16 v[56:59], v[212:215], v[164:167], v[56:59]\n"
  "v_exp_f32 v191, v191\n"
  "v_mfma_f32_16x16x32_bf16 v[88:91], v[212:215], v[180:183], v[88:91]\n"
  "v_exp_f32 v176, v176\n"
  "v_mfma_f32_16x16x32_bf16 v[112:115], v[212:215], v[232:235], v[112:115]\n"
  "v_exp_f32 v177, v177\n"
  "ds_read_b64_tr_b16 v[228:229], v14 offset:4864\n"
  "ds_read_b64_tr_b16 v[230:231], v14 offset:768\n"
  "s_waitcnt lgkmcnt(6)\n"
  "v_mfma_f32_16x16x32_bf16 v[60:63], v[216:219], v[164:167], v[60:63]\n"
  "v_exp_f32 v178, v178\n"
  "v_mfma_f32_16x16x32_bf16 v[92:95], v[216:219], v[180:183], v[92:95]\n"
  "v_exp_f32 v179, v179\n"
  "ds_read_b64_tr_b16 v[200:201], v13 offset:12288\n"
  "ds_read_b64_tr_b16 v[202:203], v13 offset:8192\n"
  "s_setprio 1\n"
  "s_waitcnt lgkmcnt(6)\n"
  "v_mfma_f32_16x16x32_bf16 v[64:67], v[220:223], v[164:167], v[64:67]\n"
  "v_cvt_pk_bf16_f32 v172, v172, v173\n"
  "v_cvt_pk_bf16_f32 v173, v174, v175\n"
  "v_mfma_f32_16x16x32_bf16 v[96:99], v[220:223], v[180:183], v[96:99]\n"
  "v_cvt_pk_bf16_f32 v174, v176, v177\n"
  "v_cvt_pk_bf16_f32 v175, v178, v179\n"
  "ds_read_b64_tr_b16 v[204:205], v14 offset:12288\n"
  "ds_read_b64_tr_b16 v[206:207], v14 offset:8192\n"
  "s_waitcnt lgkmcnt(6)\n"
  "v_mfma_f32_16x16x32_bf16 v[68:71], v[224:227], v[164:167], v[68:71]\n"
  "v_exp_f32 v192, v192\n"
  "v_mfma_f32_16x16x32_bf16 v[100:103], v[224:227], v[180:183], v[100:103]\n"
  "v_exp_f32 v193, v193\n"
  "ds_read_b64_tr_b16 v[208:209], v13 offset:12544\n"
  "ds_read_b64_tr_b16 v[210:211], v13 offset:8448\n"
  "s_waitcnt lgkmcnt(6)\n"
  "v_mfma_f32_16x16x32_bf16 v[72:75], v[228:231], v[164:167], v[72:75]\n"
  "v_exp_f32 v194, v194\n"
  "v_mfma_f32_16x16x32_bf16 v[104:107], v[228:231], v[180:183], v[104:107]\n"
  "v_exp_f32 v195, v195\n"
  "ds_read_b64_tr_b16 v[212:213], v14 offset:12544\n"
  "ds_read_b64_tr_b16 v[214:215], v14 offset:8448\n"
  "s_waitcnt lgkmcnt(6)\n"
  "v_mfma_f32_16x16x32_bf16 v[44:47], v[200:203], v[172:175], v[44:47]\n"
  "v_cvt_pk_bf16_f32 v188, v188, v189\n"
  "v_cvt_pk_bf16_f32 v189, v190, v191\n"
  "v_mfma_f32_16x16x32_bf16 v[36:39], v[32:35], v[172:175], v[36:39]\n"
  "v_cvt_pk_bf16_f32 v190, v192, v193\n"
  "v_cvt_pk_bf16_f32 v191, v194, v195\n"
  "s_nop 1\n"
  "v_mfma_f32_16x16x32_bf16 v[76:79], v[200:203], v[188:191], v[76:79]\n"
  "v_mfma_f32_16x16x32_bf16 v[40:43], v[32:35], v[188:191], v[40:43]\n"
  "ds_read_b64_tr_b16 v[216:217], v13 offset:12800\n"
  "ds_read_b64_tr_b16 v[218:219], v13 offset:8704\n"
  "s_waitcnt lgkmcnt(6)\n"
  "v_mfma_f32_16x16x32_bf16 v[48:51], v[204:207], v[172:175], v[48:51]\n"
  "s_setprio 0\n"
  "v_mfma_f32_16x16x32_bf16 v[80:83], v[204:207], v[188:191], v[80:83]\n"
  "ds_read_b64_tr_b16 v[220:221], v14 offset:12800\n"
  "ds_read_b64_tr_b16 v[222:223], v14 offset:8704\n"
  "s_waitcnt lgkmcnt(6)\n"
  "v_mfma_f32_16x16x32_bf16 v[52:55], v[208:211], v[172:175], v[52:55]\n"
  "v_mfma_f32_16x16x32_bf16 v[84:87], v[208:211], v[188:191], v[84:87]\n"
  "v_mfma_f32_16x16x32_bf16 v[108:111], v[208:211], v[236:239], v[108:111]\n"
  "ds_read_b64_tr_b16 v[224:225], v13 offset:13056\n"
  "ds_read_b64_tr_b16 v[226:227], v13 offset:8960\n"
  "s_waitcnt lgkmcnt(6)\n"
  "v_mfma_f32_16x16x32_bf16 v[56:59], v[212:215], v[172:175], v[56:59]\n"
  "v_mfma_f32_16x16x32_bf16 v[88:91], v[212:215], v[188:191], v[88:91]\n"
  "v_mfma_f32_16x16x32_bf16 v[112:115], v[212:215], v[236:239], v[112:115]\n"
  "ds_read_b64_tr_b16 v[228:229], v14 offset:13056\n"
  "ds_read_b64_tr_b16 v[230:231], v14 offset:8960\n"
  "s_waitcnt lgkmcnt(6)\n"
  "v_mfma_f32_16x16x32_bf16 v[60:63], v[216:219], v[172:175], v[60:63]\n"
  "v_mfma_f32_16x16x32_bf16 v[92:95], v[216:219], v[188:191], v[92:95]\n"
  "s_waitcnt lgkmcnt(4)\n"
  "v_mfma_f32_16x16x32_bf16 v[64:67], v[220:223], v[172:175], v[64:67]\n"
  "v_mfma_f32_16x16x32_bf16 v[96:99], v[220:223], v[188:191], v[96:99]\n"
  "s_setprio 3\n"
  "s_waitcnt vmcnt(8) lgkmcnt(0)\n"
  "s_barrier\n"
  "ds_read_b128 v[200:203], v240 offset:4096\n"
  "v_mfma_f32_16x16x32_bf16 v[68:71], v[224:227], v[172:175], v[68:71]\n"
  "ds_read_b128 v[204:207], v240 offset:4352\n"
  "s_add_u32 m0, s51, 16384\n"
  "s_add_u32 s40, s50, 32768\n"
  "buffer_load_dwordx4 v10, %[srdv], s40 offen offset:0 nt lds\n"
  "v_mfma_f32_16x16x32_bf16 v[100:103], v[224:227], v[188:191], v[100:103]\n"
  "ds_read_b128 v[208:211], v240 offset:4608\n"
  "buffer_load_dwordx4 v11, %[srdv], s40 offen offset:1024 nt lds\n"
  "v_mfma_f32_16x16x32_bf16 v[72:75], v[228:231], v[172:175], v[72:75]\n"
  "ds_read_b128 v[212:215], v240 offset:4864\n"
  "buffer_load_dwordx4 v10, %[srdv], s40 offen offset:2048 nt lds\n"
  "v_mfma_f32_16x16x32_bf16 v[104:107], v[228:231], v[188:191], v[104:107]\n"
  "buffer_load_dwordx4 v11, %[srdv], s40 offen offset:3072 nt lds\n"
  "s_waitcnt lgkmcnt(3)\n"
  "v_mfma_f32_16x16x32_bf16 v[196:199], v[200:203], v[148:151], v[28:31]\n"
  "ds_read_b128 v[216:219], v240 offset:0\n"
  "v_mfma_f32_16x16x32_bf16 v[164:167], v[200:203], v[120:123], v[20:23]\n"
  "v_mfma_f32_16x16x32_bf16 v[180:183], v[200:203], v[136:139], v[24:27]\n"
  "ds_read_b128 v[220:223], v240 offset:256\n"
  "s_waitcnt lgkmcnt(4)\n"
  "v_mfma_f32_16x16x32_bf16 v[196:199], v[204:207], v[152:155], v[196:199]\n"
  "s_setprio 2\n"
  "v_mfma_f32_16x16x32_bf16 v[164:167], v[204:207], v[116:119], v[164:167]\n"
  "ds_read_b128 v[224:227], v240 offset:512\n"
  "v_mfma_f32_16x16x32_bf16 v[180:183], v[204:207], v[132:135], v[180:183]\n"
  "s_waitcnt lgkmcnt(4)\n"
  "v_mfma_f32_16x16x32_bf16 v[196:199], v[208:211], v[156:159], v[196:199]\n"
  "ds_read_b128 v[228:231], v240 offset:768\n"
  "v_mfma_f32_16x16x32_bf16 v[164:167], v[208:211], v[128:131], v[164:167]\n"
  "v_mfma_f32_16x16x32_bf16 v[180:183], v[208:211], v[144:147], v[180:183]\n"
  "s_waitcnt lgkmcnt(4)\n"
  "v_mfma_f32_16x16x32_bf16 v[196:199], v[212:215], v[160:163], v[196:199]\n"
  "v_mfma_f32_16x16x32_bf16 v[164:167], v[212:215], v[124:127], v[164:167]\n"
  "v_mfma_f32_16x16x32_bf16 v[180:183], v[212:215], v[140:143], v[180:183]\n"
  "s_setprio 1\n"
  "s_waitcnt lgkmcnt(3)\n"
  "v_mfma_f32_16x16x32_bf16 v[168:171], v[216:219], v[120:123], v[20:23]\n"
  "ds_read_b128 v[200:203], v240 offset:12288\n"
  "v_mfma_f32_16x16x32_bf16 v[184:187], v[216:219], v[136:139], v[24:27]\n"
  "s_nop 0\n"
  "v_exp_f32 v196, v196\n"
  "ds_read_b128 v[204:207], v240 offset:12544\n"
  "s_waitcnt lgkmcnt(4)\n"
  "v_mfma_f32_16x16x32_bf16 v[168:171], v[220:223], v[116:119], v[168:171]\n"
  "v_exp_f32 v197, v197\n"
  "ds_read_b128 v[208:211], v240 offset:12800\n"
  "v_mfma_f32_16x16x32_bf16 v[184:187], v[220:223], v[132:135], v[184:187]\n"
  "v_exp_f32 v198, v198\n"
  "ds_read_b128 v[212:215], v240 offset:13056\n"
  "s_waitcnt lgkmcnt(5)\n"
  "v_mfma_f32_16x16x32_bf16 v[168:171], v[224:227], v[128:131], v[168:171]\n"
  "v_exp_f32 v199, v199\n"
  "v_mfma_f32_16x16x32_bf16 v[184:187], v[224:227], v[144:147], v[184:187]\n"
  "v_add_f32 v18, v18, v196\n"
  "v_add_f32 v19, v19, v197\n"
  "s_waitcnt lgkmcnt(4)\n"
  "v_mfma_f32_16x16x32_bf16 v[168:171], v[228:231], v[124:127], v[168:171]\n"
  "v_cvt_pk_bf16_f32 v244, v196, v197\n"
  "v_cvt_pk_bf16_f32 v245, v198, v199\n"
  "ds_write_b64 v15, v[244:245] offset:0\n"
  "v_mfma_f32_16x16x32_bf16 v[184:187], v[228:231], v[140:143], v[184:187]\n"
  "v_add_f32 v18, v18, v198\n"
  "v_add_f32 v19, v19, v199\n"
  "s_setprio 0\n"
  "s_waitcnt lgkmcnt(4)\n"
  "v_mfma_f32_16x16x32_bf16 v[172:175], v[200:203], v[120:123], v[20:23]\n"
  "v_exp_f32 v164, v164\n"
  "ds_read_b128 v[216:219], v240 offset:8192\n"
  "v_mfma_f32_16x16x32_bf16 v[188:191], v[200:203], v[136:139], v[24:27]\n"
  "v_exp_f32 v165, v165\n"
  "ds_read_b128 v[220:223], v240 offset:8448\n"
  "s_waitcnt lgkmcnt(5)\n"
  "v_mfma_f32_16x16x32_bf16 v[172:175], v[204:207], v[116:119], v[172:175]\n"
  "v_exp_f32 v166, v166\n"
  "ds_read_b128 v[224:227], v240 offset:8704\n"
  "v_mfma_f32_16x16x32_bf16 v[188:191], v[204:207], v[132:135], v[188:191]\n"
  "v_exp_f32 v167, v167\n"
  "ds_read_b128 v[228:231], v240 offset:8960\n"
  "s_waitcnt lgkmcnt(6)\n"
  "v_mfma_f32_16x16x32_bf16 v[172:175], v[208:211], v[128:131], v[172:175]\n"
  "v_exp_f32 v180, v180\n"
  "v_mfma_f32_16x16x32_bf16 v[188:191], v[208:211], v[144:147], v[188:191]\n"
  "v_exp_f32 v181, v181\n"
  "s_waitcnt lgkmcnt(5)\n"
  "v_mfma_f32_16x16x32_bf16 v[172:175], v[212:215], v[124:127], v[172:175]\n"
  "v_exp_f32 v182, v182\n"
  "v_mfma_f32_16x16x32_bf16 v[188:191], v[212:215], v[140:143], v[188:191]\n"
  "v_exp_f32 v183, v183\n"
  "s_waitcnt vmcnt(8) lgkmcnt(0)\n"
  "s_barrier\n"
  "ds_read_b64_tr_b16 v[200:201], v241 offset:4096\n"
  "ds_read_b64_tr_b16 v[202:203], v241 offset:0\n"
  "s_setprio 3\n"
  "v_mfma_f32_16x16x32_bf16 v[176:179], v[216:219], v[120:123], v[20:23]\n"
  "v_exp_f32 v168, v168\n"
  "ds_read_b64_tr_b16 v[204:205], v242 offset:4096\n"
  "ds_read_b64_tr_b16 v[206:207], v242 offset:0\n"
  "s_add_u32 m0, s51, 65536\n"
  "s_add_u32 s40, s50, 49152\n"
  "buffer_load_dwordx4 v10, %[srdk], s40 offen offset:0 nt lds\n"
  "v_mfma_f32_16x16x32_bf16 v[192:195], v[216:219], v[136:139], v[24:27]\n"
  "v_exp_f32 v169, v169\n"
  "ds_read_b64_tr_b16 v[208:209], v241 offset:4352\n"
  "ds_read_b64_tr_b16 v[210:211], v241 offset:256\n"
  "v_mfma_f32_16x16x32_bf16 v[176:179], v[220:223], v[116:119], v[176:179]\n"
  "v_exp_f32 v170, v170\n"
  "ds_read_b64_tr_b16 v[212:213], v242 offset:4352\n"
  "ds_read_b64_tr_b16 v[214:215], v242 offset:256\n"
  "buffer_load_dwordx4 v11, %[srdk], s40 offen offset:1024 nt lds\n"
  "v_mfma_f32_16x16x32_bf16 v[192:195], v[220:223], v[132:135], v[192:195]\n"
  "v_exp_f32 v171, v171\n"
  "ds_read_b64 v[232:233], v16 offset:512\n"
  "ds_read_b64 v[234:235], v16 offset:0\n"
  "v_mfma_f32_16x16x32_bf16 v[176:179], v[224:227], v[128:131], v[176:179]\n"
  "v_cvt_pk_bf16_f32 v164, v164, v165\n"
  "v_cvt_pk_bf16_f32 v165, v166, v167\n"
  "ds_read_b64 v[236:237], v16 offset:1536\n"
  "ds_read_b64 v[238:239], v16 offset:1024\n"
  "buffer_load_dwordx4 v10, %[srdk], s40 offen offset:2048 nt lds\n"
  "v_mfma_f32_16x16x32_bf16 v[192:195], v[224:227], v[144:147], v[192:195]\n"
  "v_cvt_pk_bf16_f32 v166, v168, v169\n"
  "v_cvt_pk_bf16_f32 v167, v170, v171\n"
  "v_mfma_f32_16x16x32_bf16 v[176:179], v[228:231], v[124:127], v[176:179]\n"
  "v_exp_f32 v184, v184\n"
  "buffer_load_dwordx4 v11, %[srdk], s40 offen offset:3072 nt lds\n"
  "v_mfma_f32_16x16x32_bf16 v[192:195], v[228:231], v[140:143], v[192:195]\n"
  "v_exp_f32 v185, v185\n"
  "s_waitcnt lgkmcnt(10)\n"
  "v_mfma_f32_16x16x32_bf16 v[44:47], v[200:203], v[164:167], v[44:47]\n"
  "v_exp_f32 v186, v186\n"
  "v_mfma_f32_16x16x32_bf16 v[36:39], v[32:35], v[164:167], v[36:39]\n"
  "v_exp_f32 v187, v187\n"
  "v_cvt_pk_bf16_f32 v180, v180, v181\n"
  "v_cvt_pk_bf16_f32 v181, v182, v183\n"
  "v_cvt_pk_bf16_f32 v182, v184, v185\n"
  "v_cvt_pk_bf16_f32 v183, v186, v187\n"
  "s_nop 1\n"
  "v_mfma_f32_16x16x32_bf16 v[76:79], v[200:203], v[180:183], v[76:79]\n"
  "v_exp_f32 v172, v172\n"
  "s_setprio 2\n"
  "v_mfma_f32_16x16x32_bf16 v[40:43], v[32:35], v[180:183], v[40:43]\n"
  "v_exp_f32 v173, v173\n"
  "ds_read_b64_tr_b16 v[216:217], v241 offset:4608\n"
  "ds_read_b64_tr_b16 v[218:219], v241 offset:512\n"
  "s_waitcnt lgkmcnt(10)\n"
  "v_mfma_f32_16x16x32_bf16 v[48:51], v[204:207], v[164:167], v[48:51]\n"
  "v_exp_f32 v174, v174\n"
  "v_mfma_f32_16x16x32_bf16 v[80:83], v[204:207], v[180:183], v[80:83]\n"
  "v_exp_f32 v175, v175\n"
  "ds_read_b64_tr_b16 v[220:221], v242 offset:4608\n"
  "ds_read_b64_tr_b16 v[222:223], v242 offset:512\n"
  "s_waitcnt lgkmcnt(10)\n"
  "v_mfma_f32_16x16x32_bf16 v[52:55], v[208:211], v[164:167], v[52:55]\n"
  "v_exp_f32 v188, v188\n"
  "v_mfma_f32_16x16x32_bf16 v[84:87], v[208:211], v[180:183], v[84:87]\n"
  "v_exp_f32 v189, v189\n"
  "s_waitcnt lgkmcnt(6)\n"
  "v_mfma_f32_16x16x32_bf16 v[108:111], v[208:211], v[232:235], v[108:111]\n"
  "v_exp_f32 v190, v190\n"
  "ds_read_b64_tr_b16 v[224:225], v241 offset:4864\n"
  "ds_read_b64_tr_b16 v[226:227], v241 offset:768\n"
  "v_mfma_f32_16x16x32_bf16 v[56:59], v[212:215], v[164:167], v[56:59]\n"
  "v_exp_f32 v191, v191\n"
  "v_mfma_f32_16x16x32_bf16 v[88:91], v[212:215], v[180:183], v[88:91]\n"
  "v_exp_f32 v176, v176\n"
  "v_mfma_f32_16x16x32_bf16 v[112:115], v[212:215], v[232:235], v[112:115]\n"
  "v_exp_f32 v177, v177\n"
  "ds_read_b64_tr_b16 v[228:229], v242 offset:4864\n"
  "ds_read_b64_tr_b16 v[230:231], v242 offset:768\n"
  "s_waitcnt lgkmcnt(6)\n"
  "v_mfma_f32_16x16x32_bf16 v[60:63], v[216:219], v[164:167], v[60:63]\n"
  "v_exp_f32 v178, v178\n"
  "v_mfma_f32_16x16x32_bf16 v[92:95], v[216:219], v[180:183], v[92:95]\n"
  "v_exp_f32 v179, v179\n"
  "ds_read_b64_tr_b16 v[200:201], v241 offset:12288\n"
  "ds_read_b64_tr_b16 v[202:203], v241 offset:8192\n"
  "s_setprio 1\n"
  "s_waitcnt lgkmcnt(6)\n"
  "v_mfma_f32_16x16x32_bf16 v[64:67], v[220:223], v[164:167], v[64:67]\n"
  "v_cvt_pk_bf16_f32 v172, v172, v173\n"
  "v_cvt_pk_bf16_f32 v173, v174, v175\n"
  "v_mfma_f32_16x16x32_bf16 v[96:99], v[220:223], v[180:183], v[96:99]\n"
  "v_cvt_pk_bf16_f32 v174, v176, v177\n"
  "v_cvt_pk_bf16_f32 v175, v178, v179\n"
  "ds_read_b64_tr_b16 v[204:205], v242 offset:12288\n"
  "ds_read_b64_tr_b16 v[206:207], v242 offset:8192\n"
  "s_waitcnt lgkmcnt(6)\n"
  "v_mfma_f32_16x16x32_bf16 v[68:71], v[224:227], v[164:167], v[68:71]\n"
  "v_exp_f32 v192, v192\n"
  "v_mfma_f32_16x16x32_bf16 v[100:103], v[224:227], v[180:183], v[100:103]\n"
  "v_exp_f32 v193, v193\n"
  "ds_read_b64_tr_b16 v[208:209], v241 offset:12544\n"
  "ds_read_b64_tr_b16 v[210:211], v241 offset:8448\n"
  "s_waitcnt lgkmcnt(6)\n"
  "v_mfma_f32_16x16x32_bf16 v[72:75], v[228:231], v[164:167], v[72:75]\n"
  "v_exp_f32 v194, v194\n"
  "v_mfma_f32_16x16x32_bf16 v[104:107], v[228:231], v[180:183], v[104:107]\n"
  "v_exp_f32 v195, v195\n"
  "ds_read_b64_tr_b16 v[212:213], v242 offset:12544\n"
  "ds_read_b64_tr_b16 v[214:215], v242 offset:8448\n"
  "s_waitcnt lgkmcnt(6)\n"
  "v_mfma_f32_16x16x32_bf16 v[44:47], v[200:203], v[172:175], v[44:47]\n"
  "v_cvt_pk_bf16_f32 v188, v188, v189\n"
  "v_cvt_pk_bf16_f32 v189, v190, v191\n"
  "v_mfma_f32_16x16x32_bf16 v[36:39], v[32:35], v[172:175], v[36:39]\n"
  "v_cvt_pk_bf16_f32 v190, v192, v193\n"
  "v_cvt_pk_bf16_f32 v191, v194, v195\n"
  "s_nop 1\n"
  "v_mfma_f32_16x16x32_bf16 v[76:79], v[200:203], v[188:191], v[76:79]\n"
  "v_mfma_f32_16x16x32_bf16 v[40:43], v[32:35], v[188:191], v[40:43]\n"
  "ds_read_b64_tr_b16 v[216:217], v241 offset:12800\n"
  "ds_read_b64_tr_b16 v[218:219], v241 offset:8704\n"
  "s_waitcnt lgkmcnt(6)\n"
  "v_mfma_f32_16x16x32_bf16 v[48:51], v[204:207], v[172:175], v[48:51]\n"
  "s_setprio 0\n"
  "v_mfma_f32_16x16x32_bf16 v[80:83], v[204:207], v[188:191], v[80:83]\n"
  "ds_read_b64_tr_b16 v[220:221], v242 offset:12800\n"
  "ds_read_b64_tr_b16 v[222:223], v242 offset:8704\n"
  "s_waitcnt lgkmcnt(6)\n"
  "v_mfma_f32_16x16x32_bf16 v[52:55], v[208:211], v[172:175], v[52:55]\n"
  "v_mfma_f32_16x16x32_bf16 v[84:87], v[208:211], v[188:191], v[84:87]\n"
  "v_mfma_f32_16x16x32_bf16 v[108:111], v[208:211], v[236:239], v[108:111]\n"
  "ds_read_b64_tr_b16 v[224:225], v241 offset:13056\n"
  "ds_read_b64_tr_b16 v[226:227], v241 offset:8960\n"
  "s_waitcnt lgkmcnt(6)\n"
  "v_mfma_f32_16x16x32_bf16 v[56:59], v[212:215], v[172:175], v[56:59]\n"
  "v_mfma_f32_16x16x32_bf16 v[88:91], v[212:215], v[188:191], v[88:91]\n"
  "v_mfma_f32_16x16x32_bf16 v[112:115], v[212:215], v[236:239], v[112:115]\n"
  "ds_read_b64_tr_b16 v[228:229], v242 offset:13056\n"
  "ds_read_b64_tr_b16 v[230:231], v242 offset:8960\n"
  "s_waitcnt lgkmcnt(6)\n"
  "v_mfma_f32_16x16x32_bf16 v[60:63], v[216:219], v[172:175], v[60:63]\n"
  "v_mfma_f32_16x16x32_bf16 v[92:95], v[216:219], v[188:191], v[92:95]\n"
  "s_waitcnt lgkmcnt(4)\n"
  "v_mfma_f32_16x16x32_bf16 v[64:67], v[220:223], v[172:175], v[64:67]\n"
  "v_mfma_f32_16x16x32_bf16 v[96:99], v[220:223], v[188:191], v[96:99]\n"
  "s_setprio 3\n"
  "s_waitcnt vmcnt(8) lgkmcnt(0)\n"
  "s_barrier\n"
  "ds_read_b128 v[200:203], v12 offset:4096\n"
  "v_mfma_f32_16x16x32_bf16 v[68:71], v[224:227], v[172:175], v[68:71]\n"
  "ds_read_b128 v[204:207], v12 offset:4352\n"
  "s_add_u32 m0, s51, 81920\n"
  "s_add_u32 s40, s50, 49152\n"
  "buffer_load_dwordx4 v10, %[srdv], s40 offen offset:0 nt lds\n"
  "v_mfma_f32_16x16x32_bf16 v[100:103], v[224:227], v[188:191], v[100:103]\n"
  "ds_read_b128 v[208:211], v12 offset:4608\n"
  "buffer_load_dwordx4 v11, %[srdv], s40 offen offset:1024 nt lds\n"
  "v_mfma_f32_16x16x32_bf16 v[72:75], v[228:231], v[172:175], v[72:75]\n"
  "ds_read_b128 v[212:215], v12 offset:4864\n"
  "buffer_load_dwordx4 v10, %[srdv], s40 offen offset:2048 nt lds\n"
  "v_mfma_f32_16x16x32_bf16 v[104:107], v[228:231], v[188:191], v[104:107]\n"
  "buffer_load_dwordx4 v11, %[srdv], s40 offen offset:3072 nt lds\n"
  "s_add_u32 s50, s50, 32768\n"
  "s_sub_u32 s41, s41, 1\n"
  "s_cmp_lg_u32 s41, 0\n"
  "s_cbranch_scc1 P1_LOOP1\n"
  "v_lshrrev_b32 v3, 2, v1\n"
  "v_mul_u32_u24 v5, 1280, v3\n"
  "v_bfe_u32 v3, v1, 4, 1\n"
  "v_lshlrev_b32 v3, 1, v3\n"
  "v_and_b32 v7, 3, v1\n"
  "v_xor_b32 v3, v3, v7\n"
  "v_lshl_add_u32 v252, v3, 4, v5\n"
  "s_lshl_b32 s38, %[wave], 1\n"
  "s_and_b32 s39, s38, 3\n"
  "s_mul_i32 s39, s39, 20480\n"
  "s_lshr_b32 s40, s38, 2\n"
  "s_lshl_b32 s40, s40, 6\n"
  "s_add_u32 s52, s39, s40\n"
  "s_add_u32 s38, s38, 1\n"
  "s_and_b32 s39, s38, 3\n"
  "s_mul_i32 s39, s39, 20480\n"
  "s_lshr_b32 s40, s38, 2\n"
  "s_lshl_b32 s40, s40, 6\n"
  "s_add_u32 s53, s39, s40\n"
  "s_lshr_b32 s38, %[wave], 1\n"
  "s_add_u32 s38, s38, 16\n"
  "s_and_b32 s39, s38, 3\n"
  "s_mul_i32 s39, s39, 20480\n"
  "s_lshr_b32 s40, s38, 2\n"
  "s_lshl_b32 s40, s40, 6\n"
  "s_add_u32 s54, s39, s40\n"
  "s_lshl_b32 s44, s38, 10\n"
  "s_add_u32 s44, s44, %[ldsb]\n"
  "s_lshl_b32 s43, %[wave], 11\n"
  "s_add_u32 s43, s43, %[ldsb]\n"
  "s_and_b32 s38, %[wave], 1\n"
  "s_cmp_eq_u32 s38, 0\n"
  "s_cselect_b32 s48, -1, 0\n"
  "s_cselect_b32 s49, 0, -1\n"
  "s_mul_i32 s42, %[jx], 327680\n"
  "s_waitcnt lgkmcnt(3)\n"
  "v_mfma_f32_16x16x32_bf16 v[196:199], v[200:203], v[148:151], v[28:31]\n"
  "ds_read_b128 v[216:219], v12 offset:0\n"
  "v_mfma_f32_16x16x32_bf16 v[164:167], v[200:203], v[120:123], v[20:23]\n"
  "v_mfma_f32_16x16x32_bf16 v[180:183], v[200:203], v[136:139], v[24:27]\n"
  "ds_read_b128 v[220:223], v12 offset:256\n"
  "s_waitcnt lgkmcnt(4)\n"
  "v_mfma_f32_16x16x32_bf16 v[196:199], v[204:207], v[152:155], v[196:199]\n"
  "s_setprio 2\n"
  "v_mfma_f32_16x16x32_bf16 v[164:167], v[204:207], v[116:119], v[164:167]\n"
  "ds_read_b128 v[224:227], v12 offset:512\n"
  "v_mfma_f32_16x16x32_bf16 v[180:183], v[204:207], v[132:135], v[180:183]\n"
  "s_waitcnt lgkmcnt(4)\n"
  "v_mfma_f32_16x16x32_bf16 v[196:199], v[208:211], v[156:159], v[196:199]\n"
  "ds_read_b128 v[228:231], v12 offset:768\n"
  "v_mfma_f32_16x16x32_bf16 v[164:167], v[208:211], v[128:131], v[164:167]\n"
  "v_mfma_f32_16x16x32_bf16 v[180:183], v[208:211], v[144:147], v[180:183]\n"
  "s_waitcnt lgkmcnt(4)\n"
  "v_mfma_f32_16x16x32_bf16 v[196:199], v[212:215], v[160:163], v[196:199]\n"
  "v_mfma_f32_16x16x32_bf16 v[164:167], v[212:215], v[124:127], v[164:167]\n"
  "v_mfma_f32_16x16x32_bf16 v[180:183], v[212:215], v[140:143], v[180:183]\n"
  "s_setprio 1\n"
  "s_waitcnt lgkmcnt(3)\n"
  "v_mfma_f32_16x16x32_bf16 v[168:171], v[216:219], v[120:123], v[20:23]\n"
  "ds_read_b128 v[200:203], v12 offset:12288\n"
  "v_mfma_f32_16x16x32_bf16 v[184:187], v[216:219], v[136:139], v[24:27]\n"
  "s_nop 0\n"
  "v_exp_f32 v196, v196\n"
  "ds_read_b128 v[204:207], v12 offset:12544\n"
  "s_waitcnt lgkmcnt(4)\n"
  "v_mfma_f32_16x16x32_bf16 v[168:171], v[220:223], v[116:119], v[168:171]\n"
  "v_exp_f32 v197, v197\n"
  "ds_read_b128 v[208:211], v12 offset:12800\n"
  "v_mfma_f32_16x16x32_bf16 v[184:187], v[220:223], v[132:135], v[184:187]\n"
  "v_exp_f32 v198, v198\n"
  "ds_read_b128 v[212:215], v12 offset:13056\n"
  "s_waitcnt lgkmcnt(5)\n"
  "v_mfma_f32_16x16x32_bf16 v[168:171], v[224:227], v[128:131], v[168:171]\n"
  "v_exp_f32 v199, v199\n"
  "v_mfma_f32_16x16x32_bf16 v[184:187], v[224:227], v[144:147], v[184:187]\n"
  "v_add_f32 v18, v18, v196\n"
  "v_add_f32 v19, v19, v197\n"
  "s_waitcnt lgkmcnt(4)\n"
  "v_mfma_f32_16x16x32_bf16 v[168:171], v[228:231], v[124:127], v[168:171]\n"
  "v_cvt_pk_bf16_f32 v244, v196, v197\n"
  "v_cvt_pk_bf16_f32 v245, v198, v199\n"
  "ds_write_b64 v15, v[244:245] offset:0\n"
  "v_mfma_f32_16x16x32_bf16 v[184:187], v[228:231], v[140:143], v[184:187]\n"
  "v_add_f32 v18, v18, v198\n"
  "v_add_f32 v19, v19, v199\n"
  "s_setprio 0\n"
  "s_waitcnt lgkmcnt(4)\n"
  "v_mfma_f32_16x16x32_bf16 v[172:175], v[200:203], v[120:123], v[20:23]\n"
  "v_exp_f32 v164, v164\n"
  "ds_read_b128 v[216:219], v12 offset:8192\n"
  "v_mfma_f32_16x16x32_bf16 v[188:191], v[200:203], v[136:139], v[24:27]\n"
  "v_exp_f32 v165, v165\n"
  "ds_read_b128 v[220:223], v12 offset:8448\n"
  "s_waitcnt lgkmcnt(5)\n"
  "v_mfma_f32_16x16x32_bf16 v[172:175], v[204:207], v[116:119], v[172:175]\n"
  "v_exp_f32 v166, v166\n"
  "ds_read_b128 v[224:227], v12 offset:8704\n"
  "v_mfma_f32_16x16x32_bf16 v[188:191], v[204:207], v[132:135], v[188:191]\n"
  "v_exp_f32 v167, v167\n"
  "ds_read_b128 v[228:231], v12 offset:8960\n"
  "s_waitcnt lgkmcnt(6)\n"
  "v_mfma_f32_16x16x32_bf16 v[172:175], v[208:211], v[128:131], v[172:175]\n"
  "v_exp_f32 v180, v180\n"
  "v_mfma_f32_16x16x32_bf16 v[188:191], v[208:211], v[144:147], v[188:191]\n"
  "v_exp_f32 v181, v181\n"
  "s_waitcnt lgkmcnt(5)\n"
  "v_mfma_f32_16x16x32_bf16 v[172:175], v[212:215], v[124:127], v[172:175]\n"
  "v_exp_f32 v182, v182\n"
  "v_mfma_f32_16x16x32_bf16 v[188:191], v[212:215], v[140:143], v[188:191]\n"
  "v_exp_f32 v183, v183\n"
  "s_waitcnt vmcnt(8) lgkmcnt(0)\n"
  "s_barrier\n"
  "ds_read_b64_tr_b16 v[200:201], v13 offset:4096\n"
  "ds_read_b64_tr_b16 v[202:203], v13 offset:0\n"
  "s_setprio 3\n"
  "v_mfma_f32_16x16x32_bf16 v[176:179], v[216:219], v[120:123], v[20:23]\n"
  "v_exp_f32 v168, v168\n"
  "ds_read_b64_tr_b16 v[204:205], v14 offset:4096\n"
  "ds_read_b64_tr_b16 v[206:207], v14 offset:0\n"
  "v_mfma_f32_16x16x32_bf16 v[192:195], v[216:219], v[136:139], v[24:27]\n"
  "v_exp_f32 v169, v169\n"
  "ds_read_b64_tr_b16 v[208:209], v13 offset:4352\n"
  "ds_read_b64_tr_b16 v[210:211], v13 offset:256\n"
  "v_mfma_f32_16x16x32_bf16 v[176:179], v[220:223], v[116:119], v[176:179]\n"
  "v_exp_f32 v170, v170\n"
  "ds_read_b64_tr_b16 v[212:213], v14 offset:4352\n"
  "ds_read_b64_tr_b16 v[214:215], v14 offset:256\n"
  "v_mfma_f32_16x16x32_bf16 v[192:195], v[220:223], v[132:135], v[192:195]\n"
  "v_exp_f32 v171, v171\n"
  "ds_read_b64 v[232:233], v16 offset:512\n"
  "ds_read_b64 v[234:235], v16 offset:0\n"
  "v_mfma_f32_16x16x32_bf16 v[176:179], v[224:227], v[128:131], v[176:179]\n"
  "v_cvt_pk_bf16_f32 v164, v164, v165\n"
  "v_cvt_pk_bf16_f32 v165, v166, v167\n"
  "ds_read_b64 v[236:237], v16 offset:1536\n"
  "ds_read_b64 v[238:239], v16 offset:1024\n"
  "v_mfma_f32_16x16x32_bf16 v[192:195], v[224:227], v[144:147], v[192:195]\n"
  "v_cvt_pk_bf16_f32 v166, v168, v169\n"
  "v_cvt_pk_bf16_f32 v167, v170, v171\n"
  "v_mfma_f32_16x16x32_bf16 v[176:179], v[228:231], v[124:127], v[176:179]\n"
  "v_exp_f32 v184, v184\n"
  "v_mfma_f32_16x16x32_bf16 v[192:195], v[228:231], v[140:143], v[192:195]\n"
  "v_exp_f32 v185, v185\n"
  "s_waitcnt lgkmcnt(10)\n"
  "v_mfma_f32_16x16x32_bf16 v[44:47], v[200:203], v[164:167], v[44:47]\n"
  "v_exp_f32 v186, v186\n"
  "v_mfma_f32_16x16x32_bf16 v[36:39], v[32:35], v[164:167], v[36:39]\n"
  "v_exp_f32 v187, v187\n"
  "v_cvt_pk_bf16_f32 v180, v180, v181\n"
  "v_cvt_pk_bf16_f32 v181, v182, v183\n"
  "v_cvt_pk_bf16_f32 v182, v184, v185\n"
  "v_cvt_pk_bf16_f32 v183, v186, v187\n"
  "s_nop 1\n"
  "v_mfma_f32_16x16x32_bf16 v[76:79], v[200:203], v[180:183], v[76:79]\n"
  "v_exp_f32 v172, v172\n"
  "s_setprio 2\n"
  "v_mfma_f32_16x16x32_bf16 v[40:43], v[32:35], v[180:183], v[40:43]\n"
  "v_exp_f32 v173, v173\n"
  "ds_read_b64_tr_b16 v[216:217], v13 offset:4608\n"
  "ds_read_b64_tr_b16 v[218:219], v13 offset:512\n"
  "s_waitcnt lgkmcnt(10)\n"
  "v_mfma_f32_16x16x32_bf16 v[48:51], v[204:207], v[164:167], v[48:51]\n"
  "v_exp_f32 v174, v174\n"
  "v_mfma_f32_16x16x32_bf16 v[80:83], v[204:207], v[180:183], v[80:83]\n"
  "v_exp_f32 v175, v175\n"
  "ds_read_b64_tr_b16 v[220:221], v14 offset:4608\n"
  "ds_read_b64_tr_b16 v[222:223], v14 offset:512\n"
  "s_waitcnt lgkmcnt(10)\n"
  "v_mfma_f32_16x16x32_bf16 v[52:55], v[208:211], v[164:167], v[52:55]\n"
  "v_exp_f32 v188, v188\n"
  "v_mfma_f32_16x16x32_bf16 v[84:87], v[208:211], v[180:183], v[84:87]\n"
  "v_exp_f32 v189, v189\n"
  "s_waitcnt lgkmcnt(6)\n"
  "v_mfma_f32_16x16x32_bf16 v[108:111], v[208:211], v[232:235], v[108:111]\n"
  "v_exp_f32 v190, v190\n"
  "ds_read_b64_tr_b16 v[224:225], v13 offset:4864\n"
  "ds_read_b64_tr_b16 v[226:227], v13 offset:768\n"
  "v_mfma_f32_16x16x32_bf16 v[56:59], v[212:215], v[164:167], v[56:59]\n"
  "v_exp_f32 v191, v191\n"
  "v_mfma_f32_16x16x32_bf16 v[88:91], v[212:215], v[180:183], v[88:91]\n"
  "v_exp_f32 v176, v176\n"
  "v_mfma_f32_16x16x32_bf16 v[112:115], v[212:215], v[232:235], v[112:115]\n"
  "v_exp_f32 v177, v177\n"
  "ds_read_b64_tr_b16 v[228:229], v14 offset:4864\n"
  "ds_read_b64_tr_b16 v[230:231], v14 offset:768\n"
  "s_waitcnt lgkmcnt(6)\n"
  "v_mfma_f32_16x16x32_bf16 v[60:63], v[216:219], v[164:167], v[60:63]\n"
  "v_exp_f32 v178, v178\n"
  "v_mfma_f32_16x16x32_bf16 v[92:95], v[216:219], v[180:183], v[92:95]\n"
  "v_exp_f32 v179, v179\n"
  "ds_read_b64_tr_b16 v[200:201], v13 offset:12288\n"
  "ds_read_b64_tr_b16 v[202:203], v13 offset:8192\n"
  "s_setprio 1\n"
  "s_waitcnt lgkmcnt(6)\n"
  "v_mfma_f32_16x16x32_bf16 v[64:67], v[220:223], v[164:167], v[64:67]\n"
  "v_cvt_pk_bf16_f32 v172, v172, v173\n"
  "v_cvt_pk_bf16_f32 v173, v174, v175\n"
  "v_mfma_f32_16x16x32_bf16 v[96:99], v[220:223], v[180:183], v[96:99]\n"
  "v_cvt_pk_bf16_f32 v174, v176, v177\n"
  "v_cvt_pk_bf16_f32 v175, v178, v179\n"
  "ds_read_b64_tr_b16 v[204:205], v14 offset:12288\n"
  "ds_read_b64_tr_b16 v[206:207], v14 offset:8192\n"
  "s_waitcnt lgkmcnt(6)\n"
  "v_mfma_f32_16x16x32_bf16 v[68:71], v[224:227], v[164:167], v[68:71]\n"
  "v_exp_f32 v192, v192\n"
  "v_mfma_f32_16x16x32_bf16 v[100:103], v[224:227], v[180:183], v[100:103]\n"
  "v_exp_f32 v193, v193\n"
  "ds_read_b64_tr_b16 v[208:209], v13 offset:12544\n"
  "ds_read_b64_tr_b16 v[210:211], v13 offset:8448\n"
  "s_waitcnt lgkmcnt(6)\n"
  "v_mfma_f32_16x16x32_bf16 v[72:75], v[228:231], v[164:167], v[72:75]\n"
  "v_exp_f32 v194, v194\n"
  "v_mfma_f32_16x16x32_bf16 v[104:107], v[228:231], v[180:183], v[104:107]\n"
  "v_exp_f32 v195, v195\n"
  "ds_read_b64_tr_b16 v[212:213], v14 offset:12544\n"
  "ds_read_b64_tr_b16 v[214:215], v14 offset:8448\n"
  "s_waitcnt lgkmcnt(6)\n"
  "v_mfma_f32_16x16x32_bf16 v[44:47], v[200:203], v[172:175], v[44:47]\n"
  "v_cvt_pk_bf16_f32 v188, v188, v189\n"
  "v_cvt_pk_bf16_f32 v189, v190, v191\n"
  "v_mfma_f32_16x16x32_bf16 v[36:39], v[32:35], v[172:175], v[36:39]\n"
  "v_cvt_pk_bf16_f32 v190, v192, v193\n"
  "v_cvt_pk_bf16_f32 v191, v194, v195\n"
  "s_nop 1\n"
  "v_mfma_f32_16x16x32_bf16 v[76:79], v[200:203], v[188:191], v[76:79]\n"
  "v_mfma_f32_16x16x32_bf16 v[40:43], v[32:35], v[188:191], v[40:43]\n"
  "ds_read_b64_tr_b16 v[216:217], v13 offset:12800\n"
  "ds_read_b64_tr_b16 v[218:219], v13 offset:8704\n"
  "s_waitcnt lgkmcnt(6)\n"
  "v_mfma_f32_16x16x32_bf16 v[48:51], v[204:207], v[172:175], v[48:51]\n"
  "s_setprio 0\n"
  "v_mfma_f32_16x16x32_bf16 v[80:83], v[204:207], v[188:191], v[80:83]\n"
  "ds_read_b64_tr_b16 v[220:221], v14 offset:12800\n"
  "ds_read_b64_tr_b16 v[222:223], v14 offset:8704\n"
  "s_waitcnt lgkmcnt(6)\n"
  "v_mfma_f32_16x16x32_bf16 v[52:55], v[208:211], v[172:175], v[52:55]\n"
  "v_mfma_f32_16x16x32_bf16 v[84:87], v[208:211], v[188:191], v[84:87]\n"
  "v_mfma_f32_16x16x32_bf16 v[108:111], v[208:211], v[236:239], v[108:111]\n"
  "ds_read_b64_tr_b16 v[224:225], v13 offset:13056\n"
  "ds_read_b64_tr_b16 v[226:227], v13 offset:8960\n"
  "s_waitcnt lgkmcnt(6)\n"
  "v_mfma_f32_16x16x32_bf16 v[56:59], v[212:215], v[172:175], v[56:59]\n"
  "v_mfma_f32_16x16x32_bf16 v[88:91], v[212:215], v[188:191], v[88:91]\n"
  "v_mfma_f32_16x16x32_bf16 v[112:115], v[212:215], v[236:239], v[112:115]\n"
  "ds_read_b64_tr_b16 v[228:229], v14 offset:13056\n"
  "ds_read_b64_tr_b16 v[230:231], v14 offset:8960\n"
  "s_waitcnt lgkmcnt(6)\n"
  "v_mfma_f32_16x16x32_bf16 v[60:63], v[216:219], v[172:175], v[60:63]\n"
  "v_mfma_f32_16x16x32_bf16 v[92:95], v[216:219], v[188:191], v[92:95]\n"
  "s_waitcnt lgkmcnt(4)\n"
  "v_mfma_f32_16x16x32_bf16 v[64:67], v[220:223], v[172:175], v[64:67]\n"
  "v_mfma_f32_16x16x32_bf16 v[96:99], v[220:223], v[188:191], v[96:99]\n"
  "s_setprio 3\n"
  "s_waitcnt vmcnt(4) lgkmcnt(0)\n"
  "s_barrier\n"
  "ds_read_b128 v[200:203], v240 offset:4096\n"
  "v_mfma_f32_16x16x32_bf16 v[68:71], v[224:227], v[172:175], v[68:71]\n"
  "ds_read_b128 v[204:207], v240 offset:4352\n"
  "s_add_u32 m0, s43, 0\n"
  "s_add_u32 s40, s42, s52\n"
  "buffer_load_dwordx4 v252, %[srdwo], s40 offen offset:0 lds\n"
  "v_mfma_f32_16x16x32_bf16 v[100:103], v[224:227], v[188:191], v[100:103]\n"
  "ds_read_b128 v[208:211], v240 offset:4608\n"
  "s_add_u32 m0, s43, 1024\n"
  "s_add_u32 s40, s42, s53\n"
  "buffer_load_dwordx4 v252, %[srdwo], s40 offen offset:0 lds\n"
  "v_mfma_f32_16x16x32_bf16 v[72:75], v[228:231], v[172:175], v[72:75]\n"
  "ds_read_b128 v[212:215], v240 offset:4864\n"
  "s_add_u32 m0, s44, 0\n"
  "s_add_u32 s40, s42, s54\n"
  "s_mov_b64 exec, s[48:49]\n"
  "buffer_load_dwordx4 v252, %[srdwo], s40 offen offset:0 lds\n"
  "s_mov_b64 exec, -1\n"
  "s_add_u32 s42, s42, 81920\n"
  "s_cmp_ge_u32 s42, 1310720\n"
  "s_cselect_b32 s40, 1310720, 0\n"
  "s_sub_u32 s42, s42, s40\n"
  "v_mfma_f32_16x16x32_bf16 v[104:107], v[228:231], v[188:191], v[104:107]\n"
  "s_add_u32 m0, s43, 38912\n"
  "s_add_u32 s40, s42, s52\n"
  "buffer_load_dwordx4 v252, %[srdwo], s40 offen offset:0 lds\n"
  "s_add_u32 m0, s43, 39936\n"
  "s_add_u32 s40, s42, s53\n"
  "buffer_load_dwordx4 v252, %[srdwo], s40 offen offset:0 lds\n"
  "s_add_u32 m0, s44, 38912\n"
  "s_add_u32 s40, s42, s54\n"
  "s_mov_b64 exec, s[48:49]\n"
  "buffer_load_dwordx4 v252, %[srdwo], s40 offen offset:0 lds\n"
  "s_mov_b64 exec, -1\n"
  "s_add_u32 s42, s42, 81920\n"
  "s_cmp_ge_u32 s42, 1310720\n"
  "s_cselect_b32 s40, 1310720, 0\n"
  "s_sub_u32 s42, s42, s40\n"
  "s_waitcnt lgkmcnt(3)\n"
  "v_mfma_f32_16x16x32_bf16 v[196:199], v[200:203], v[148:151], v[28:31]\n"
  "ds_read_b128 v[216:219], v240 offset:0\n"
  "v_mfma_f32_16x16x32_bf16 v[164:167], v[200:203], v[120:123], v[20:23]\n"
  "v_mfma_f32_16x16x32_bf16 v[180:183], v[200:203], v[136:139], v[24:27]\n"
  "ds_read_b128 v[220:223], v240 offset:256\n"
  "s_waitcnt lgkmcnt(4)\n"
  "v_mfma_f32_16x16x32_bf16 v[196:199], v[204:207], v[152:155], v[196:199]\n"
  "s_setprio 2\n"
  "v_mfma_f32_16x16x32_bf16 v[164:167], v[204:207], v[116:119], v[164:167]\n"
  "ds_read_b128 v[224:227], v240 offset:512\n"
  "v_mfma_f32_16x16x32_bf16 v[180:183], v[204:207], v[132:135], v[180:183]\n"
  "s_waitcnt lgkmcnt(4)\n"
  "v_mfma_f32_16x16x32_bf16 v[196:199], v[208:211], v[156:159], v[196:199]\n"
  "ds_read_b128 v[228:231], v240 offset:768\n"
  "v_mfma_f32_16x16x32_bf16 v[164:167], v[208:211], v[128:131], v[164:167]\n"
  "v_mfma_f32_16x16x32_bf16 v[180:183], v[208:211], v[144:147], v[180:183]\n"
  "s_waitcnt lgkmcnt(4)\n"
  "v_mfma_f32_16x16x32_bf16 v[196:199], v[212:215], v[160:163], v[196:199]\n"
  "v_mfma_f32_16x16x32_bf16 v[164:167], v[212:215], v[124:127], v[164:167]\n"
  "v_mfma_f32_16x16x32_bf16 v[180:183], v[212:215], v[140:143], v[180:183]\n"
  "s_setprio 1\n"
  "s_waitcnt lgkmcnt(3)\n"
  "v_mfma_f32_16x16x32_bf16 v[168:171], v[216:219], v[120:123], v[20:23]\n"
  "ds_read_b128 v[200:203], v240 offset:12288\n"
  "v_mfma_f32_16x16x32_bf16 v[184:187], v[216:219], v[136:139], v[24:27]\n"
  "s_nop 0\n"
  "v_exp_f32 v196, v196\n"
  "ds_read_b128 v[204:207], v240 offset:12544\n"
  "s_waitcnt lgkmcnt(4)\n"
  "v_mfma_f32_16x16x32_bf16 v[168:171], v[220:223], v[116:119], v[168:171]\n"
  "v_exp_f32 v197, v197\n"
  "ds_read_b128 v[208:211], v240 offset:12800\n"
  "v_mfma_f32_16x16x32_bf16 v[184:187], v[220:223], v[132:135], v[184:187]\n"
  "v_exp_f32 v198, v198\n"
  "ds_read_b128 v[212:215], v240 offset:13056\n"
  "s_waitcnt lgkmcnt(5)\n"
  "v_mfma_f32_16x16x32_bf16 v[168:171], v[224:227], v[128:131], v[168:171]\n"
  "v_exp_f32 v199, v199\n"
  "v_mfma_f32_16x16x32_bf16 v[184:187], v[224:227], v[144:147], v[184:187]\n"
  "v_add_f32 v18, v18, v196\n"
  "v_add_f32 v19, v19, v197\n"
  "s_waitcnt lgkmcnt(4)\n"
  "v_mfma_f32_16x16x32_bf16 v[168:171], v[228:231], v[124:127], v[168:171]\n"
  "v_cvt_pk_bf16_f32 v244, v196, v197\n"
  "v_cvt_pk_bf16_f32 v245, v198, v199\n"
  "ds_write_b64 v15, v[244:245] offset:0\n"
  "v_mfma_f32_16x16x32_bf16 v[184:187], v[228:231], v[140:143], v[184:187]\n"
  "v_add_f32 v18, v18, v198\n"
  "v_add_f32 v19, v19, v199\n"
  "s_setprio 0\n"
  "s_waitcnt lgkmcnt(4)\n"
  "v_mfma_f32_16x16x32_bf16 v[172:175], v[200:203], v[120:123], v[20:23]\n"
  "v_exp_f32 v164, v164\n"
  "ds_read_b128 v[216:219], v240 offset:8192\n"
  "v_mfma_f32_16x16x32_bf16 v[188:191], v[200:203], v[136:139], v[24:27]\n"
  "v_exp_f32 v165, v165\n"
  "ds_read_b128 v[220:223], v240 offset:8448\n"
  "s_waitcnt lgkmcnt(5)\n"
  "v_mfma_f32_16x16x32_bf16 v[172:175], v[204:207], v[116:119], v[172:175]\n"
  "v_exp_f32 v166, v166\n"
  "ds_read_b128 v[224:227], v240 offset:8704\n"
  "v_mfma_f32_16x16x32_bf16 v[188:191], v[204:207], v[132:135], v[188:191]\n"
  "v_exp_f32 v167, v167\n"
  "ds_read_b128 v[228:231], v240 offset:8960\n"
  "s_waitcnt lgkmcnt(6)\n"
  "v_mfma_f32_16x16x32_bf16 v[172:175], v[208:211], v[128:131], v[172:175]\n"
  "v_exp_f32 v180, v180\n"
  "v_mfma_f32_16x16x32_bf16 v[188:191], v[208:211], v[144:147], v[188:191]\n"
  "v_exp_f32 v181, v181\n"
  "s_waitcnt lgkmcnt(5)\n"
  "v_mfma_f32_16x16x32_bf16 v[172:175], v[212:215], v[124:127], v[172:175]\n"
  "v_exp_f32 v182, v182\n"
  "v_mfma_f32_16x16x32_bf16 v[188:191], v[212:215], v[140:143], v[188:191]\n"
  "v_exp_f32 v183, v183\n"
  "s_waitcnt vmcnt(6) lgkmcnt(0)\n"
  "s_barrier\n"
  "ds_read_b64_tr_b16 v[200:201], v241 offset:4096\n"
  "ds_read_b64_tr_b16 v[202:203], v241 offset:0\n"
  "s_setprio 3\n"
  "v_mfma_f32_16x16x32_bf16 v[176:179], v[216:219], v[120:123], v[20:23]\n"
  "v_exp_f32 v168, v168\n"
  "ds_read_b64_tr_b16 v[204:205], v242 offset:4096\n"
  "ds_read_b64_tr_b16 v[206:207], v242 offset:0\n"
  "v_mfma_f32_16x16x32_bf16 v[192:195], v[216:219], v[136:139], v[24:27]\n"
  "v_exp_f32 v169, v169\n"
  "ds_read_b64_tr_b16 v[208:209], v241 offset:4352\n"
  "ds_read_b64_tr_b16 v[210:211], v241 offset:256\n"
  "v_mfma_f32_16x16x32_bf16 v[176:179], v[220:223], v[116:119], v[176:179]\n"
  "v_exp_f32 v170, v170\n"
  "ds_read_b64_tr_b16 v[212:213], v242 offset:4352\n"
  "ds_read_b64_tr_b16 v[214:215], v242 offset:256\n"
  "v_mfma_f32_16x16x32_bf16 v[192:195], v[220:223], v[132:135], v[192:195]\n"
  "v_exp_f32 v171, v171\n"
  "ds_read_b64 v[232:233], v16 offset:512\n"
  "ds_read_b64 v[234:235], v16 offset:0\n"
  "v_mfma_f32_16x16x32_bf16 v[176:179], v[224:227], v[128:131], v[176:179]\n"
  "v_cvt_pk_bf16_f32 v164, v164, v165\n"
  "v_cvt_pk_bf16_f32 v165, v166, v167\n"
  "ds_read_b64 v[236:237], v16 offset:1536\n"
  "ds_read_b64 v[238:239], v16 offset:1024\n"
  "v_mfma_f32_16x16x32_bf16 v[192:195], v[224:227], v[144:147], v[192:195]\n"
  "v_cvt_pk_bf16_f32 v166, v168, v169\n"
  "v_cvt_pk_bf16_f32 v167, v170, v171\n"
  "v_mfma_f32_16x16x32_bf16 v[176:179], v[228:231], v[124:127], v[176:179]\n"
  "v_exp_f32 v184, v184\n"
  "v_mfma_f32_16x16x32_bf16 v[192:195], v[228:231], v[140:143], v[192:195]\n"
  "v_exp_f32 v185, v185\n"
  "s_waitcnt lgkmcnt(10)\n"
  "v_mfma_f32_16x16x32_bf16 v[44:47], v[200:203], v[164:167], v[44:47]\n"
  "v_exp_f32 v186, v186\n"
  "v_mfma_f32_16x16x32_bf16 v[36:39], v[32:35], v[164:167], v[36:39]\n"
  "v_exp_f32 v187, v187\n"
  "v_cvt_pk_bf16_f32 v180, v180, v181\n"
  "v_cvt_pk_bf16_f32 v181, v182, v183\n"
  "v_cvt_pk_bf16_f32 v182, v184, v185\n"
  "v_cvt_pk_bf16_f32 v183, v186, v187\n"
  "s_nop 1\n"
  "v_mfma_f32_16x16x32_bf16 v[76:79], v[200:203], v[180:183], v[76:79]\n"
  "v_exp_f32 v172, v172\n"
  "s_setprio 2\n"
  "v_mfma_f32_16x16x32_bf16 v[40:43], v[32:35], v[180:183], v[40:43]\n"
  "v_exp_f32 v173, v173\n"
  "ds_read_b64_tr_b16 v[216:217], v241 offset:4608\n"
  "ds_read_b64_tr_b16 v[218:219], v241 offset:512\n"
  "s_waitcnt lgkmcnt(10)\n"
  "v_mfma_f32_16x16x32_bf16 v[48:51], v[204:207], v[164:167], v[48:51]\n"
  "v_exp_f32 v174, v174\n"
  "v_mfma_f32_16x16x32_bf16 v[80:83], v[204:207], v[180:183], v[80:83]\n"
  "v_exp_f32 v175, v175\n"
  "ds_read_b64_tr_b16 v[220:221], v242 offset:4608\n"
  "ds_read_b64_tr_b16 v[222:223], v242 offset:512\n"
  "s_waitcnt lgkmcnt(10)\n"
  "v_mfma_f32_16x16x32_bf16 v[52:55], v[208:211], v[164:167], v[52:55]\n"
  "v_exp_f32 v188, v188\n"
  "v_mfma_f32_16x16x32_bf16 v[84:87], v[208:211], v[180:183], v[84:87]\n"
  "v_exp_f32 v189, v189\n"
  "s_waitcnt lgkmcnt(6)\n"
  "v_mfma_f32_16x16x32_bf16 v[108:111], v[208:211], v[232:235], v[108:111]\n"
  "v_exp_f32 v190, v190\n"
  "ds_read_b64_tr_b16 v[224:225], v241 offset:4864\n"
  "ds_read_b64_tr_b16 v[226:227], v241 offset:768\n"
  "v_mfma_f32_16x16x32_bf16 v[56:59], v[212:215], v[164:167], v[56:59]\n"
  "v_exp_f32 v191, v191\n"
  "v_mfma_f32_16x16x32_bf16 v[88:91], v[212:215], v[180:183], v[88:91]\n"
  "v_exp_f32 v176, v176\n"
  "v_mfma_f32_16x16x32_bf16 v[112:115], v[212:215], v[232:235], v[112:115]\n"
  "v_exp_f32 v177, v177\n"
  "ds_read_b64_tr_b16 v[228:229], v242 offset:4864\n"
  "ds_read_b64_tr_b16 v[230:231], v242 offset:768\n"
  "s_waitcnt lgkmcnt(6)\n"
  "v_mfma_f32_16x16x32_bf16 v[60:63], v[216:219], v[164:167], v[60:63]\n"
  "v_exp_f32 v178, v178\n"
  "v_mfma_f32_16x16x32_bf16 v[92:95], v[216:219], v[180:183], v[92:95]\n"
  "v_exp_f32 v179, v179\n"
  "ds_read_b64_tr_b16 v[200:201], v241 offset:12288\n"
  "ds_read_b64_tr_b16 v[202:203], v241 offset:8192\n"
  "s_setprio 1\n"
  "s_waitcnt lgkmcnt(6)\n"
  "v_mfma_f32_16x16x32_bf16 v[64:67], v[220:223], v[164:167], v[64:67]\n"
  "v_cvt_pk_bf16_f32 v172, v172, v173\n"
  "v_cvt_pk_bf16_f32 v173, v174, v175\n"
  "v_mfma_f32_16x16x32_bf16 v[96:99], v[220:223], v[180:183], v[96:99]\n"
  "v_cvt_pk_bf16_f32 v174, v176, v177\n"
  "v_cvt_pk_bf16_f32 v175, v178, v179\n"
  "ds_read_b64_tr_b16 v[204:205], v242 offset:12288\n"
  "ds_read_b64_tr_b16 v[206:207], v242 offset:8192\n"
  "s_waitcnt lgkmcnt(6)\n"
  "v_mfma_f32_16x16x32_bf16 v[68:71], v[224:227], v[164:167], v[68:71]\n"
  "v_exp_f32 v192, v192\n"
  "v_mfma_f32_16x16x32_bf16 v[100:103], v[224:227], v[180:183], v[100:103]\n"
  "v_exp_f32 v193, v193\n"
  "ds_read_b64_tr_b16 v[208:209], v241 offset:12544\n"
  "ds_read_b64_tr_b16 v[210:211], v241 offset:8448\n"
  "s_waitcnt lgkmcnt(6)\n"
  "v_mfma_f32_16x16x32_bf16 v[72:75], v[228:231], v[164:167], v[72:75]\n"
  "v_exp_f32 v194, v194\n"
  "v_mfma_f32_16x16x32_bf16 v[104:107], v[228:231], v[180:183], v[104:107]\n"
  "v_exp_f32 v195, v195\n"
  "ds_read_b64_tr_b16 v[212:213], v242 offset:12544\n"
  "ds_read_b64_tr_b16 v[214:215], v242 offset:8448\n"
  "s_waitcnt lgkmcnt(6)\n"
  "v_mfma_f32_16x16x32_bf16 v[44:47], v[200:203], v[172:175], v[44:47]\n"
  "v_cvt_pk_bf16_f32 v188, v188, v189\n"
  "v_cvt_pk_bf16_f32 v189, v190, v191\n"
  "v_mfma_f32_16x16x32_bf16 v[36:39], v[32:35], v[172:175], v[36:39]\n"
  "v_cvt_pk_bf16_f32 v190, v192, v193\n"
  "v_cvt_pk_bf16_f32 v191, v194, v195\n"
  "s_nop 1\n"
  "v_mfma_f32_16x16x32_bf16 v[76:79], v[200:203], v[188:191], v[76:79]\n"
  "v_lshrrev_b32 v3, 2, v1\n"
  "v_lshlrev_b32 v3, 6, v3\n"
  "v_mfma_f32_16x16x32_bf16 v[40:43], v[32:35], v[188:191], v[40:43]\n"
  "v_bfe_u32 v5, v1, 1, 1\n"
  "v_lshl_add_u32 v3, v5, 4, v3\n"
  "ds_read_b64_tr_b16 v[216:217], v241 offset:12800\n"
  "ds_read_b64_tr_b16 v[218:219], v241 offset:8704\n"
  "s_waitcnt lgkmcnt(6)\n"
  "v_mfma_f32_16x16x32_bf16 v[48:51], v[204:207], v[172:175], v[48:51]\n"
  "v_and_b32 v5, 1, v1\n"
  "v_lshl_add_u32 v3, v5, 3, v3\n"
  "s_setprio 0\n"
  "v_mfma_f32_16x16x32_bf16 v[80:83], v[204:207], v[188:191], v[80:83]\n"
  "v_bfe_u32 v5, v1, 4, 1\n"
  "v_add_u32 v3, %[ldsb], v3\n"
  "ds_read_b64_tr_b16 v[220:221], v242 offset:12800\n"
  "ds_read_b64_tr_b16 v[222:223], v242 offset:8704\n"
  "s_waitcnt lgkmcnt(6)\n"
  "v_mfma_f32_16x16x32_bf16 v[52:55], v[208:211], v[172:175], v[52:55]\n"
  "s_add_u32 s38, s37, 0\n"
  "s_cmp_ge_u32 s38, 5\n"
  "s_cselect_b32 s39, 5, 0\n"
  "s_sub_u32 s38, s38, s39\n"
  "s_mul_i32 s39, s36, 5\n"
  "s_add_u32 s39, s39, s38\n"
  "s_and_b32 s38, s39, 1\n"
  "s_lshr_b32 s39, s39, 1\n"
  "s_lshl_b32 s39, s39, 12\n"
  "v_xor_b32 v7, s38, v5\n"
  "v_lshl_add_u32 v7, v7, 5, v3\n"
  "v_mfma_f32_16x16x32_bf16 v[84:87], v[208:211], v[188:191], v[84:87]\n"
  "v_add_u32 v20, s39, v7\n"
  "v_add_u32 v25, 77824, v20\n"
  "v_mfma_f32_16x16x32_bf16 v[108:111], v[208:211], v[236:239], v[108:111]\n"
  "s_add_u32 s38, s37, 1\n"
  "s_cmp_ge_u32 s38, 5\n"
  "s_cselect_b32 s39, 5, 0\n"
  "s_sub_u32 s38, s38, s39\n"
  "s_mul_i32 s39, s36, 5\n"
  "s_add_u32 s39, s39, s38\n"
  "s_and_b32 s38, s39, 1\n"
  "s_lshr_b32 s39, s39, 1\n"
  "s_lshl_b32 s39, s39, 12\n"
  "v_xor_b32 v7, s38, v5\n"
  "v_lshl_add_u32 v7, v7, 5, v3\n"
  "ds_read_b64_tr_b16 v[224:225], v241 offset:13056\n"
  "ds_read_b64_tr_b16 v[226:227], v241 offset:8960\n"
  "s_waitcnt lgkmcnt(6)\n"
  "v_mfma_f32_16x16x32_bf16 v[56:59], v[212:215], v[172:175], v[56:59]\n"
  "v_add_u32 v21, s39, v7\n"
  "v_add_u32 v26, 77824, v21\n"
  "v_mfma_f32_16x16x32_bf16 v[88:91], v[212:215], v[188:191], v[88:91]\n"
  "s_add_u32 s38, s37, 2\n"
  "s_cmp_ge_u32 s38, 5\n"
  "s_cselect_b32 s39, 5, 0\n"
  "s_sub_u32 s38, s38, s39\n"
  "s_mul_i32 s39, s36, 5\n"
  "s_add_u32 s39, s39, s38\n"
  "s_and_b32 s38, s39, 1\n"
  "s_lshr_b32 s39, s39, 1\n"
  "s_lshl_b32 s39, s39, 12\n"
  "v_xor_b32 v7, s38, v5\n"
  "v_lshl_add_u32 v7, v7, 5, v3\n"
  "v_mfma_f32_16x16x32_bf16 v[112:115], v[212:215], v[236:239], v[112:115]\n"
  "v_add_u32 v22, s39, v7\n"
  "v_add_u32 v27, 77824, v22\n"
  "ds_read_b64_tr_b16 v[228:229], v242 offset:13056\n"
  "ds_read_b64_tr_b16 v[230:231], v242 offset:8960\n"
  "s_waitcnt lgkmcnt(6)\n"
  "v_mfma_f32_16x16x32_bf16 v[60:63], v[216:219], v[172:175], v[60:63]\n"
  "s_add_u32 s38, s37, 3\n"
  "s_cmp_ge_u32 s38, 5\n"
  "s_cselect_b32 s39, 5, 0\n"
  "s_sub_u32 s38, s38, s39\n"
  "s_mul_i32 s39, s36, 5\n"
  "s_add_u32 s39, s39, s38\n"
  "s_and_b32 s38, s39, 1\n"
  "s_lshr_b32 s39, s39, 1\n"
  "s_lshl_b32 s39, s39, 12\n"
  "v_xor_b32 v7, s38, v5\n"
  "v_lshl_add_u32 v7, v7, 5, v3\n"
  "v_mfma_f32_16x16x32_bf16 v[92:95], v[216:219], v[188:191], v[92:95]\n"
  "v_add_u32 v23, s39, v7\n"
  "v_add_u32 v28, 77824, v23\n"
  "s_waitcnt lgkmcnt(4)\n"
  "v_mfma_f32_16x16x32_bf16 v[64:67], v[220:223], v[172:175], v[64:67]\n"
  "s_add_u32 s38, s37, 4\n"
  "s_cmp_ge_u32 s38, 5\n"
  "s_cselect_b32 s39, 5, 0\n"
  "s_sub_u32 s38, s38, s39\n"
  "s_mul_i32 s39, s36, 5\n"
  "s_add_u32 s39, s39, s38\n"
  "s_and_b32 s38, s39, 1\n"
  "s_lshr_b32 s39, s39, 1\n"
  "s_lshl_b32 s39, s39, 12\n"
  "v_xor_b32 v7, s38, v5\n"
  "v_lshl_add_u32 v7, v7, 5, v3\n"
  "v_mfma_f32_16x16x32_bf16 v[96:99], v[220:223], v[188:191], v[96:99]\n"
  "v_add_u32 v24, s39, v7\n"
  "v_add_u32 v29, 77824, v24\n"
  "s_setprio 3\n"
  "s_waitcnt lgkmcnt(2)\n"
  "v_mfma_f32_16x16x32_bf16 v[68:71], v[224:227], v[172:175], v[68:71]\n"
  "s_mov_b32 s38, 4\n"
  "s_mul_i32 s39, s36, 5\n"
  "s_add_u32 s39, s39, s38\n"
  "s_and_b32 s38, s39, 1\n"
  "s_lshr_b32 s39, s39, 1\n"
  "s_lshl_b32 s39, s39, 12\n"
  "v_xor_b32 v7, s38, v5\n"
  "v_lshl_add_u32 v7, v7, 5, v3\n"
  "v_mfma_f32_16x16x32_bf16 v[100:103], v[224:227], v[188:191], v[100:103]\n"
  "v_add_u32 v30, s39, v7\n"
  "v_add_u32 v31, 77824, v30\n"
  "s_waitcnt lgkmcnt(0)\n"
  "v_mfma_f32_16x16x32_bf16 v[72:75], v[228:231], v[172:175], v[72:75]\n"
  "s_lshl_b32 s38, s37, 12\n"
  "s_add_u32 s38, s38, 20480\n"
  "s_add_u32 s38, s38, %[ldsb]\n"
  "v_add_u32 v244, s38, v17\n"
  "v_add_u32 v245, 77824, v244\n"
  "v_mfma_f32_16x16x32_bf16 v[104:107], v[228:231], v[188:191], v[104:107]\n"
  "s_add_u32 s38, %[ldsb], 36864\n"
  "v_add_u32 v246, s38, v17\n"
  "v_add_u32 v247, 77824, v246\n"
  "s_mul_i32 s38, s36, 77824\n"
  "s_add_u32 s38, s38, 20480\n"
  "s_add_u32 s38, s38, %[ldsb]\n"
  "s_lshl_b32 s39, s37, 12\n"
  "s_add_u32 s39, s39, s38\n"
  "v_add_u32 v13, s39, v17\n"
  "s_mul_i32 s38, s36, 77824\n"
  "s_add_u32 s38, s38, 36864\n"
  "s_add_u32 s38, s38, %[ldsb]\n"
  "s_lshr_b32 s39, s37, 1\n"
  "s_mul_i32 s39, s39, 38912\n"
  "s_add_u32 s38, s38, s39\n"
  "s_and_b32 s39, s37, 1\n"
  "s_lshl_b32 s39, s39, 10\n"
  "s_add_u32 s38, s38, s39\n"
  "v_add_u32 v14, s38, v17\n"
  "v_and_b32 v7, 15, v1\n"
  "v_mul_u32_u24 v7, 2560, v7\n"
  "v_lshrrev_b32 v5, 4, v1\n"
  "v_lshl_add_u32 v15, v5, 3, v7\n"
  "v_mov_b64 v[248:249], 0\n"
  "v_max3_f32 v248, abs(v44), abs(v45), v248\n"
  "v_max3_f32 v249, abs(v46), abs(v47), v249\n"
  "v_max3_f32 v248, abs(v76), abs(v77), v248\n"
  "v_max3_f32 v249, abs(v78), abs(v79), v249\n"
  "v_max3_f32 v248, abs(v48), abs(v49), v248\n"
  "v_max3_f32 v249, abs(v50), abs(v51), v249\n"
  "v_max3_f32 v248, abs(v80), abs(v81), v248\n"
  "v_max3_f32 v249, abs(v82), abs(v83), v249\n"
  "v_max3_f32 v248, abs(v52), abs(v53), v248\n"
  "v_max3_f32 v249, abs(v54), abs(v55), v249\n"
  "v_max3_f32 v248, abs(v84), abs(v85), v248\n"
  "v_max3_f32 v249, abs(v86), abs(v87), v249\n"
  "v_max3_f32 v248, abs(v56), abs(v57), v248\n"
  "v_max3_f32 v249, abs(v58), abs(v59), v249\n"
  "v_max3_f32 v248, abs(v88), abs(v89), v248\n"
  "v_max3_f32 v249, abs(v90), abs(v91), v249\n"
  "v_max3_f32 v248, abs(v60), abs(v61), v248\n"
  "v_max3_f32 v249, abs(v62), abs(v63), v249\n"
  "v_max3_f32 v248, abs(v92), abs(v93), v248\n"
  "v_max3_f32 v249, abs(v94), abs(v95), v249\n"
  "v_max3_f32 v248, abs(v64), abs(v65), v248\n"
  "v_max3_f32 v249, abs(v66), abs(v67), v249\n"
  "v_max3_f32 v248, abs(v96), abs(v97), v248\n"
  "v_max3_f32 v249, abs(v98), abs(v99), v249\n"
  "v_max3_f32 v248, abs(v68), abs(v69), v248\n"
  "v_max3_f32 v249, abs(v70), abs(v71), v249\n"
  "v_max3_f32 v248, abs(v100), abs(v101), v248\n"
  "v_max3_f32 v249, abs(v102), abs(v103), v249\n"
  "v_max3_f32 v248, abs(v72), abs(v73), v248\n"
  "v_max3_f32 v249, abs(v74), abs(v75), v249\n"
  "v_max3_f32 v248, abs(v104), abs(v105), v248\n"
  "v_max3_f32 v249, abs(v106), abs(v107), v249\n"
  "v_max3_f32 v248, abs(v108), abs(v109), v248\n"
  "v_max3_f32 v249, abs(v110), abs(v111), v249\n"
  "v_max3_f32 v248, abs(v112), abs(v113), v248\n"
  "v_max3_f32 v249, abs(v114), abs(v115), v249\n"
  "s_branch P1_EPI\n"
  "P1_W2:\n"
  "ds_read_b128 v[216:219], v12 offset:0\n"
  "ds_read_b128 v[220:223], v12 offset:256\n"
  "ds_read_b128 v[224:227], v12 offset:512\n"
  "ds_read_b128 v[228:231], v12 offset:768\n"
  "s_waitcnt lgkmcnt(3)\n"
  "v_mfma_f32_16x16x32_bf16 v[196:199], v[216:219], v[148:151], 0\n"
  "v_mfma_f32_16x16x32_bf16 v[164:167], v[216:219], v[124:127], 0\n"
  "v_mfma_f32_16x16x32_bf16 v[180:183], v[216:219], v[140:143], 0\n"
  "s_waitcnt lgkmcnt(2)\n"
  "v_mfma_f32_16x16x32_bf16 v[196:199], v[220:223], v[152:155], v[196:199]\n"
  "v_mfma_f32_16x16x32_bf16 v[164:167], v[220:223], v[128:131], v[164:167]\n"
  "v_mfma_f32_16x16x32_bf16 v[180:183], v[220:223], v[144:147], v[180:183]\n"
  "s_waitcnt lgkmcnt(1)\n"
  "v_mfma_f32_16x16x32_bf16 v[196:199], v[224:227], v[156:159], v[196:199]\n"
  "v_mfma_f32_16x16x32_bf16 v[164:167], v[224:227], v[116:119], v[164:167]\n"
  "v_mfma_f32_16x16x32_bf16 v[180:183], v[224:227], v[132:135], v[180:183]\n"
  "s_waitcnt lgkmcnt(0)\n"
  "v_mfma_f32_16x16x32_bf16 v[196:199], v[228:231], v[160:163], v[196:199]\n"
  "v_mfma_f32_16x16x32_bf16 v[164:167], v[228:231], v[120:123], v[164:167]\n"
  "v_mfma_f32_16x16x32_bf16 v[180:183], v[228:231], v[136:139], v[180:183]\n"
  "s_nop 5\n"
  "v_max3_f32 v248, v196, v197, v198\n"
  "v_max_f32 v248, v248, v199\n"
  "v_mov_b32 v249, v248\n"
  "s_nop 1\n"
  "v_permlane32_swap_b32 v248, v249\n"
  "v_max_f32 v248, v248, v249\n"
  "v_mov_b32 v249, v248\n"
  "s_nop 1\n"
  "v_permlane16_swap_b32 v248, v249\n"
  "v_max_f32 v248, v248, v249\n"
  "v_sub_f32 v28, 0, v248\n"
  "v_mov_b32 v29, v28\n"
  "v_mov_b32 v30, v28\n"
  "v_mov_b32 v31, v28\n"
  "v_max3_f32 v248, v164, v165, v166\n"
  "v_max_f32 v248, v248, v167\n"
  "v_mov_b32 v249, v248\n"
  "s_nop 1\n"
  "v_permlane32_swap_b32 v248, v249\n"
  "v_max_f32 v248, v248, v249\n"
  "v_mov_b32 v249, v248\n"
  "s_nop 1\n"
  "v_permlane16_swap_b32 v248, v249\n"
  "v_max_f32 v248, v248, v249\n"
  "v_sub_f32 v20, 0, v248\n"
  "v_mov_b32 v21, v20\n"
  "v_mov_b32 v22, v20\n"
  "v_mov_b32 v23, v20\n"
  "v_max3_f32 v248, v180, v181, v182\n"
  "v_max_f32 v248, v248, v183\n"
  "v_mov_b32 v249, v248\n"
  "s_nop 1\n"
  "v_permlane32_swap_b32 v248, v249\n"
  "v_max_f32 v248, v248, v249\n"
  "v_mov_b32 v249, v248\n"
  "s_nop 1\n"
  "v_permlane16_swap_b32 v248, v249\n"
  "v_max_f32 v248, v248, v249\n"
  "v_sub_f32 v24, 0, v248\n"
  "v_mov_b32 v25, v24\n"
  "v_mov_b32 v26, v24\n"
  "v_mov_b32 v27, v24\n"
  "v_lshrrev_b32 v7, 2, v1\n"
  "v_mul_u32_u24 v7, 1280, v7\n"
  "v_and_b32 v8, 3, v1\n"
  "v_lshl_add_u32 v7, v8, 7, v7\n"
  "s_lshl_b32 s38, %[bb], 3\n"
  "s_add_u32 s38, s38, %[wave]\n"
  "s_mul_i32 s38, s38, 20480\n"
  "buffer_load_dword v254, v7, %[srdwo], s38 offen offset:0\n"
  "s_waitcnt lgkmcnt(0)\n"
  "s_nop 7\n"
  "s_nop 3\n"
  "P1_RST2:\n"
  "ds_read_b128 v[200:203], v12 offset:8192\n"
  "ds_read_b128 v[204:207], v12 offset:8448\n"
  "ds_read_b128 v[208:211], v12 offset:8704\n"
  "ds_read_b128 v[212:215], v12 offset:8960\n"
  "s_nop 7\n"
  "s_nop 3\n"
  "s_mov_b32 s41, 3\n"
  "P1_LOOP2:\n"
  "s_waitcnt lgkmcnt(3)\n"
  "v_mfma_f32_16x16x32_bf16 v[196:199], v[200:203], v[148:151], v[28:31]\n"
  "ds_read_b128 v[216:219], v12 offset:12288\n"
  "v_mfma_f32_16x16x32_bf16 v[164:167], v[200:203], v[124:127], v[20:23]\n"
  "v_mfma_f32_16x16x32_bf16 v[180:183], v[200:203], v[140:143], v[24:27]\n"
  "ds_read_b128 v[220:223], v12 offset:12544\n"
  "s_waitcnt lgkmcnt(4)\n"
  "v_mfma_f32_16x16x32_bf16 v[196:199], v[204:207], v[152:155], v[196:199]\n"
  "s_setprio 2\n"
  "v_mfma_f32_16x16x32_bf16 v[164:167], v[204:207], v[128:131], v[164:167]\n"
  "ds_read_b128 v[224:227], v12 offset:12800\n"
  "v_mfma_f32_16x16x32_bf16 v[180:183], v[204:207], v[144:147], v[180:183]\n"
  "s_waitcnt lgkmcnt(4)\n"
  "v_mfma_f32_16x16x32_bf16 v[196:199], v[208:211], v[156:159], v[196:199]\n"
  "ds_read_b128 v[228:231], v12 offset:13056\n"
  "v_mfma_f32_16x16x32_bf16 v[164:167], v[208:211], v[116:119], v[164:167]\n"
  "v_mfma_f32_16x16x32_bf16 v[180:183], v[208:211], v[132:135], v[180:183]\n"
  "s_waitcnt lgkmcnt(4)\n"
  "v_mfma_f32_16x16x32_bf16 v[196:199], v[212:215], v[160:163], v[196:199]\n"
  "v_mfma_f32_16x16x32_bf16 v[164:167], v[212:215], v[120:123], v[164:167]\n"
  "v_mfma_f32_16x16x32_bf16 v[180:183], v[212:215], v[136:139], v[180:183]\n"
  "s_setprio 1\n"
  "s_waitcnt lgkmcnt(3)\n"
  "v_mfma_f32_16x16x32_bf16 v[168:171], v[216:219], v[124:127], v[20:23]\n"
  "ds_read_b128 v[200:203], v12 offset:0\n"
  "v_mfma_f32_16x16x32_bf16 v[184:187], v[216:219], v[140:143], v[24:27]\n"
  "s_nop 0\n"
  "v_exp_f32 v196, v196\n"
  "ds_read_b128 v[204:207], v12 offset:256\n"
  "s_waitcnt lgkmcnt(4)\n"
  "v_mfma_f32_16x16x32_bf16 v[168:171], v[220:223], v[128:131], v[168:171]\n"
  "v_exp_f32 v197, v197\n"
  "ds_read_b128 v[208:211], v12 offset:512\n"
  "v_mfma_f32_16x16x32_bf16 v[184:187], v[220:223], v[144:147], v[184:187]\n"
  "v_exp_f32 v198, v198\n"
  "ds_read_b128 v[212:215], v12 offset:768\n"
  "s_waitcnt lgkmcnt(5)\n"
  "v_mfma_f32_16x16x32_bf16 v[168:171], v[224:227], v[116:119], v[168:171]\n"
  "v_exp_f32 v199, v199\n"
  "v_mfma_f32_16x16x32_bf16 v[184:187], v[224:227], v[132:135], v[184:187]\n"
  "v_add_f32 v18, v18, v196\n"
  "v_add_f32 v19, v19, v197\n"
  "s_waitcnt lgkmcnt(4)\n"
  "v_mfma_f32_16x16x32_bf16 v[168:171], v[228:231], v[120:123], v[168:171]\n"
  "v_cvt_pk_bf16_f32 v244, v196, v197\n"
  "v_cvt_pk_bf16_f32 v245, v198, v199\n"
  "ds_write_b64 v15, v[244:245] offset:0\n"
  "v_mfma_f32_16x16x32_bf16 v[184:187], v[228:231], v[136:139], v[184:187]\n"
  "v_add_f32 v18, v18, v198\n"
  "v_add_f32 v19, v19, v199\n"
  "s_setprio 0\n"
  "s_waitcnt lgkmcnt(4)\n"
  "v_mfma_f32_16x16x32_bf16 v[172:175], v[200:203], v[124:127], v[20:23]\n"
  "v_exp_f32 v164, v164\n"
  "ds_read_b128 v[216:219], v12 offset:4096\n"
  "v_mfma_f32_16x16x32_bf16 v[188:191], v[200:203], v[140:143], v[24:27]\n"
  "v_exp_f32 v165, v165\n"
  "ds_read_b128 v[220:223], v12 offset:4352\n"
  "s_waitcnt lgkmcnt(5)\n"
  "v_mfma_f32_16x16x32_bf16 v[172:175], v[204:207], v[128:131], v[172:175]\n"
  "v_exp_f32 v166, v166\n"
  "ds_read_b128 v[224:227], v12 offset:4608\n"
  "v_mfma_f32_16x16x32_bf16 v[188:191], v[204:207], v[144:147], v[188:191]\n"
  "v_exp_f32 v167, v167\n"
  "ds_read_b128 v[228:231], v12 offset:4864\n"
  "s_waitcnt lgkmcnt(6)\n"
  "v_mfma_f32_16x16x32_bf16 v[172:175], v[208:211], v[116:119], v[172:175]\n"
  "v_exp_f32 v180, v180\n"
  "v_mfma_f32_16x16x32_bf16 v[188:191], v[208:211], v[132:135], v[188:191]\n"
  "v_exp_f32 v181, v181\n"
  "s_waitcnt lgkmcnt(5)\n"
  "v_mfma_f32_16x16x32_bf16 v[172:175], v[212:215], v[120:123], v[172:175]\n"
  "v_exp_f32 v182, v182\n"
  "v_mfma_f32_16x16x32_bf16 v[188:191], v[212:215], v[136:139], v[188:191]\n"
  "v_exp_f32 v183, v183\n"
  "s_waitcnt vmcnt(8) lgkmcnt(0)\n"
  "s_barrier\n"
  "ds_read_b64_tr_b16 v[200:201], v13 offset:8192\n"
  "ds_read_b64_tr_b16 v[202:203], v13 offset:12288\n"
  "s_setprio 3\n"
  "v_mfma_f32_16x16x32_bf16 v[176:179], v[216:219], v[124:127], v[20:23]\n"
  "v_exp_f32 v168, v168\n"
  "ds_read_b64_tr_b16 v[204:205], v14 offset:8192\n"
  "ds_read_b64_tr_b16 v[206:207], v14 offset:12288\n"
  "s_add_u32 m0, s51, 0\n"
  "s_add_u32 s40, s50, 32768\n"
  "buffer_load_dwordx4 v10, %[srdk], s40 offen offset:0 nt lds\n"
  "v_mfma_f32_16x16x32_bf16 v[192:195], v[216:219], v[140:143], v[24:27]\n"
  "v_exp_f32 v169, v169\n"
  "ds_read_b64_tr_b16 v[208:209], v13 offset:8448\n"
  "ds_read_b64_tr_b16 v[210:211], v13 offset:12544\n"
  "v_mfma_f32_16x16x32_bf16 v[176:179], v[220:223], v[128:131], v[176:179]\n"
  "v_exp_f32 v170, v170\n"
  "ds_read_b64_tr_b16 v[212:213], v14 offset:8448\n"
  "ds_read_b64_tr_b16 v[214:215], v14 offset:12544\n"
  "buffer_load_dwordx4 v11, %[srdk], s40 offen offset:1024 nt lds\n"
  "v_mfma_f32_16x16x32_bf16 v[192:195], v[220:223], v[144:147], v[192:195]\n"
  "v_exp_f32 v171, v171\n"
  "ds_read_b64 v[232:233], v16 offset:1024\n"
  "ds_read_b64 v[234:235], v16 offset:1536\n"
  "v_mfma_f32_16x16x32_bf16 v[176:179], v[224:227], v[116:119], v[176:179]\n"
  "v_cvt_pk_bf16_f32 v164, v164, v165\n"
  "v_cvt_pk_bf16_f32 v165, v166, v167\n"
  "ds_read_b64 v[236:237], v16 offset:0\n"
  "ds_read_b64 v[238:239], v16 offset:512\n"
  "buffer_load_dwordx4 v10, %[srdk], s40 offen offset:2048 nt lds\n"
  "v_mfma_f32_16x16x32_bf16 v[192:195], v[224:227], v[132:135], v[192:195]\n"
  "v_cvt_pk_bf16_f32 v166, v168, v169\n"
  "v_cvt_pk_bf16_f32 v167, v170, v171\n"
  "v_mfma_f32_16x16x32_bf16 v[176:179], v[228:231], v[120:123], v[176:179]\n"
  "v_exp_f32 v184, v184\n"
  "buffer_load_dwordx4 v11, %[srdk], s40 offen offset:3072 nt lds\n"
  "v_mfma_f32_16x16x32_bf16 v[192:195], v[228:231], v[136:139], v[192:195]\n"
  "v_exp_f32 v185, v185\n"
  "s_waitcnt lgkmcnt(10)\n"
  "v_mfma_f32_16x16x32_bf16 v[44:47], v[200:203], v[164:167], v[44:47]\n"
  "v_exp_f32 v186, v186\n"
  "v_mfma_f32_16x16x32_bf16 v[36:39], v[32:35], v[164:167], v[36:39]\n"
  "v_exp_f32 v187, v187\n"
  "v_cvt_pk_bf16_f32 v180, v180, v181\n"
  "v_cvt_pk_bf16_f32 v181, v182, v183\n"
  "v_cvt_pk_bf16_f32 v182, v184, v185\n"
  "v_cvt_pk_bf16_f32 v183, v186, v187\n"
  "s_nop 1\n"
  "v_mfma_f32_16x16x32_bf16 v[76:79], v[200:203], v[180:183], v[76:79]\n"
  "v_exp_f32 v172, v172\n"
  "s_setprio 2\n"
  "v_mfma_f32_16x16x32_bf16 v[40:43], v[32:35], v[180:183], v[40:43]\n"
  "v_exp_f32 v173, v173\n"
  "ds_read_b64_tr_b16 v[216:217], v13 offset:8704\n"
  "ds_read_b64_tr_b16 v[218:219], v13 offset:12800\n"
  "s_waitcnt lgkmcnt(10)\n"
  "v_mfma_f32_16x16x32_bf16 v[48:51], v[204:207], v[164:167], v[48:51]\n"
  "v_exp_f32 v174, v174\n"
  "v_mfma_f32_16x16x32_bf16 v[80:83], v[204:207], v[180:183], v[80:83]\n"
  "v_exp_f32 v175, v175\n"
  "ds_read_b64_tr_b16 v[220:221], v14 offset:8704\n"
  "ds_read_b64_tr_b16 v[222:223], v14 offset:12800\n"
  "s_waitcnt lgkmcnt(10)\n"
  "v_mfma_f32_16x16x32_bf16 v[52:55], v[208:211], v[164:167], v[52:55]\n"
  "v_exp_f32 v188, v188\n"
  "v_mfma_f32_16x16x32_bf16 v[84:87], v[208:211], v[180:183], v[84:87]\n"
  "v_exp_f32 v189, v189\n"
  "ds_read_b64_tr_b16 v[224:225], v13 offset:8960\n"
  "ds_read_b64_tr_b16 v[226:227], v13 offset:13056\n"
  "s_waitcnt lgkmcnt(10)\n"
  "v_mfma_f32_16x16x32_bf16 v[56:59], v[212:215], v[164:167], v[56:59]\n"
  "v_exp_f32 v190, v190\n"
  "v_mfma_f32_16x16x32_bf16 v[88:91], v[212:215], v[180:183], v[88:91]\n"
  "v_exp_f32 v191, v191\n"
  "ds_read_b64_tr_b16 v[228:229], v14 offset:8960\n"
  "ds_read_b64_tr_b16 v[230:231], v14 offset:13056\n"
  "s_waitcnt lgkmcnt(6)\n"
  "v_mfma_f32_16x16x32_bf16 v[60:63], v[216:219], v[164:167], v[60:63]\n"
  "v_exp_f32 v176, v176\n"
  "v_mfma_f32_16x16x32_bf16 v[92:95], v[216:219], v[180:183], v[92:95]\n"
  "v_exp_f32 v177, v177\n"
  "v_mfma_f32_16x16x32_bf16 v[108:111], v[216:219], v[232:235], v[108:111]\n"
  "v_exp_f32 v178, v178\n"
  "ds_read_b64_tr_b16 v[200:201], v13 offset:0\n"
  "ds_read_b64_tr_b16 v[202:203], v13 offset:4096\n"
  "s_waitcnt lgkmcnt(6)\n"
  "v_mfma_f32_16x16x32_bf16 v[64:67], v[220:223], v[164:167], v[64:67]\n"
  "v_exp_f32 v179, v179\n"
  "s_setprio 1\n"
  "v_mfma_f32_16x16x32_bf16 v[96:99], v[220:223], v[180:183], v[96:99]\n"
  "v_cvt_pk_bf16_f32 v172, v172, v173\n"
  "v_cvt_pk_bf16_f32 v173, v174, v175\n"
  "v_mfma_f32_16x16x32_bf16 v[112:115], v[220:223], v[232:235], v[112:115]\n"
  "v_cvt_pk_bf16_f32 v174, v176, v177\n"
  "v_cvt_pk_bf16_f32 v175, v178, v179\n"
  "ds_read_b64_tr_b16 v[204:205], v14 offset:0\n"
  "ds_read_b64_tr_b16 v[206:207], v14 offset:4096\n"
  "s_waitcnt lgkmcnt(6)\n"
  "v_mfma_f32_16x16x32_bf16 v[68:71], v[224:227], v[164:167], v[68:71]\n"
  "v_exp_f32 v192, v192\n"
  "v_mfma_f32_16x16x32_bf16 v[100:103], v[224:227], v[180:183], v[100:103]\n"
  "v_exp_f32 v193, v193\n"
  "ds_read_b64_tr_b16 v[208:209], v13 offset:256\n"
  "ds_read_b64_tr_b16 v[210:211], v13 offset:4352\n"
  "s_waitcnt lgkmcnt(6)\n"
  "v_mfma_f32_16x16x32_bf16 v[72:75], v[228:231], v[164:167], v[72:75]\n"
  "v_exp_f32 v194, v194\n"
  "v_mfma_f32_16x16x32_bf16 v[104:107], v[228:231], v[180:183], v[104:107]\n"
  "v_exp_f32 v195, v195\n"
  "ds_read_b64_tr_b16 v[212:213], v14 offset:256\n"
  "ds_read_b64_tr_b16 v[214:215], v14 offset:4352\n"
  "s_waitcnt lgkmcnt(6)\n"
  "v_mfma_f32_16x16x32_bf16 v[44:47], v[200:203], v[172:175], v[44:47]\n"
  "v_cvt_pk_bf16_f32 v188, v188, v189\n"
  "v_cvt_pk_bf16_f32 v189, v190, v191\n"
  "v_mfma_f32_16x16x32_bf16 v[36:39], v[32:35], v[172:175], v[36:39]\n"
  "v_cvt_pk_bf16_f32 v190, v192, v193\n"
  "v_cvt_pk_bf16_f32 v191, v194, v195\n"
  "s_nop 1\n"
  "v_mfma_f32_16x16x32_bf16 v[76:79], v[200:203], v[188:191], v[76:79]\n"
  "v_mfma_f32_16x16x32_bf16 v[40:43], v[32:35], v[188:191], v[40:43]\n"
  "ds_read_b64_tr_b16 v[216:217], v13 offset:512\n"
  "ds_read_b64_tr_b16 v[218:219], v13 offset:4608\n"
  "s_waitcnt lgkmcnt(6)\n"
  "v_mfma_f32_16x16x32_bf16 v[48:51], v[204:207], v[172:175], v[48:51]\n"
  "s_setprio 0\n"
  "v_mfma_f32_16x16x32_bf16 v[80:83], v[204:207], v[188:191], v[80:83]\n"
  "ds_read_b64_tr_b16 v[220:221], v14 offset:512\n"
  "ds_read_b64_tr_b16 v[222:223], v14 offset:4608\n"
  "s_waitcnt lgkmcnt(6)\n"
  "v_mfma_f32_16x16x32_bf16 v[52:55], v[208:211], v[172:175], v[52:55]\n"
  "v_mfma_f32_16x16x32_bf16 v[84:87], v[208:211], v[188:191], v[84:87]\n"
  "ds_read_b64_tr_b16 v[224:225], v13 offset:768\n"
  "ds_read_b64_tr_b16 v[226:227], v13 offset:4864\n"
  "s_waitcnt lgkmcnt(6)\n"
  "v_mfma_f32_16x16x32_bf16 v[56:59], v[212:215], v[172:175], v[56:59]\n"
  "v_mfma_f32_16x16x32_bf16 v[88:91], v[212:215], v[188:191], v[88:91]\n"
  "ds_read_b64_tr_b16 v[228:229], v14 offset:768\n"
  "ds_read_b64_tr_b16 v[230:231], v14 offset:4864\n"
  "s_waitcnt lgkmcnt(6)\n"
  "v_mfma_f32_16x16x32_bf16 v[60:63], v[216:219], v[172:175], v[60:63]\n"
  "v_mfma_f32_16x16x32_bf16 v[92:95], v[216:219], v[188:191], v[92:95]\n"
  "v_mfma_f32_16x16x32_bf16 v[108:111], v[216:219], v[236:239], v[108:111]\n"
  "s_waitcnt lgkmcnt(4)\n"
  "v_mfma_f32_16x16x32_bf16 v[64:67], v[220:223], v[172:175], v[64:67]\n"
  "v_mfma_f32_16x16x32_bf16 v[96:99], v[220:223], v[188:191], v[96:99]\n"
  "v_mfma_f32_16x16x32_bf16 v[112:115], v[220:223], v[236:239], v[112:115]\n"
  "s_setprio 3\n"
  "s_waitcnt vmcnt(8) lgkmcnt(0)\n"
  "s_barrier\n"
  "ds_read_b128 v[200:203], v240 offset:8192\n"
  "v_mfma_f32_16x16x32_bf16 v[68:71], v[224:227], v[172:175], v[68:71]\n"
  "ds_read_b128 v[204:207], v240 offset:8448\n"
  "s_add_u32 m0, s51, 16384\n"
  "s_add_u32 s40, s50, 32768\n"
  "buffer_load_dwordx4 v10, %[srdv], s40 offen offset:0 nt lds\n"
  "v_mfma_f32_16x16x32_bf16 v[100:103], v[224:227], v[188:191], v[100:103]\n"
  "ds_read_b128 v[208:211], v240 offset:8704\n"
  "buffer_load_dwordx4 v11, %[srdv], s40 offen offset:1024 nt lds\n"
  "v_mfma_f32_16x16x32_bf16 v[72:75], v[228:231], v[172:175], v[72:75]\n"
  "ds_read_b128 v[212:215], v240 offset:8960\n"
  "buffer_load_dwordx4 v10, %[srdv], s40 offen offset:2048 nt lds\n"
  "v_mfma_f32_16x16x32_bf16 v[104:107], v[228:231], v[188:191], v[104:107]\n"
  "buffer_load_dwordx4 v11, %[srdv], s40 offen offset:3072 nt lds\n"
  "s_waitcnt lgkmcnt(3)\n"
  "v_mfma_f32_16x16x32_bf16 v[196:199], v[200:203], v[148:151], v[28:31]\n"
  "ds_read_b128 v[216:219], v240 offset:12288\n"
  "v_mfma_f32_16x16x32_bf16 v[164:167], v[200:203], v[124:127], v[20:23]\n"
  "v_mfma_f32_16x16x32_bf16 v[180:183], v[200:203], v[140:143], v[24:27]\n"
  "ds_read_b128 v[220:223], v240 offset:12544\n"
  "s_waitcnt lgkmcnt(4)\n"
  "v_mfma_f32_16x16x32_bf16 v[196:199], v[204:207], v[152:155], v[196:199]\n"
  "s_setprio 2\n"
  "v_mfma_f32_16x16x32_bf16 v[164:167], v[204:207], v[128:131], v[164:167]\n"
  "ds_read_b128 v[224:227], v240 offset:12800\n"
  "v_mfma_f32_16x16x32_bf16 v[180:183], v[204:207], v[144:147], v[180:183]\n"
  "s_waitcnt lgkmcnt(4)\n"
  "v_mfma_f32_16x16x32_bf16 v[196:199], v[208:211], v[156:159], v[196:199]\n"
  "ds_read_b128 v[228:231], v240 offset:13056\n"
  "v_mfma_f32_16x16x32_bf16 v[164:167], v[208:211], v[116:119], v[164:167]\n"
  "v_mfma_f32_16x16x32_bf16 v[180:183], v[208:211], v[132:135], v[180:183]\n"
  "s_waitcnt lgkmcnt(4)\n"
  "v_mfma_f32_16x16x32_bf16 v[196:199], v[212:215], v[160:163], v[196:199]\n"
  "v_mfma_f32_16x16x32_bf16 v[164:167], v[212:215], v[120:123], v[164:167]\n"
  "v_mfma_f32_16x16x32_bf16 v[180:183], v[212:215], v[136:139], v[180:183]\n"
  "s_setprio 1\n"
  "s_waitcnt lgkmcnt(3)\n"
  "v_mfma_f32_16x16x32_bf16 v[168:171], v[216:219], v[124:127], v[20:23]\n"
  "ds_read_b128 v[200:203], v240 offset:0\n"
  "v_mfma_f32_16x16x32_bf16 v[184:187], v[216:219], v[140:143], v[24:27]\n"
  "s_nop 0\n"
  "v_exp_f32 v196, v196\n"
  "ds_read_b128 v[204:207], v240 offset:256\n"
  "s_waitcnt lgkmcnt(4)\n"
  "v_mfma_f32_16x16x32_bf16 v[168:171], v[220:223], v[128:131], v[168:171]\n"
  "v_exp_f32 v197, v197\n"
  "ds_read_b128 v[208:211], v240 offset:512\n"
  "v_mfma_f32_16x16x32_bf16 v[184:187], v[220:223], v[144:147], v[184:187]\n"
  "v_exp_f32 v198, v198\n"
  "ds_read_b128 v[212:215], v240 offset:768\n"
  "s_waitcnt lgkmcnt(5)\n"
  "v_mfma_f32_16x16x32_bf16 v[168:171], v[224:227], v[116:119], v[168:171]\n"
  "v_exp_f32 v199, v199\n"
  "v_mfma_f32_16x16x32_bf16 v[184:187], v[224:227], v[132:135], v[184:187]\n"
  "v_add_f32 v18, v18, v196\n"
  "v_add_f32 v19, v19, v197\n"
  "s_waitcnt lgkmcnt(4)\n"
  "v_mfma_f32_16x16x32_bf16 v[168:171], v[228:231], v[120:123], v[168:171]\n"
  "v_cvt_pk_bf16_f32 v244, v196, v197\n"
  "v_cvt_pk_bf16_f32 v245, v198, v199\n"
  "ds_write_b64 v15, v[244:245] offset:0\n"
  "v_mfma_f32_16x16x32_bf16 v[184:187], v[228:231], v[136:139], v[184:187]\n"
  "v_add_f32 v18, v18, v198\n"
  "v_add_f32 v19, v19, v199\n"
  "s_setprio 0\n"
  "s_waitcnt lgkmcnt(4)\n"
  "v_mfma_f32_16x16x32_bf16 v[172:175], v[200:203], v[124:127], v[20:23]\n"
  "v_exp_f32 v164, v164\n"
  "ds_read_b128 v[216:219], v240 offset:4096\n"
  "v_mfma_f32_16x16x32_bf16 v[188:191], v[200:203], v[140:143], v[24:27]\n"
  "v_exp_f32 v165, v165\n"
  "ds_read_b128 v[220:223], v240 offset:4352\n"
  "s_waitcnt lgkmcnt(5)\n"
  "v_mfma_f32_16x16x32_bf16 v[172:175], v[204:207], v[128:131], v[172:175]\n"
  "v_exp_f32 v166, v166\n"
  "ds_read_b128 v[224:227], v240 offset:4608\n"
  "v_mfma_f32_16x16x32_bf16 v[188:191], v[204:207], v[144:147], v[188:191]\n"
  "v_exp_f32 v167, v167\n"
  "ds_read_b128 v[228:231], v240 offset:4864\n"
  "s_waitcnt lgkmcnt(6)\n"
  "v_mfma_f32_16x16x32_bf16 v[172:175], v[208:211], v[116:119], v[172:175]\n"
  "v_exp_f32 v180, v180\n"
  "v_mfma_f32_16x16x32_bf16 v[188:191], v[208:211], v[132:135], v[188:191]\n"
  "v_exp_f32 v181, v181\n"
  "s_waitcnt lgkmcnt(5)\n"
  "v_mfma_f32_16x16x32_bf16 v[172:175], v[212:215], v[120:123], v[172:175]\n"
  "v_exp_f32 v182, v182\n"
  "v_mfma_f32_16x16x32_bf16 v[188:191], v[212:215], v[136:139], v[188:191]\n"
  "v_exp_f32 v183, v183\n"
  "s_waitcnt vmcnt(8) lgkmcnt(0)\n"
  "s_barrier\n"
  "ds_read_b64_tr_b16 v[200:201], v241 offset:8192\n"
  "ds_read_b64_tr_b16 v[202:203], v241 offset:12288\n"
  "s_setprio 3\n"
  "v_mfma_f32_16x16x32_bf16 v[176:179], v[216:219], v[124:127], v[20:23]\n"
  "v_exp_f32 v168, v168\n"
  "ds_read_b64_tr_b16 v[204:205], v242 offset:8192\n"
  "ds_read_b64_tr_b16 v[206:207], v242 offset:12288\n"
  "s_add_u32 m0, s51, 65536\n"
  "s_add_u32 s40, s50, 49152\n"
  "buffer_load_dwordx4 v10, %[srdk], s40 offen offset:0 nt lds\n"
  "v_mfma_f32_16x16x32_bf16 v[192:195], v[216:219], v[140:143], v[24:27]\n"
  "v_exp_f32 v169, v169\n"
  "ds_read_b64_tr_b16 v[208:209], v241 offset:8448\n"
  "ds_read_b64_tr_b16 v[210:211], v241 offset:12544\n"
  "v_mfma_f32_16x16x32_bf16 v[176:179], v[220:223], v[128:131], v[176:179]\n"
  "v_exp_f32 v170, v170\n"
  "ds_read_b64_tr_b16 v[212:213], v242 offset:8448\n"
  "ds_read_b64_tr_b16 v[214:215], v242 offset:12544\n"
  "buffer_load_dwordx4 v11, %[srdk], s40 offen offset:1024 nt lds\n"
  "v_mfma_f32_16x16x32_bf16 v[192:195], v[220:223], v[144:147], v[192:195]\n"
  "v_exp_f32 v171, v171\n"
  "ds_read_b64 v[232:233], v16 offset:1024\n"
  "ds_read_b64 v[234:235], v16 offset:1536\n"
  "v_mfma_f32_16x16x32_bf16 v[176:179], v[224:227], v[116:119], v[176:179]\n"
  "v_cvt_pk_bf16_f32 v164, v164, v165\n"
  "v_cvt_pk_bf16_f32 v165, v166, v167\n"
  "ds_read_b64 v[236:237], v16 offset:0\n"
  "ds_read_b64 v[238:239], v16 offset:512\n"
  "buffer_load_dwordx4 v10, %[srdk], s40 offen offset:2048 nt lds\n"
  "v_mfma_f32_16x16x32_bf16 v[192:195], v[224:227], v[132:135], v[192:195]\n"
  "v_cvt_pk_bf16_f32 v166, v168, v169\n"
  "v_cvt_pk_bf16_f32 v167, v170, v171\n"
  "v_mfma_f32_16x16x32_bf16 v[176:179], v[228:231], v[120:123], v[176:179]\n"
  "v_exp_f32 v184, v184\n"
  "buffer_load_dwordx4 v11, %[srdk], s40 offen offset:3072 nt lds\n"
  "v_mfma_f32_16x16x32_bf16 v[192:195], v[228:231], v[136:139], v[192:195]\n"
  "v_exp_f32 v185, v185\n"
  "s_waitcnt lgkmcnt(10)\n"
  "v_mfma_f32_16x16x32_bf16 v[44:47], v[200:203], v[164:167], v[44:47]\n"
  "v_exp_f32 v186, v186\n"
  "v_mfma_f32_16x16x32_bf16 v[36:39], v[32:35], v[164:167], v[36:39]\n"
  "v_exp_f32 v187, v187\n"
  "v_cvt_pk_bf16_f32 v180, v180, v181\n"
  "v_cvt_pk_bf16_f32 v181, v182, v183\n"
  "v_cvt_pk_bf16_f32 v182, v184, v185\n"
  "v_cvt_pk_bf16_f32 v183, v186, v187\n"
  "s_nop 1\n"
  "v_mfma_f32_16x16x32_bf16 v[76:79], v[200:203], v[180:183], v[76:79]\n"
  "v_exp_f32 v172, v172\n"
  "s_setprio 2\n"
  "v_mfma_f32_16x16x32_bf16 v[40:43], v[32:35], v[180:183], v[40:43]\n"
  "v_exp_f32 v173, v173\n"
  "ds_read_b64_tr_b16 v[216:217], v241 offset:8704\n"
  "ds_read_b64_tr_b16 v[218:219], v241 offset:12800\n"
  "s_waitcnt lgkmcnt(10)\n"
  "v_mfma_f32_16x16x32_bf16 v[48:51], v[204:207], v[164:167], v[48:51]\n"
  "v_exp_f32 v174, v174\n"
  "v_mfma_f32_16x16x32_bf16 v[80:83], v[204:207], v[180:183], v[80:83]\n"
  "v_exp_f32 v175, v175\n"
  "ds_read_b64_tr_b16 v[220:221], v242 offset:8704\n"
  "ds_read_b64_tr_b16 v[222:223], v242 offset:12800\n"
  "s_waitcnt lgkmcnt(10)\n"
  "v_mfma_f32_16x16x32_bf16 v[52:55], v[208:211], v[164:167], v[52:55]\n"
  "v_exp_f32 v188, v188\n"
  "v_mfma_f32_16x16x32_bf16 v[84:87], v[208:211], v[180:183], v[84:87]\n"
  "v_exp_f32 v189, v189\n"
  "ds_read_b64_tr_b16 v[224:225], v241 offset:8960\n"
  "ds_read_b64_tr_b16 v[226:227], v241 offset:13056\n"
  "s_waitcnt lgkmcnt(10)\n"
  "v_mfma_f32_16x16x32_bf16 v[56:59], v[212:215], v[164:167], v[56:59]\n"
  "v_exp_f32 v190, v190\n"
  "v_mfma_f32_16x16x32_bf16 v[88:91], v[212:215], v[180:183], v[88:91]\n"
  "v_exp_f32 v191, v191\n"
  "ds_read_b64_tr_b16 v[228:229], v242 offset:8960\n"
  "ds_read_b64_tr_b16 v[230:231], v242 offset:13056\n"
  "s_waitcnt lgkmcnt(6)\n"
  "v_mfma_f32_16x16x32_bf16 v[60:63], v[216:219], v[164:167], v[60:63]\n"
  "v_exp_f32 v176, v176\n"
  "v_mfma_f32_16x16x32_bf16 v[92:95], v[216:219], v[180:183], v[92:95]\n"
  "v_exp_f32 v177, v177\n"
  "v_mfma_f32_16x16x32_bf16 v[108:111], v[216:219], v[232:235], v[108:111]\n"
  "v_exp_f32 v178, v178\n"
  "ds_read_b64_tr_b16 v[200:201], v241 offset:0\n"
  "ds_read_b64_tr_b16 v[202:203], v241 offset:4096\n"
  "s_waitcnt lgkmcnt(6)\n"
  "v_mfma_f32_16x16x32_bf16 v[64:67], v[220:223], v[164:167], v[64:67]\n"
  "v_exp_f32 v179, v179\n"
  "s_setprio 1\n"
  "v_mfma_f32_16x16x32_bf16 v[96:99], v[220:223], v[180:183], v[96:99]\n"
  "v_cvt_pk_bf16_f32 v172, v172, v173\n"
  "v_cvt_pk_bf16_f32 v173, v174, v175\n"
  "v_mfma_f32_16x16x32_bf16 v[112:115], v[220:223], v[232:235], v[112:115]\n"
  "v_cvt_pk_bf16_f32 v174, v176, v177\n"
  "v_cvt_pk_bf16_f32 v175, v178, v179\n"
  "ds_read_b64_tr_b16 v[204:205], v242 offset:0\n"
  "ds_read_b64_tr_b16 v[206:207], v242 offset:4096\n"
  "s_waitcnt lgkmcnt(6)\n"
  "v_mfma_f32_16x16x32_bf16 v[68:71], v[224:227], v[164:167], v[68:71]\n"
  "v_exp_f32 v192, v192\n"
  "v_mfma_f32_16x16x32_bf16 v[100:103], v[224:227], v[180:183], v[100:103]\n"
  "v_exp_f32 v193, v193\n"
  "ds_read_b64_tr_b16 v[208:209], v241 offset:256\n"
  "ds_read_b64_tr_b16 v[210:211], v241 offset:4352\n"
  "s_waitcnt lgkmcnt(6)\n"
  "v_mfma_f32_16x16x32_bf16 v[72:75], v[228:231], v[164:167], v[72:75]\n"
  "v_exp_f32 v194, v194\n"
  "v_mfma_f32_16x16x32_bf16 v[104:107], v[228:231], v[180:183], v[104:107]\n"
  "v_exp_f32 v195, v195\n"
  "ds_read_b64_tr_b16 v[212:213], v242 offset:256\n"
  "ds_read_b64_tr_b16 v[214:215], v242 offset:4352\n"
  "s_waitcnt lgkmcnt(6)\n"
  "v_mfma_f32_16x16x32_bf16 v[44:47], v[200:203], v[172:175], v[44:47]\n"
  "v_cvt_pk_bf16_f32 v188, v188, v189\n"
  "v_cvt_pk_bf16_f32 v189, v190, v191\n"
  "v_mfma_f32_16x16x32_bf16 v[36:39], v[32:35], v[172:175], v[36:39]\n"
  "v_cvt_pk_bf16_f32 v190, v192, v193\n"
  "v_cvt_pk_bf16_f32 v191, v194, v195\n"
  "s_nop 1\n"
  "v_mfma_f32_16x16x32_bf16 v[76:79], v[200:203], v[188:191], v[76:79]\n"
  "v_mfma_f32_16x16x32_bf16 v[40:43], v[32:35], v[188:191], v[40:43]\n"
  "ds_read_b64_tr_b16 v[216:217], v241 offset:512\n"
  "ds_read_b64_tr_b16 v[218:219], v241 offset:4608\n"
  "s_waitcnt lgkmcnt(6)\n"
  "v_mfma_f32_16x16x32_bf16 v[48:51], v[204:207], v[172:175], v[48:51]\n"
  "s_setprio 0\n"
  "v_mfma_f32_16x16x32_bf16 v[80:83], v[204:207], v[188:191], v[80:83]\n"
  "ds_read_b64_tr_b16 v[220:221], v242 offset:512\n"
  "ds_read_b64_tr_b16 v[222:223], v242 offset:4608\n"
  "s_waitcnt lgkmcnt(6)\n"
  "v_mfma_f32_16x16x32_bf16 v[52:55], v[208:211], v[172:175], v[52:55]\n"
  "v_mfma_f32_16x16x32_bf16 v[84:87], v[208:211], v[188:191], v[84:87]\n"
  "ds_read_b64_tr_b16 v[224:225], v241 offset:768\n"
  "ds_read_b64_tr_b16 v[226:227], v241 offset:4864\n"
  "s_waitcnt lgkmcnt(6)\n"
  "v_mfma_f32_16x16x32_bf16 v[56:59], v[212:215], v[172:175], v[56:59]\n"
  "v_mfma_f32_16x16x32_bf16 v[88:91], v[212:215], v[188:191], v[88:91]\n"
  "ds_read_b64_tr_b16 v[228:229], v242 offset:768\n"
  "ds_read_b64_tr_b16 v[230:231], v242 offset:4864\n"
  "s_waitcnt lgkmcnt(6)\n"
  "v_mfma_f32_16x16x32_bf16 v[60:63], v[216:219], v[172:175], v[60:63]\n"
  "v_mfma_f32_16x16x32_bf16 v[92:95], v[216:219], v[188:191], v[92:95]\n"
  "v_mfma_f32_16x16x32_bf16 v[108:111], v[216:219], v[236:239], v[108:111]\n"
  "s_waitcnt lgkmcnt(4)\n"
  "v_mfma_f32_16x16x32_bf16 v[64:67], v[220:223], v[172:175], v[64:67]\n"
  "v_mfma_f32_16x16x32_bf16 v[96:99], v[220:223], v[188:191], v[96:99]\n"
  "v_mfma_f32_16x16x32_bf16 v[112:115], v[220:223], v[236:239], v[112:115]\n"
  "s_setprio 3\n"
  "s_waitcnt vmcnt(8) lgkmcnt(0)\n"
  "s_barrier\n"
  "ds_read_b128 v[200:203], v12 offset:8192\n"
  "v_mfma_f32_16x16x32_bf16 v[68:71], v[224:227], v[172:175], v[68:71]\n"
  "ds_read_b128 v[204:207], v12 offset:8448\n"
  "s_add_u32 m0, s51, 81920\n"
  "s_add_u32 s40, s50, 49152\n"
  "buffer_load_dwordx4 v10, %[srdv], s40 offen offset:0 nt lds\n"
  "v_mfma_f32_16x16x32_bf16 v[100:103], v[224:227], v[188:191], v[100:103]\n"
  "ds_read_b128 v[208:211], v12 offset:8704\n"
  "buffer_load_dwordx4 v11, %[srdv], s40 offen offset:1024 nt lds\n"
  "v_mfma_f32_16x16x32_bf16 v[72:75], v[228:231], v[172:175], v[72:75]\n"
  "ds_read_b128 v[212:215], v12 offset:8960\n"
  "buffer_load_dwordx4 v10, %[srdv], s40 offen offset:2048 nt lds\n"
  "v_mfma_f32_16x16x32_bf16 v[104:107], v[228:231], v[188:191], v[104:107]\n"
  "buffer_load_dwordx4 v11, %[srdv], s40 offen offset:3072 nt lds\n"
  "s_add_u32 s50, s50, 32768\n"
  "s_sub_u32 s41, s41, 1\n"
  "s_cmp_lg_u32 s41, 0\n"
  "s_cbranch_scc1 P1_LOOP2\n"
  "v_lshrrev_b32 v3, 2, v1\n"
  "v_mul_u32_u24 v5, 1280, v3\n"
  "v_bfe_u32 v3, v1, 4, 1\n"
  "v_lshlrev_b32 v3, 1, v3\n"
  "v_and_b32 v7, 3, v1\n"
  "v_xor_b32 v3, v3, v7\n"
  "v_lshl_add_u32 v252, v3, 4, v5\n"
  "s_lshl_b32 s38, %[wave], 1\n"
  "s_and_b32 s39, s38, 3\n"
  "s_mul_i32 s39, s39, 20480\n"
  "s_lshr_b32 s40, s38, 2\n"
  "s_lshl_b32 s40, s40, 6\n"
  "s_add_u32 s52, s39, s40\n"
  "s_add_u32 s38, s38, 1\n"
  "s_and_b32 s39, s38, 3\n"
  "s_mul_i32 s39, s39, 20480\n"
  "s_lshr_b32 s40, s38, 2\n"
  "s_lshl_b32 s40, s40, 6\n"
  "s_add_u32 s53, s39, s40\n"
  "s_lshr_b32 s38, %[wave], 1\n"
  "s_add_u32 s38, s38, 16\n"
  "s_and_b32 s39, s38, 3\n"
  "s_mul_i32 s39, s39, 20480\n"
  "s_lshr_b32 s40, s38, 2\n"
  "s_lshl_b32 s40, s40, 6\n"
  "s_add_u32 s54, s39, s40\n"
  "s_lshl_b32 s44, s38, 10\n"
  "s_add_u32 s44, s44, %[ldsb]\n"
  "s_lshl_b32 s43, %[wave], 11\n"
  "s_add_u32 s43, s43, %[ldsb]\n"
  "s_and_b32 s38, %[wave], 1\n"
  "s_cmp_eq_u32 s38, 0\n"
  "s_cselect_b32 s48, -1, 0\n"
  "s_cselect_b32 s49, 0, -1\n"
  "s_mul_i32 s42, %[jx], 327680\n"
  "s_waitcnt lgkmcnt(3)\n"
  "v_mfma_f32_16x16x32_bf16 v[196:199], v[200:203], v[148:151], v[28:31]\n"
  "ds_read_b128 v[216:219], v12 offset:12288\n"
  "v_mfma_f32_16x16x32_bf16 v[164:167], v[200:203], v[124:127], v[20:23]\n"
  "v_mfma_f32_16x16x32_bf16 v[180:183], v[200:203], v[140:143], v[24:27]\n"
  "ds_read_b128 v[220:223], v12 offset:12544\n"
  "s_waitcnt lgkmcnt(4)\n"
  "v_mfma_f32_16x16x32_bf16 v[196:199], v[204:207], v[152:155], v[196:199]\n"
  "s_setprio 2\n"
  "v_mfma_f32_16x16x32_bf16 v[164:167], v[204:207], v[128:131], v[164:167]\n"
  "ds_read_b128 v[224:227], v12 offset:12800\n"
  "v_mfma_f32_16x16x32_bf16 v[180:183], v[204:207], v[144:147], v[180:183]\n"
  "s_waitcnt lgkmcnt(4)\n"
  "v_mfma_f32_16x16x32_bf16 v[196:199], v[208:211], v[156:159], v[196:199]\n"
  "ds_read_b128 v[228:231], v12 offset:13056\n"
  "v_mfma_f32_16x16x32_bf16 v[164:167], v[208:211], v[116:119], v[164:167]\n"
  "v_mfma_f32_16x16x32_bf16 v[180:183], v[208:211], v[132:135], v[180:183]\n"
  "s_waitcnt lgkmcnt(4)\n"
  "v_mfma_f32_16x16x32_bf16 v[196:199], v[212:215], v[160:163], v[196:199]\n"
  "v_mfma_f32_16x16x32_bf16 v[164:167], v[212:215], v[120:123], v[164:167]\n"
  "v_mfma_f32_16x16x32_bf16 v[180:183], v[212:215], v[136:139], v[180:183]\n"
  "s_setprio 1\n"
  "s_waitcnt lgkmcnt(3)\n"
  "v_mfma_f32_16x16x32_bf16 v[168:171], v[216:219], v[124:127], v[20:23]\n"
  "ds_read_b128 v[200:203], v12 offset:0\n"
  "v_mfma_f32_16x16x32_bf16 v[184:187], v[216:219], v[140:143], v[24:27]\n"
  "s_nop 0\n"
  "v_exp_f32 v196, v196\n"
  "ds_read_b128 v[204:207], v12 offset:256\n"
  "s_waitcnt lgkmcnt(4)\n"
  "v_mfma_f32_16x16x32_bf16 v[168:171], v[220:223], v[128:131], v[168:171]\n"
  "v_exp_f32 v197, v197\n"
  "ds_read_b128 v[208:211], v12 offset:512\n"
  "v_mfma_f32_16x16x32_bf16 v[184:187], v[220:223], v[144:147], v[184:187]\n"
  "v_exp_f32 v198, v198\n"
  "ds_read_b128 v[212:215], v12 offset:768\n"
  "s_waitcnt lgkmcnt(5)\n"
  "v_mfma_f32_16x16x32_bf16 v[168:171], v[224:227], v[116:119], v[168:171]\n"
  "v_exp_f32 v199, v199\n"
  "v_mfma_f32_16x16x32_bf16 v[184:187], v[224:227], v[132:135], v[184:187]\n"
  "v_add_f32 v18, v18, v196\n"
  "v_add_f32 v19, v19, v197\n"
  "s_waitcnt lgkmcnt(4)\n"
  "v_mfma_f32_16x16x32_bf16 v[168:171], v[228:231], v[120:123], v[168:171]\n"
  "v_cvt_pk_bf16_f32 v244, v196, v197\n"
  "v_cvt_pk_bf16_f32 v245, v198, v199\n"
  "ds_write_b64 v15, v[244:245] offset:0\n"
  "v_mfma_f32_16x16x32_bf16 v[184:187], v[228:231], v[136:139], v[184:187]\n"
  "v_add_f32 v18, v18, v198\n"
  "v_add_f32 v19, v19, v199\n"
  "s_setprio 0\n"
  "s_waitcnt lgkmcnt(4)\n"
  "v_mfma_f32_16x16x32_bf16 v[172:175], v[200:203], v[124:127], v[20:23]\n"
  "v_exp_f32 v164, v164\n"
  "ds_read_b128 v[216:219], v12 offset:4096\n"
  "v_mfma_f32_16x16x32_bf16 v[188:191], v[200:203], v[140:143], v[24:27]\n"
  "v_exp_f32 v165, v165\n"
  "ds_read_b128 v[220:223], v12 offset:4352\n"
  "s_waitcnt lgkmcnt(5)\n"
  "v_mfma_f32_16x16x32_bf16 v[172:175], v[204:207], v[128:131], v[172:175]\n"
  "v_exp_f32 v166, v166\n"
  "ds_read_b128 v[224:227], v12 offset:4608\n"
  "v_mfma_f32_16x16x32_bf16 v[188:191], v[204:207], v[144:147], v[188:191]\n"
  "v_exp_f32 v167, v167\n"
  "ds_read_b128 v[228:231], v12 offset:4864\n"
  "s_waitcnt lgkmcnt(6)\n"
  "v_mfma_f32_16x16x32_bf16 v[172:175], v[208:211], v[116:119], v[172:175]\n"
  "v_exp_f32 v180, v180\n"
  "v_mfma_f32_16x16x32_bf16 v[188:191], v[208:211], v[132:135], v[188:191]\n"
  "v_exp_f32 v181, v181\n"
  "s_waitcnt lgkmcnt(5)\n"
  "v_mfma_f32_16x16x32_bf16 v[172:175], v[212:215], v[120:123], v[172:175]\n"
  "v_exp_f32 v182, v182\n"
  "v_mfma_f32_16x16x32_bf16 v[188:191], v[212:215], v[136:139], v[188:191]\n"
  "v_exp_f32 v183, v183\n"
  "s_waitcnt vmcnt(8) lgkmcnt(0)\n"
  "s_barrier\n"
  "ds_read_b64_tr_b16 v[200:201], v13 offset:8192\n"
  "ds_read_b64_tr_b16 v[202:203], v13 offset:12288\n"
  "s_setprio 3\n"
  "v_mfma_f32_16x16x32_bf16 v[176:179], v[216:219], v[124:127], v[20:23]\n"
  "v_exp_f32 v168, v168\n"
  "ds_read_b64_tr_b16 v[204:205], v14 offset:8192\n"
  "ds_read_b64_tr_b16 v[206:207], v14 offset:12288\n"
  "v_mfma_f32_16x16x32_bf16 v[192:195], v[216:219], v[140:143], v[24:27]\n"
  "v_exp_f32 v169, v169\n"
  "ds_read_b64_tr_b16 v[208:209], v13 offset:8448\n"
  "ds_read_b64_tr_b16 v[210:211], v13 offset:12544\n"
  "v_mfma_f32_16x16x32_bf16 v[176:179], v[220:223], v[128:131], v[176:179]\n"
  "v_exp_f32 v170, v170\n"
  "ds_read_b64_tr_b16 v[212:213], v14 offset:8448\n"
  "ds_read_b64_tr_b16 v[214:215], v14 offset:12544\n"
  "v_mfma_f32_16x16x32_bf16 v[192:195], v[220:223], v[144:147], v[192:195]\n"
  "v_exp_f32 v171, v171\n"
  "ds_read_b64 v[232:233], v16 offset:1024\n"
  "ds_read_b64 v[234:235], v16 offset:1536\n"
  "v_mfma_f32_16x16x32_bf16 v[176:179], v[224:227], v[116:119], v[176:179]\n"
  "v_cvt_pk_bf16_f32 v164, v164, v165\n"
  "v_cvt_pk_bf16_f32 v165, v166, v167\n"
  "ds_read_b64 v[236:237], v16 offset:0\n"
  "ds_read_b64 v[238:239], v16 offset:512\n"
  "v_mfma_f32_16x16x32_bf16 v[192:195], v[224:227], v[132:135], v[192:195]\n"
  "v_cvt_pk_bf16_f32 v166, v168, v169\n"
  "v_cvt_pk_bf16_f32 v167, v170, v171\n"
  "v_mfma_f32_16x16x32_bf16 v[176:179], v[228:231], v[120:123], v[176:179]\n"
  "v_exp_f32 v184, v184\n"
  "v_mfma_f32_16x16x32_bf16 v[192:195], v[228:231], v[136:139], v[192:195]\n"
  "v_exp_f32 v185, v185\n"
  "s_waitcnt lgkmcnt(10)\n"
  "v_mfma_f32_16x16x32_bf16 v[44:47], v[200:203], v[164:167], v[44:47]\n"
  "v_exp_f32 v186, v186\n"
  "v_mfma_f32_16x16x32_bf16 v[36:39], v[32:35], v[164:167], v[36:39]\n"
  "v_exp_f32 v187, v187\n"
  "v_cvt_pk_bf16_f32 v180, v180, v181\n"
  "v_cvt_pk_bf16_f32 v181, v182, v183\n"
  "v_cvt_pk_bf16_f32 v182, v184, v185\n"
  "v_cvt_pk_bf16_f32 v183, v186, v187\n"
  "s_nop 1\n"
  "v_mfma_f32_16x16x32_bf16 v[76:79], v[200:203], v[180:183], v[76:79]\n"
  "v_exp_f32 v172, v172\n"
  "s_setprio 2\n"
  "v_mfma_f32_16x16x32_bf16 v[40:43], v[32:35], v[180:183], v[40:43]\n"
  "v_exp_f32 v173, v173\n"
  "ds_read_b64_tr_b16 v[216:217], v13 offset:8704\n"
  "ds_read_b64_tr_b16 v[218:219], v13 offset:12800\n"
  "s_waitcnt lgkmcnt(10)\n"
  "v_mfma_f32_16x16x32_bf16 v[48:51], v[204:207], v[164:167], v[48:51]\n"
  "v_exp_f32 v174, v174\n"
  "v_mfma_f32_16x16x32_bf16 v[80:83], v[204:207], v[180:183], v[80:83]\n"
  "v_exp_f32 v175, v175\n"
  "ds_read_b64_tr_b16 v[220:221], v14 offset:8704\n"
  "ds_read_b64_tr_b16 v[222:223], v14 offset:12800\n"
  "s_waitcnt lgkmcnt(10)\n"
  "v_mfma_f32_16x16x32_bf16 v[52:55], v[208:211], v[164:167], v[52:55]\n"
  "v_exp_f32 v188, v188\n"
  "v_mfma_f32_16x16x32_bf16 v[84:87], v[208:211], v[180:183], v[84:87]\n"
  "v_exp_f32 v189, v189\n"
  "ds_read_b64_tr_b16 v[224:225], v13 offset:8960\n"
  "ds_read_b64_tr_b16 v[226:227], v13 offset:13056\n"
  "s_waitcnt lgkmcnt(10)\n"
  "v_mfma_f32_16x16x32_bf16 v[56:59], v[212:215], v[164:167], v[56:59]\n"
  "v_exp_f32 v190, v190\n"
  "v_mfma_f32_16x16x32_bf16 v[88:91], v[212:215], v[180:183], v[88:91]\n"
  "v_exp_f32 v191, v191\n"
  "ds_read_b64_tr_b16 v[228:229], v14 offset:8960\n"
  "ds_read_b64_tr_b16 v[230:231], v14 offset:13056\n"
  "s_waitcnt lgkmcnt(6)\n"
  "v_mfma_f32_16x16x32_bf16 v[60:63], v[216:219], v[164:167], v[60:63]\n"
  "v_exp_f32 v176, v176\n"
  "v_mfma_f32_16x16x32_bf16 v[92:95], v[216:219], v[180:183], v[92:95]\n"
  "v_exp_f32 v177, v177\n"
  "v_mfma_f32_16x16x32_bf16 v[108:111], v[216:219], v[232:235], v[108:111]\n"
  "v_exp_f32 v178, v178\n"
  "ds_read_b64_tr_b16 v[200:201], v13 offset:0\n"
  "ds_read_b64_tr_b16 v[202:203], v13 offset:4096\n"
  "s_waitcnt lgkmcnt(6)\n"
  "v_mfma_f32_16x16x32_bf16 v[64:67], v[220:223], v[164:167], v[64:67]\n"
  "v_exp_f32 v179, v179\n"
  "s_setprio 1\n"
  "v_mfma_f32_16x16x32_bf16 v[96:99], v[220:223], v[180:183], v[96:99]\n"
  "v_cvt_pk_bf16_f32 v172, v172, v173\n"
  "v_cvt_pk_bf16_f32 v173, v174, v175\n"
  "v_mfma_f32_16x16x32_bf16 v[112:115], v[220:223], v[232:235], v[112:115]\n"
  "v_cvt_pk_bf16_f32 v174, v176, v177\n"
  "v_cvt_pk_bf16_f32 v175, v178, v179\n"
  "ds_read_b64_tr_b16 v[204:205], v14 offset:0\n"
  "ds_read_b64_tr_b16 v[206:207], v14 offset:4096\n"
  "s_waitcnt lgkmcnt(6)\n"
  "v_mfma_f32_16x16x32_bf16 v[68:71], v[224:227], v[164:167], v[68:71]\n"
  "v_exp_f32 v192, v192\n"
  "v_mfma_f32_16x16x32_bf16 v[100:103], v[224:227], v[180:183], v[100:103]\n"
  "v_exp_f32 v193, v193\n"
  "ds_read_b64_tr_b16 v[208:209], v13 offset:256\n"
  "ds_read_b64_tr_b16 v[210:211], v13 offset:4352\n"
  "s_waitcnt lgkmcnt(6)\n"
  "v_mfma_f32_16x16x32_bf16 v[72:75], v[228:231], v[164:167], v[72:75]\n"
  "v_exp_f32 v194, v194\n"
  "v_mfma_f32_16x16x32_bf16 v[104:107], v[228:231], v[180:183], v[104:107]\n"
  "v_exp_f32 v195, v195\n"
  "ds_read_b64_tr_b16 v[212:213], v14 offset:256\n"
  "ds_read_b64_tr_b16 v[214:215], v14 offset:4352\n"
  "s_waitcnt lgkmcnt(6)\n"
  "v_mfma_f32_16x16x32_bf16 v[44:47], v[200:203], v[172:175], v[44:47]\n"
  "v_cvt_pk_bf16_f32 v188, v188, v189\n"
  "v_cvt_pk_bf16_f32 v189, v190, v191\n"
  "v_mfma_f32_16x16x32_bf16 v[36:39], v[32:35], v[172:175], v[36:39]\n"
  "v_cvt_pk_bf16_f32 v190, v192, v193\n"
  "v_cvt_pk_bf16_f32 v191, v194, v195\n"
  "s_nop 1\n"
  "v_mfma_f32_16x16x32_bf16 v[76:79], v[200:203], v[188:191], v[76:79]\n"
  "v_mfma_f32_16x16x32_bf16 v[40:43], v[32:35], v[188:191], v[40:43]\n"
  "ds_read_b64_tr_b16 v[216:217], v13 offset:512\n"
  "ds_read_b64_tr_b16 v[218:219], v13 offset:4608\n"
  "s_waitcnt lgkmcnt(6)\n"
  "v_mfma_f32_16x16x32_bf16 v[48:51], v[204:207], v[172:175], v[48:51]\n"
  "s_setprio 0\n"
  "v_mfma_f32_16x16x32_bf16 v[80:83], v[204:207], v[188:191], v[80:83]\n"
  "ds_read_b64_tr_b16 v[220:221], v14 offset:512\n"
  "ds_read_b64_tr_b16 v[222:223], v14 offset:4608\n"
  "s_waitcnt lgkmcnt(6)\n"
  "v_mfma_f32_16x16x32_bf16 v[52:55], v[208:211], v[172:175], v[52:55]\n"
  "v_mfma_f32_16x16x32_bf16 v[84:87], v[208:211], v[188:191], v[84:87]\n"
  "ds_read_b64_tr_b16 v[224:225], v13 offset:768\n"
  "ds_read_b64_tr_b16 v[226:227], v13 offset:4864\n"
  "s_waitcnt lgkmcnt(6)\n"
  "v_mfma_f32_16x16x32_bf16 v[56:59], v[212:215], v[172:175], v[56:59]\n"
  "v_mfma_f32_16x16x32_bf16 v[88:91], v[212:215], v[188:191], v[88:91]\n"
  "ds_read_b64_tr_b16 v[228:229], v14 offset:768\n"
  "ds_read_b64_tr_b16 v[230:231], v14 offset:4864\n"
  "s_waitcnt lgkmcnt(6)\n"
  "v_mfma_f32_16x16x32_bf16 v[60:63], v[216:219], v[172:175], v[60:63]\n"
  "v_mfma_f32_16x16x32_bf16 v[92:95], v[216:219], v[188:191], v[92:95]\n"
  "v_mfma_f32_16x16x32_bf16 v[108:111], v[216:219], v[236:239], v[108:111]\n"
  "s_waitcnt lgkmcnt(4)\n"
  "v_mfma_f32_16x16x32_bf16 v[64:67], v[220:223], v[172:175], v[64:67]\n"
  "v_mfma_f32_16x16x32_bf16 v[96:99], v[220:223], v[188:191], v[96:99]\n"
  "v_mfma_f32_16x16x32_bf16 v[112:115], v[220:223], v[236:239], v[112:115]\n"
  "s_setprio 3\n"
  "s_waitcnt vmcnt(4) lgkmcnt(0)\n"
  "s_barrier\n"
  "ds_read_b128 v[200:203], v240 offset:8192\n"
  "v_mfma_f32_16x16x32_bf16 v[68:71], v[224:227], v[172:175], v[68:71]\n"
  "ds_read_b128 v[204:207], v240 offset:8448\n"
  "s_add_u32 m0, s43, 0\n"
  "s_add_u32 s40, s42, s52\n"
  "buffer_load_dwordx4 v252, %[srdwo], s40 offen offset:0 lds\n"
  "v_mfma_f32_16x16x32_bf16 v[100:103], v[224:227], v[188:191], v[100:103]\n"
  "ds_read_b128 v[208:211], v240 offset:8704\n"
  "s_add_u32 m0, s43, 1024\n"
  "s_add_u32 s40, s42, s53\n"
  "buffer_load_dwordx4 v252, %[srdwo], s40 offen offset:0 lds\n"
  "v_mfma_f32_16x16x32_bf16 v[72:75], v[228:231], v[172:175], v[72:75]\n"
  "ds_read_b128 v[212:215], v240 offset:8960\n"
  "s_add_u32 m0, s44, 0\n"
  "s_add_u32 s40, s42, s54\n"
  "s_mov_b64 exec, s[48:49]\n"
  "buffer_load_dwordx4 v252, %[srdwo], s40 offen offset:0 lds\n"
  "s_mov_b64 exec, -1\n"
  "s_add_u32 s42, s42, 81920\n"
  "s_cmp_ge_u32 s42, 1310720\n"
  "s_cselect_b32 s40, 1310720, 0\n"
  "s_sub_u32 s42, s42, s40\n"
  "v_mfma_f32_16x16x32_bf16 v[104:107], v[228:231], v[188:191], v[104:107]\n"
  "s_add_u32 m0, s43, 38912\n"
  "s_add_u32 s40, s42, s52\n"
  "buffer_load_dwordx4 v252, %[srdwo], s40 offen offset:0 lds\n"
  "s_add_u32 m0, s43, 39936\n"
  "s_add_u32 s40, s42, s53\n"
  "buffer_load_dwordx4 v252, %[srdwo], s40 offen offset:0 lds\n"
  "s_add_u32 m0, s44, 38912\n"
  "s_add_u32 s40, s42, s54\n"
  "s_mov_b64 exec, s[48:49]\n"
  "buffer_load_dwordx4 v252, %[srdwo], s40 offen offset:0 lds\n"
  "s_mov_b64 exec, -1\n"
  "s_add_u32 s42, s42, 81920\n"
  "s_cmp_ge_u32 s42, 1310720\n"
  "s_cselect_b32 s40, 1310720, 0\n"
  "s_sub_u32 s42, s42, s40\n"
  "s_waitcnt lgkmcnt(3)\n"
  "v_mfma_f32_16x16x32_bf16 v[196:199], v[200:203], v[148:151], v[28:31]\n"
  "ds_read_b128 v[216:219], v240 offset:12288\n"
  "v_mfma_f32_16x16x32_bf16 v[164:167], v[200:203], v[124:127], v[20:23]\n"
  "v_mfma_f32_16x16x32_bf16 v[180:183], v[200:203], v[140:143], v[24:27]\n"
  "ds_read_b128 v[220:223], v240 offset:12544\n"
  "s_waitcnt lgkmcnt(4)\n"
  "v_mfma_f32_16x16x32_bf16 v[196:199], v[204:207], v[152:155], v[196:199]\n"
  "s_setprio 2\n"
  "v_mfma_f32_16x16x32_bf16 v[164:167], v[204:207], v[128:131], v[164:167]\n"
  "ds_read_b128 v[224:227], v240 offset:12800\n"
  "v_mfma_f32_16x16x32_bf16 v[180:183], v[204:207], v[144:147], v[180:183]\n"
  "s_waitcnt lgkmcnt(4)\n"
  "v_mfma_f32_16x16x32_bf16 v[196:199], v[208:211], v[156:159], v[196:199]\n"
  "ds_read_b128 v[228:231], v240 offset:13056\n"
  "v_mfma_f32_16x16x32_bf16 v[164:167], v[208:211], v[116:119], v[164:167]\n"
  "v_mfma_f32_16x16x32_bf16 v[180:183], v[208:211], v[132:135], v[180:183]\n"
  "s_waitcnt lgkmcnt(4)\n"
  "v_mfma_f32_16x16x32_bf16 v[196:199], v[212:215], v[160:163], v[196:199]\n"
  "v_mfma_f32_16x16x32_bf16 v[164:167], v[212:215], v[120:123], v[164:167]\n"
  "v_mfma_f32_16x16x32_bf16 v[180:183], v[212:215], v[136:139], v[180:183]\n"
  "s_setprio 1\n"
  "s_waitcnt lgkmcnt(3)\n"
  "v_mfma_f32_16x16x32_bf16 v[168:171], v[216:219], v[124:127], v[20:23]\n"
  "ds_read_b128 v[200:203], v240 offset:0\n"
  "v_mfma_f32_16x16x32_bf16 v[184:187], v[216:219], v[140:143], v[24:27]\n"
  "s_nop 0\n"
  "v_exp_f32 v196, v196\n"
  "ds_read_b128 v[204:207], v240 offset:256\n"
  "s_waitcnt lgkmcnt(4)\n"
  "v_mfma_f32_16x16x32_bf16 v[168:171], v[220:223], v[128:131], v[168:171]\n"
  "v_exp_f32 v197, v197\n"
  "ds_read_b128 v[208:211], v240 offset:512\n"
  "v_mfma_f32_16x16x32_bf16 v[184:187], v[220:223], v[144:147], v[184:187]\n"
  "v_exp_f32 v198, v198\n"
  "ds_read_b128 v[212:215], v240 offset:768\n"
  "s_waitcnt lgkmcnt(5)\n"
  "v_mfma_f32_16x16x32_bf16 v[168:171], v[224:227], v[116:119], v[168:171]\n"
  "v_exp_f32 v199, v199\n"
  "v_mfma_f32_16x16x32_bf16 v[184:187], v[224:227], v[132:135], v[184:187]\n"
  "v_add_f32 v18, v18, v196\n"
  "v_add_f32 v19, v19, v197\n"
  "s_waitcnt lgkmcnt(4)\n"
  "v_mfma_f32_16x16x32_bf16 v[168:171], v[228:231], v[120:123], v[168:171]\n"
  "v_cvt_pk_bf16_f32 v244, v196, v197\n"
  "v_cvt_pk_bf16_f32 v245, v198, v199\n"
  "ds_write_b64 v15, v[244:245] offset:0\n"
  "v_mfma_f32_16x16x32_bf16 v[184:187], v[228:231], v[136:139], v[184:187]\n"
  "v_add_f32 v18, v18, v198\n"
  "v_add_f32 v19, v19, v199\n"
  "s_setprio 0\n"
  "s_waitcnt lgkmcnt(4)\n"
  "v_mfma_f32_16x16x32_bf16 v[172:175], v[200:203], v[124:127], v[20:23]\n"
  "v_exp_f32 v164, v164\n"
  "ds_read_b128 v[216:219], v240 offset:4096\n"
  "v_mfma_f32_16x16x32_bf16 v[188:191], v[200:203], v[140:143], v[24:27]\n"
  "v_exp_f32 v165, v165\n"
  "ds_read_b128 v[220:223], v240 offset:4352\n"
  "s_waitcnt lgkmcnt(5)\n"
  "v_mfma_f32_16x16x32_bf16 v[172:175], v[204:207], v[128:131], v[172:175]\n"
  "v_exp_f32 v166, v166\n"
  "ds_read_b128 v[224:227], v240 offset:4608\n"
  "v_mfma_f32_16x16x32_bf16 v[188:191], v[204:207], v[144:147], v[188:191]\n"
  "v_exp_f32 v167, v167\n"
  "ds_read_b128 v[228:231], v240 offset:4864\n"
  "s_waitcnt lgkmcnt(6)\n"
  "v_mfma_f32_16x16x32_bf16 v[172:175], v[208:211], v[116:119], v[172:175]\n"
  "v_exp_f32 v180, v180\n"
  "v_mfma_f32_16x16x32_bf16 v[188:191], v[208:211], v[132:135], v[188:191]\n"
  "v_exp_f32 v181, v181\n"
  "s_waitcnt lgkmcnt(5)\n"
  "v_mfma_f32_16x16x32_bf16 v[172:175], v[212:215], v[120:123], v[172:175]\n"
  "v_exp_f32 v182, v182\n"
  "v_mfma_f32_16x16x32_bf16 v[188:191], v[212:215], v[136:139], v[188:191]\n"
  "v_exp_f32 v183, v183\n"
  "s_waitcnt vmcnt(6) lgkmcnt(0)\n"
  "s_barrier\n"
  "ds_read_b64_tr_b16 v[200:201], v241 offset:8192\n"
  "ds_read_b64_tr_b16 v[202:203], v241 offset:12288\n"
  "s_setprio 3\n"
  "v_mfma_f32_16x16x32_bf16 v[176:179], v[216:219], v[124:127], v[20:23]\n"
  "v_exp_f32 v168, v168\n"
  "ds_read_b64_tr_b16 v[204:205], v242 offset:8192\n"
  "ds_read_b64_tr_b16 v[206:207], v242 offset:12288\n"
  "v_mfma_f32_16x16x32_bf16 v[192:195], v[216:219], v[140:143], v[24:27]\n"
  "v_exp_f32 v169, v169\n"
  "ds_read_b64_tr_b16 v[208:209], v241 offset:8448\n"
  "ds_read_b64_tr_b16 v[210:211], v241 offset:12544\n"
  "v_mfma_f32_16x16x32_bf16 v[176:179], v[220:223], v[128:131], v[176:179]\n"
  "v_exp_f32 v170, v170\n"
  "ds_read_b64_tr_b16 v[212:213], v242 offset:8448\n"
  "ds_read_b64_tr_b16 v[214:215], v242 offset:12544\n"
  "v_mfma_f32_16x16x32_bf16 v[192:195], v[220:223], v[144:147], v[192:195]\n"
  "v_exp_f32 v171, v171\n"
  "ds_read_b64 v[232:233], v16 offset:1024\n"
  "ds_read_b64 v[234:235], v16 offset:1536\n"
  "v_mfma_f32_16x16x32_bf16 v[176:179], v[224:227], v[116:119], v[176:179]\n"
  "v_cvt_pk_bf16_f32 v164, v164, v165\n"
  "v_cvt_pk_bf16_f32 v165, v166, v167\n"
  "ds_read_b64 v[236:237], v16 offset:0\n"
  "ds_read_b64 v[238:239], v16 offset:512\n"
  "v_mfma_f32_16x16x32_bf16 v[192:195], v[224:227], v[132:135], v[192:195]\n"
  "v_cvt_pk_bf16_f32 v166, v168, v169\n"
  "v_cvt_pk_bf16_f32 v167, v170, v171\n"
  "v_mfma_f32_16x16x32_bf16 v[176:179], v[228:231], v[120:123], v[176:179]\n"
  "v_exp_f32 v184, v184\n"
  "v_mfma_f32_16x16x32_bf16 v[192:195], v[228:231], v[136:139], v[192:195]\n"
  "v_exp_f32 v185, v185\n"
  "s_waitcnt lgkmcnt(10)\n"
  "v_mfma_f32_16x16x32_bf16 v[44:47], v[200:203], v[164:167], v[44:47]\n"
  "v_exp_f32 v186, v186\n"
  "v_mfma_f32_16x16x32_bf16 v[36:39], v[32:35], v[164:167], v[36:39]\n"
  "v_exp_f32 v187, v187\n"
  "v_cvt_pk_bf16_f32 v180, v180, v181\n"
  "v_cvt_pk_bf16_f32 v181, v182, v183\n"
  "v_cvt_pk_bf16_f32 v182, v184, v185\n"
  "v_cvt_pk_bf16_f32 v183, v186, v187\n"
  "s_nop 1\n"
  "v_mfma_f32_16x16x32_bf16 v[76:79], v[200:203], v[180:183], v[76:79]\n"
  "v_exp_f32 v172, v172\n"
  "s_setprio 2\n"
  "v_mfma_f32_16x16x32_bf16 v[40:43], v[32:35], v[180:183], v[40:43]\n"
  "v_exp_f32 v173, v173\n"
  "ds_read_b64_tr_b16 v[216:217], v241 offset:8704\n"
  "ds_read_b64_tr_b16 v[218:219], v241 offset:12800\n"
  "s_waitcnt lgkmcnt(10)\n"
  "v_mfma_f32_16x16x32_bf16 v[48:51], v[204:207], v[164:167], v[48:51]\n"
  "v_exp_f32 v174, v174\n"
  "v_mfma_f32_16x16x32_bf16 v[80:83], v[204:207], v[180:183], v[80:83]\n"
  "v_exp_f32 v175, v175\n"
  "ds_read_b64_tr_b16 v[220:221], v242 offset:8704\n"
  "ds_read_b64_tr_b16 v[222:223], v242 offset:12800\n"
  "s_waitcnt lgkmcnt(10)\n"
  "v_mfma_f32_16x16x32_bf16 v[52:55], v[208:211], v[164:167], v[52:55]\n"
  "v_exp_f32 v188, v188\n"
  "v_mfma_f32_16x16x32_bf16 v[84:87], v[208:211], v[180:183], v[84:87]\n"
  "v_exp_f32 v189, v189\n"
  "ds_read_b64_tr_b16 v[224:225], v241 offset:8960\n"
  "ds_read_b64_tr_b16 v[226:227], v241 offset:13056\n"
  "s_waitcnt lgkmcnt(10)\n"
  "v_mfma_f32_16x16x32_bf16 v[56:59], v[212:215], v[164:167], v[56:59]\n"
  "v_exp_f32 v190, v190\n"
  "v_mfma_f32_16x16x32_bf16 v[88:91], v[212:215], v[180:183], v[88:91]\n"
  "v_exp_f32 v191, v191\n"
  "ds_read_b64_tr_b16 v[228:229], v242 offset:8960\n"
  "ds_read_b64_tr_b16 v[230:231], v242 offset:13056\n"
  "s_waitcnt lgkmcnt(6)\n"
  "v_mfma_f32_16x16x32_bf16 v[60:63], v[216:219], v[164:167], v[60:63]\n"
  "v_exp_f32 v176, v176\n"
  "v_mfma_f32_16x16x32_bf16 v[92:95], v[216:219], v[180:183], v[92:95]\n"
  "v_exp_f32 v177, v177\n"
  "v_mfma_f32_16x16x32_bf16 v[108:111], v[216:219], v[232:235], v[108:111]\n"
  "v_exp_f32 v178, v178\n"
  "ds_read_b64_tr_b16 v[200:201], v241 offset:0\n"
  "ds_read_b64_tr_b16 v[202:203], v241 offset:4096\n"
  "s_waitcnt lgkmcnt(6)\n"
  "v_mfma_f32_16x16x32_bf16 v[64:67], v[220:223], v[164:167], v[64:67]\n"
  "v_exp_f32 v179, v179\n"
  "s_setprio 1\n"
  "v_mfma_f32_16x16x32_bf16 v[96:99], v[220:223], v[180:183], v[96:99]\n"
  "v_cvt_pk_bf16_f32 v172, v172, v173\n"
  "v_cvt_pk_bf16_f32 v173, v174, v175\n"
  "v_mfma_f32_16x16x32_bf16 v[112:115], v[220:223], v[232:235], v[112:115]\n"
  "v_cvt_pk_bf16_f32 v174, v176, v177\n"
  "v_cvt_pk_bf16_f32 v175, v178, v179\n"
  "ds_read_b64_tr_b16 v[204:205], v242 offset:0\n"
  "ds_read_b64_tr_b16 v[206:207], v242 offset:4096\n"
  "s_waitcnt lgkmcnt(6)\n"
  "v_mfma_f32_16x16x32_bf16 v[68:71], v[224:227], v[164:167], v[68:71]\n"
  "v_exp_f32 v192, v192\n"
  "v_mfma_f32_16x16x32_bf16 v[100:103], v[224:227], v[180:183], v[100:103]\n"
  "v_exp_f32 v193, v193\n"
  "ds_read_b64_tr_b16 v[208:209], v241 offset:256\n"
  "ds_read_b64_tr_b16 v[210:211], v241 offset:4352\n"
  "s_waitcnt lgkmcnt(6)\n"
  "v_mfma_f32_16x16x32_bf16 v[72:75], v[228:231], v[164:167], v[72:75]\n"
  "v_exp_f32 v194, v194\n"
  "v_mfma_f32_16x16x32_bf16 v[104:107], v[228:231], v[180:183], v[104:107]\n"
  "v_exp_f32 v195, v195\n"
  "ds_read_b64_tr_b16 v[212:213], v242 offset:256\n"
  "ds_read_b64_tr_b16 v[214:215], v242 offset:4352\n"
  "s_waitcnt lgkmcnt(6)\n"
  "v_mfma_f32_16x16x32_bf16 v[44:47], v[200:203], v[172:175], v[44:47]\n"
  "v_cvt_pk_bf16_f32 v188, v188, v189\n"
  "v_cvt_pk_bf16_f32 v189, v190, v191\n"
  "v_mfma_f32_16x16x32_bf16 v[36:39], v[32:35], v[172:175], v[36:39]\n"
  "v_cvt_pk_bf16_f32 v190, v192, v193\n"
  "v_cvt_pk_bf16_f32 v191, v194, v195\n"
  "s_nop 1\n"
  "v_mfma_f32_16x16x32_bf16 v[76:79], v[200:203], v[188:191], v[76:79]\n"
  "v_lshrrev_b32 v3, 2, v1\n"
  "v_lshlrev_b32 v3, 6, v3\n"
  "v_mfma_f32_16x16x32_bf16 v[40:43], v[32:35], v[188:191], v[40:43]\n"
  "v_bfe_u32 v5, v1, 1, 1\n"
  "v_lshl_add_u32 v3, v5, 4, v3\n"
  "ds_read_b64_tr_b16 v[216:217], v241 offset:512\n"
  "ds_read_b64_tr_b16 v[218:219], v241 offset:4608\n"
  "s_waitcnt lgkmcnt(6)\n"
  "v_mfma_f32_16x16x32_bf16 v[48:51], v[204:207], v[172:175], v[48:51]\n"
  "v_and_b32 v5, 1, v1\n"
  "v_lshl_add_u32 v3, v5, 3, v3\n"
  "s_setprio 0\n"
  "v_mfma_f32_16x16x32_bf16 v[80:83], v[204:207], v[188:191], v[80:83]\n"
  "v_bfe_u32 v5, v1, 4, 1\n"
  "v_add_u32 v3, %[ldsb], v3\n"
  "ds_read_b64_tr_b16 v[220:221], v242 offset:512\n"
  "ds_read_b64_tr_b16 v[222:223], v242 offset:4608\n"
  "s_waitcnt lgkmcnt(6)\n"
  "v_mfma_f32_16x16x32_bf16 v[52:55], v[208:211], v[172:175], v[52:55]\n"
  "s_add_u32 s38, s37, 0\n"
  "s_cmp_ge_u32 s38, 5\n"
  "s_cselect_b32 s39, 5, 0\n"
  "s_sub_u32 s38, s38, s39\n"
  "s_mul_i32 s39, s36, 5\n"
  "s_add_u32 s39, s39, s38\n"
  "s_and_b32 s38, s39, 1\n"
  "s_lshr_b32 s39, s39, 1\n"
  "s_lshl_b32 s39, s39, 12\n"
  "v_xor_b32 v7, s38, v5\n"
  "v_lshl_add_u32 v7, v7, 5, v3\n"
  "v_mfma_f32_16x16x32_bf16 v[84:87], v[208:211], v[188:191], v[84:87]\n"
  "v_add_u32 v20, s39, v7\n"
  "v_add_u32 v25, 77824, v20\n"
  "ds_read_b64_tr_b16 v[224:225], v241 offset:768\n"
  "ds_read_b64_tr_b16 v[226:227], v241 offset:4864\n"
  "s_waitcnt lgkmcnt(6)\n"
  "v_mfma_f32_16x16x32_bf16 v[56:59], v[212:215], v[172:175], v[56:59]\n"
  "s_add_u32 s38, s37, 1\n"
  "s_cmp_ge_u32 s38, 5\n"
  "s_cselect_b32 s39, 5, 0\n"
  "s_sub_u32 s38, s38, s39\n"
  "s_mul_i32 s39, s36, 5\n"
  "s_add_u32 s39, s39, s38\n"
  "s_and_b32 s38, s39, 1\n"
  "s_lshr_b32 s39, s39, 1\n"
  "s_lshl_b32 s39, s39, 12\n"
  "v_xor_b32 v7, s38, v5\n"
  "v_lshl_add_u32 v7, v7, 5, v3\n"
  "v_mfma_f32_16x16x32_bf16 v[88:91], v[212:215], v[188:191], v[88:91]\n"
  "v_add_u32 v21, s39, v7\n"
  "v_add_u32 v26, 77824, v21\n"
  "ds_read_b64_tr_b16 v[228:229], v242 offset:768\n"
  "ds_read_b64_tr_b16 v[230:231], v242 offset:4864\n"
  "s_waitcnt lgkmcnt(6)\n"
  "v_mfma_f32_16x16x32_bf16 v[60:63], v[216:219], v[172:175], v[60:63]\n"
  "s_add_u32 s38, s37, 2\n"
  "s_cmp_ge_u32 s38, 5\n"
  "s_cselect_b32 s39, 5, 0\n"
  "s_sub_u32 s38, s38, s39\n"
  "s_mul_i32 s39, s36, 5\n"
  "s_add_u32 s39, s39, s38\n"
  "s_and_b32 s38, s39, 1\n"
  "s_lshr_b32 s39, s39, 1\n"
  "s_lshl_b32 s39, s39, 12\n"
  "v_xor_b32 v7, s38, v5\n"
  "v_lshl_add_u32 v7, v7, 5, v3\n"
  "v_mfma_f32_16x16x32_bf16 v[92:95], v[216:219], v[188:191], v[92:95]\n"
  "v_add_u32 v22, s39, v7\n"
  "v_add_u32 v27, 77824, v22\n"
  "v_mfma_f32_16x16x32_bf16 v[108:111], v[216:219], v[236:239], v[108:111]\n"
  "s_add_u32 s38, s37, 3\n"
  "s_cmp_ge_u32 s38, 5\n"
  "s_cselect_b32 s39, 5, 0\n"
  "s_sub_u32 s38, s38, s39\n"
  "s_mul_i32 s39, s36, 5\n"
  "s_add_u32 s39, s39, s38\n"
  "s_and_b32 s38, s39, 1\n"
  "s_lshr_b32 s39, s39, 1\n"
  "s_lshl_b32 s39, s39, 12\n"
  "v_xor_b32 v7, s38, v5\n"
  "v_lshl_add_u32 v7, v7, 5, v3\n"
  "s_waitcnt lgkmcnt(4)\n"
  "v_mfma_f32_16x16x32_bf16 v[64:67], v[220:223], v[172:175], v[64:67]\n"
  "v_add_u32 v23, s39, v7\n"
  "v_add_u32 v28, 77824, v23\n"
  "v_mfma_f32_16x16x32_bf16 v[96:99], v[220:223], v[188:191], v[96:99]\n"
  "s_add_u32 s38, s37, 4\n"
  "s_cmp_ge_u32 s38, 5\n"
  "s_cselect_b32 s39, 5, 0\n"
  "s_sub_u32 s38, s38, s39\n"
  "s_mul_i32 s39, s36, 5\n"
  "s_add_u32 s39, s39, s38\n"
  "s_and_b32 s38, s39, 1\n"
  "s_lshr_b32 s39, s39, 1\n"
  "s_lshl_b32 s39, s39, 12\n"
  "v_xor_b32 v7, s38, v5\n"
  "v_lshl_add_u32 v7, v7, 5, v3\n"
  "v_mfma_f32_16x16x32_bf16 v[112:115], v[220:223], v[236:239], v[112:115]\n"
  "v_add_u32 v24, s39, v7\n"
  "v_add_u32 v29, 77824, v24\n"
  "s_setprio 3\n"
  "s_waitcnt lgkmcnt(2)\n"
  "v_mfma_f32_16x16x32_bf16 v[68:71], v[224:227], v[172:175], v[68:71]\n"
  "s_mov_b32 s38, 4\n"
  "s_mul_i32 s39, s36, 5\n"
  "s_add_u32 s39, s39, s38\n"
  "s_and_b32 s38, s39, 1\n"
  "s_lshr_b32 s39, s39, 1\n"
  "s_lshl_b32 s39, s39, 12\n"
  "v_xor_b32 v7, s38, v5\n"
  "v_lshl_add_u32 v7, v7, 5, v3\n"
  "v_mfma_f32_16x16x32_bf16 v[100:103], v[224:227], v[188:191], v[100:103]\n"
  "v_add_u32 v30, s39, v7\n"
  "v_add_u32 v31, 77824, v30\n"
  "s_waitcnt lgkmcnt(0)\n"
  "v_mfma_f32_16x16x32_bf16 v[72:75], v[228:231], v[172:175], v[72:75]\n"
  "s_lshl_b32 s38, s37, 12\n"
  "s_add_u32 s38, s38, 20480\n"
  "s_add_u32 s38, s38, %[ldsb]\n"
  "v_add_u32 v244, s38, v17\n"
  "v_add_u32 v245, 77824, v244\n"
  "v_mfma_f32_16x16x32_bf16 v[104:107], v[228:231], v[188:191], v[104:107]\n"
  "s_add_u32 s38, %[ldsb], 36864\n"
  "v_add_u32 v246, s38, v17\n"
  "v_add_u32 v247, 77824, v246\n"
  "s_mul_i32 s38, s36, 77824\n"
  "s_add_u32 s38, s38, 20480\n"
  "s_add_u32 s38, s38, %[ldsb]\n"
  "s_lshl_b32 s39, s37, 12\n"
  "s_add_u32 s39, s39, s38\n"
  "v_add_u32 v13, s39, v17\n"
  "s_mul_i32 s38, s36, 77824\n"
  "s_add_u32 s38, s38, 36864\n"
  "s_add_u32 s38, s38, %[ldsb]\n"
  "s_lshr_b32 s39, s37, 1\n"
  "s_mul_i32 s39, s39, 38912\n"
  "s_add_u32 s38, s38, s39\n"
  "s_and_b32 s39, s37, 1\n"
  "s_lshl_b32 s39, s39, 10\n"
  "s_add_u32 s38, s38, s39\n"
  "v_add_u32 v14, s38, v17\n"
  "v_and_b32 v7, 15, v1\n"
  "v_mul_u32_u24 v7, 2560, v7\n"
  "v_lshrrev_b32 v5, 4, v1\n"
  "v_lshl_add_u32 v15, v5, 3, v7\n"
  "v_mov_b64 v[248:249], 0\n"
  "v_max3_f32 v248, abs(v44), abs(v45), v248\n"
  "v_max3_f32 v249, abs(v46), abs(v47), v249\n"
  "v_max3_f32 v248, abs(v76), abs(v77), v248\n"
  "v_max3_f32 v249, abs(v78), abs(v79), v249\n"
  "v_max3_f32 v248, abs(v48), abs(v49), v248\n"
  "v_max3_f32 v249, abs(v50), abs(v51), v249\n"
  "v_max3_f32 v248, abs(v80), abs(v81), v248\n"
  "v_max3_f32 v249, abs(v82), abs(v83), v249\n"
  "v_max3_f32 v248, abs(v52), abs(v53), v248\n"
  "v_max3_f32 v249, abs(v54), abs(v55), v249\n"
  "v_max3_f32 v248, abs(v84), abs(v85), v248\n"
  "v_max3_f32 v249, abs(v86), abs(v87), v249\n"
  "v_max3_f32 v248, abs(v56), abs(v57), v248\n"
  "v_max3_f32 v249, abs(v58), abs(v59), v249\n"
  "v_max3_f32 v248, abs(v88), abs(v89), v248\n"
  "v_max3_f32 v249, abs(v90), abs(v91), v249\n"
  "v_max3_f32 v248, abs(v60), abs(v61), v248\n"
  "v_max3_f32 v249, abs(v62), abs(v63), v249\n"
  "v_max3_f32 v248, abs(v92), abs(v93), v248\n"
  "v_max3_f32 v249, abs(v94), abs(v95), v249\n"
  "v_max3_f32 v248, abs(v64), abs(v65), v248\n"
  "v_max3_f32 v249, abs(v66), abs(v67), v249\n"
  "v_max3_f32 v248, abs(v96), abs(v97), v248\n"
  "v_max3_f32 v249, abs(v98), abs(v99), v249\n"
  "v_max3_f32 v248, abs(v68), abs(v69), v248\n"
  "v_max3_f32 v249, abs(v70), abs(v71), v249\n"
  "v_max3_f32 v248, abs(v100), abs(v101), v248\n"
  "v_max3_f32 v249, abs(v102), abs(v103), v249\n"
  "v_max3_f32 v248, abs(v72), abs(v73), v248\n"
  "v_max3_f32 v249, abs(v74), abs(v75), v249\n"
  "v_max3_f32 v248, abs(v104), abs(v105), v248\n"
  "v_max3_f32 v249, abs(v106), abs(v107), v249\n"
  "v_max3_f32 v248, abs(v108), abs(v109), v248\n"
  "v_max3_f32 v249, abs(v110), abs(v111), v249\n"
  "v_max3_f32 v248, abs(v112), abs(v113), v248\n"
  "v_max3_f32 v249, abs(v114), abs(v115), v249\n"
  "s_branch P1_EPI\n"
  "P1_W3:\n"
  "ds_read_b128 v[216:219], v12 offset:0\n"
  "ds_read_b128 v[220:223], v12 offset:256\n"
  "ds_read_b128 v[224:227], v12 offset:512\n"
  "ds_read_b128 v[228:231], v12 offset:768\n"
  "s_waitcnt lgkmcnt(3)\n"
  "v_mfma_f32_16x16x32_bf16 v[196:199], v[216:219], v[148:151], 0\n"
  "v_mfma_f32_16x16x32_bf16 v[164:167], v[216:219], v[128:131], 0\n"
  "v_mfma_f32_16x16x32_bf16 v[180:183], v[216:219], v[144:147], 0\n"
  "s_waitcnt lgkmcnt(2)\n"
  "v_mfma_f32_16x16x32_bf16 v[196:199], v[220:223], v[152:155], v[196:199]\n"
  "v_mfma_f32_16x16x32_bf16 v[164:167], v[220:223], v[124:127], v[164:167]\n"
  "v_mfma_f32_16x16x32_bf16 v[180:183], v[220:223], v[140:143], v[180:183]\n"
  "s_waitcnt lgkmcnt(1)\n"
  "v_mfma_f32_16x16x32_bf16 v[196:199], v[224:227], v[156:159], v[196:199]\n"
  "v_mfma_f32_16x16x32_bf16 v[164:167], v[224:227], v[120:123], v[164:167]\n"
  "v_mfma_f32_16x16x32_bf16 v[180:183], v[224:227], v[136:139], v[180:183]\n"
  "s_waitcnt lgkmcnt(0)\n"
  "v_mfma_f32_16x16x32_bf16 v[196:199], v[228:231], v[160:163], v[196:199]\n"
  "v_mfma_f32_16x16x32_bf16 v[164:167], v[228:231], v[116:119], v[164:167]\n"
  "v_mfma_f32_16x16x32_bf16 v[180:183], v[228:231], v[132:135], v[180:183]\n"
  "s_nop 5\n"
  "v_max3_f32 v248, v196, v197, v198\n"
  "v_max_f32 v248, v248, v199\n"
  "v_mov_b32 v249, v248\n"
  "s_nop 1\n"
  "v_permlane32_swap_b32 v248, v249\n"
  "v_max_f32 v248, v248, v249\n"
  "v_mov_b32 v249, v248\n"
  "s_nop 1\n"
  "v_permlane16_swap_b32 v248, v249\n"
  "v_max_f32 v248, v248, v249\n"
  "v_sub_f32 v28, 0, v248\n"
  "v_mov_b32 v29, v28\n"
  "v_mov_b32 v30, v28\n"
  "v_mov_b32 v31, v28\n"
  "v_max3_f32 v248, v164, v165, v166\n"
  "v_max_f32 v248, v248, v167\n"
  "v_mov_b32 v249, v248\n"
  "s_nop 1\n"
  "v_permlane32_swap_b32 v248, v249\n"
  "v_max_f32 v248, v248, v249\n"
  "v_mov_b32 v249, v248\n"
  "s_nop 1\n"
  "v_permlane16_swap_b32 v248, v249\n"
  "v_max_f32 v248, v248, v249\n"
  "v_sub_f32 v20, 0, v248\n"
  "v_mov_b32 v21, v20\n"
  "v_mov_b32 v22, v20\n"
  "v_mov_b32 v23, v20\n"
  "v_max3_f32 v248, v180, v181, v182\n"
  "v_max_f32 v248, v248, v183\n"
  "v_mov_b32 v249, v248\n"
  "s_nop 1\n"
  "v_permlane32_swap_b32 v248, v249\n"
  "v_max_f32 v248, v248, v249\n"
  "v_mov_b32 v249, v248\n"
  "s_nop 1\n"
  "v_permlane16_swap_b32 v248, v249\n"
  "v_max_f32 v248, v248, v249\n"
  "v_sub_f32 v24, 0, v248\n"
  "v_mov_b32 v25, v24\n"
  "v_mov_b32 v26, v24\n"
  "v_mov_b32 v27, v24\n"
  "v_lshrrev_b32 v7, 2, v1\n"
  "v_mul_u32_u24 v7, 1280, v7\n"
  "v_and_b32 v8, 3, v1\n"
  "v_lshl_add_u32 v7, v8, 7, v7\n"
  "s_lshl_b32 s38, %[bb], 3\n"
  "s_add_u32 s38, s38, %[wave]\n"
  "s_mul_i32 s38, s38, 20480\n"
  "buffer_load_dword v254, v7, %[srdwo], s38 offen offset:0\n"
  "s_waitcnt lgkmcnt(0)\n"
  "s_nop 7\n"
  "s_nop 3\n"
  "P1_RST3:\n"
  "ds_read_b128 v[200:203], v12 offset:12288\n"
  "ds_read_b128 v[204:207], v12 offset:12544\n"
  "ds_read_b128 v[208:211], v12 offset:12800\n"
  "ds_read_b128 v[212:215], v12 offset:13056\n"
  "s_nop 7\n"
  "s_nop 3\n"
  "s_mov_b32 s41, 3\n"
  "P1_LOOP3:\n"
  "s_waitcnt lgkmcnt(3)\n"
  "v_mfma_f32_16x16x32_bf16 v[196:199], v[200:203], v[148:151], v[28:31]\n"
  "ds_read_b128 v[216:219], v12 offset:8192\n"
  "v_mfma_f32_16x16x32_bf16 v[164:167], v[200:203], v[128:131], v[20:23]\n"
  "s_setprio 2\n"
  "v_mfma_f32_16x16x32_bf16 v[180:183], v[200:203], v[144:147], v[24:27]\n"
  "ds_read_b128 v[220:223], v12 offset:8448\n"
  "s_waitcnt lgkmcnt(4)\n"
  "v_mfma_f32_16x16x32_bf16 v[196:199], v[204:207], v[152:155], v[196:199]\n"
  "v_mfma_f32_16x16x32_bf16 v[164:167], v[204:207], v[124:127], v[164:167]\n"
  "ds_read_b128 v[224:227], v12 offset:8704\n"
  "v_mfma_f32_16x16x32_bf16 v[180:183], v[204:207], v[140:143], v[180:183]\n"
  "s_waitcnt lgkmcnt(4)\n"
  "v_mfma_f32_16x16x32_bf16 v[196:199], v[208:211], v[156:159], v[196:199]\n"
  "ds_read_b128 v[228:231], v12 offset:8960\n"
  "v_mfma_f32_16x16x32_bf16 v[164:167], v[208:211], v[120:123], v[164:167]\n"
  "v_mfma_f32_16x16x32_bf16 v[180:183], v[208:211], v[136:139], v[180:183]\n"
  "s_waitcnt lgkmcnt(4)\n"
  "v_mfma_f32_16x16x32_bf16 v[196:199], v[212:215], v[160:163], v[196:199]\n"
  "v_mfma_f32_16x16x32_bf16 v[164:167], v[212:215], v[116:119], v[164:167]\n"
  "s_setprio 1\n"
  "v_mfma_f32_16x16x32_bf16 v[180:183], v[212:215], v[132:135], v[180:183]\n"
  "s_waitcnt lgkmcnt(3)\n"
  "v_mfma_f32_16x16x32_bf16 v[168:171], v[216:219], v[128:131], v[20:23]\n"
  "ds_read_b128 v[200:203], v12 offset:4096\n"
  "v_mfma_f32_16x16x32_bf16 v[184:187], v[216:219], v[144:147], v[24:27]\n"
  "s_nop 0\n"
  "v_exp_f32 v196, v196\n"
  "ds_read_b128 v[204:207], v12 offset:4352\n"
  "s_waitcnt lgkmcnt(4)\n"
  "v_mfma_f32_16x16x32_bf16 v[168:171], v[220:223], v[124:127], v[168:171]\n"
  "v_exp_f32 v197, v197\n"
  "ds_read_b128 v[208:211], v12 offset:4608\n"
  "v_mfma_f32_16x16x32_bf16 v[184:187], v[220:223], v[140:143], v[184:187]\n"
  "v_exp_f32 v198, v198\n"
  "ds_read_b128 v[212:215], v12 offset:4864\n"
  "s_waitcnt lgkmcnt(5)\n"
  "v_mfma_f32_16x16x32_bf16 v[168:171], v[224:227], v[120:123], v[168:171]\n"
  "v_exp_f32 v199, v199\n"
  "v_mfma_f32_16x16x32_bf16 v[184:187], v[224:227], v[136:139], v[184:187]\n"
  "v_add_f32 v18, v18, v196\n"
  "v_add_f32 v19, v19, v197\n"
  "s_waitcnt lgkmcnt(4)\n"
  "v_mfma_f32_16x16x32_bf16 v[168:171], v[228:231], v[116:119], v[168:171]\n"
  "v_cvt_pk_bf16_f32 v244, v196, v197\n"
  "v_cvt_pk_bf16_f32 v245, v198, v199\n"
  "ds_write_b64 v15, v[244:245] offset:0\n"
  "s_setprio 0\n"
  "v_mfma_f32_16x16x32_bf16 v[184:187], v[228:231], v[132:135], v[184:187]\n"
  "v_add_f32 v18, v18, v198\n"
  "v_add_f32 v19, v19, v199\n"
  "s_waitcnt lgkmcnt(4)\n"
  "v_mfma_f32_16x16x32_bf16 v[172:175], v[200:203], v[128:131], v[20:23]\n"
  "v_exp_f32 v164, v164\n"
  "ds_read_b128 v[216:219], v12 offset:0\n"
  "v_mfma_f32_16x16x32_bf16 v[188:191], v[200:203], v[144:147], v[24:27]\n"
  "v_exp_f32 v165, v165\n"
  "ds_read_b128 v[220:223], v12 offset:256\n"
  "s_waitcnt lgkmcnt(5)\n"
  "v_mfma_f32_16x16x32_bf16 v[172:175], v[204:207], v[124:127], v[172:175]\n"
  "v_exp_f32 v166, v166\n"
  "ds_read_b128 v[224:227], v12 offset:512\n"
  "v_mfma_f32_16x16x32_bf16 v[188:191], v[204:207], v[140:143], v[188:191]\n"
  "v_exp_f32 v167, v167\n"
  "ds_read_b128 v[228:231], v12 offset:768\n"
  "s_waitcnt lgkmcnt(6)\n"
  "v_mfma_f32_16x16x32_bf16 v[172:175], v[208:211], v[120:123], v[172:175]\n"
  "v_exp_f32 v180, v180\n"
  "v_mfma_f32_16x16x32_bf16 v[188:191], v[208:211], v[136:139], v[188:191]\n"
  "v_exp_f32 v181, v181\n"
  "s_waitcnt lgkmcnt(5)\n"
  "v_mfma_f32_16x16x32_bf16 v[172:175], v[212:215], v[116:119], v[172:175]\n"
  "v_exp_f32 v182, v182\n"
  "v_mfma_f32_16x16x32_bf16 v[188:191], v[212:215], v[132:135], v[188:191]\n"
  "v_exp_f32 v183, v183\n"
  "s_waitcnt vmcnt(8) lgkmcnt(0)\n"
  "s_barrier\n"
  "ds_read_b64_tr_b16 v[200:201], v13 offset:12288\n"
  "ds_read_b64_tr_b16 v[202:203], v13 offset:8192\n"
  "s_setprio 3\n"
  "v_mfma_f32_16x16x32_bf16 v[176:179], v[216:219], v[128:131], v[20:23]\n"
  "v_exp_f32 v168, v168\n"
  "ds_read_b64_tr_b16 v[204:205], v14 offset:12288\n"
  "ds_read_b64_tr_b16 v[206:207], v14 offset:8192\n"
  "s_add_u32 m0, s51, 0\n"
  "s_add_u32 s40, s50, 32768\n"
  "buffer_load_dwordx4 v10, %[srdk], s40 offen offset:0 nt lds\n"
  "v_mfma_f32_16x16x32_bf16 v[192:195], v[216:219], v[144:147], v[24:27]\n"
  "v_exp_f32 v169, v169\n"
  "ds_read_b64_tr_b16 v[208:209], v13 offset:12544\n"
  "ds_read_b64_tr_b16 v[210:211], v13 offset:8448\n"
  "v_mfma_f32_16x16x32_bf16 v[176:179], v[220:223], v[124:127], v[176:179]\n"
  "v_exp_f32 v170, v170\n"
  "ds_read_b64_tr_b16 v[212:213], v14 offset:12544\n"
  "ds_read_b64_tr_b16 v[214:215], v14 offset:8448\n"
  "buffer_load_dwordx4 v11, %[srdk], s40 offen offset:1024 nt lds\n"
  "v_mfma_f32_16x16x32_bf16 v[192:195], v[220:223], v[140:143], v[192:195]\n"
  "v_exp_f32 v171, v171\n"
  "ds_read_b64 v[232:233], v16 offset:1536\n"
  "ds_read_b64 v[234:235], v16 offset:1024\n"
  "v_mfma_f32_16x16x32_bf16 v[176:179], v[224:227], v[120:123], v[176:179]\n"
  "v_cvt_pk_bf16_f32 v164, v164, v165\n"
  "v_cvt_pk_bf16_f32 v165, v166, v167\n"
  "ds_read_b64 v[236:237], v16 offset:512\n"
  "ds_read_b64 v[238:239], v16 offset:0\n"
  "buffer_load_dwordx4 v10, %[srdk], s40 offen offset:2048 nt lds\n"
  "v_mfma_f32_16x16x32_bf16 v[192:195], v[224:227], v[136:139], v[192:195]\n"
  "v_cvt_pk_bf16_f32 v166, v168, v169\n"
  "v_cvt_pk_bf16_f32 v167, v170, v171\n"
  "v_mfma_f32_16x16x32_bf16 v[176:179], v[228:231], v[116:119], v[176:179]\n"
  "v_exp_f32 v184, v184\n"
  "buffer_load_dwordx4 v11, %[srdk], s40 offen offset:3072 nt lds\n"
  "v_mfma_f32_16x16x32_bf16 v[192:195], v[228:231], v[132:135], v[192:195]\n"
  "v_exp_f32 v185, v185\n"
  "s_waitcnt lgkmcnt(10)\n"
  "v_mfma_f32_16x16x32_bf16 v[44:47], v[200:203], v[164:167], v[44:47]\n"
  "v_exp_f32 v186, v186\n"
  "v_mfma_f32_16x16x32_bf16 v[36:39], v[32:35], v[164:167], v[36:39]\n"
  "v_exp_f32 v187, v187\n"
  "s_setprio 2\n"
  "v_cvt_pk_bf16_f32 v180, v180, v181\n"
  "v_cvt_pk_bf16_f32 v181, v182, v183\n"
  "v_cvt_pk_bf16_f32 v182, v184, v185\n"
  "v_cvt_pk_bf16_f32 v183, v186, v187\n"
  "s_nop 1\n"
  "v_mfma_f32_16x16x32_bf16 v[76:79], v[200:203], v[180:183], v[76:79]\n"
  "v_exp_f32 v172, v172\n"
  "v_mfma_f32_16x16x32_bf16 v[40:43], v[32:35], v[180:183], v[40:43]\n"
  "v_exp_f32 v173, v173\n"
  "ds_read_b64_tr_b16 v[216:217], v13 offset:12800\n"
  "ds_read_b64_tr_b16 v[218:219], v13 offset:8704\n"
  "s_waitcnt lgkmcnt(10)\n"
  "v_mfma_f32_16x16x32_bf16 v[48:51], v[204:207], v[164:167], v[48:51]\n"
  "v_exp_f32 v174, v174\n"
  "v_mfma_f32_16x16x32_bf16 v[80:83], v[204:207], v[180:183], v[80:83]\n"
  "v_exp_f32 v175, v175\n"
  "ds_read_b64_tr_b16 v[220:221], v14 offset:12800\n"
  "ds_read_b64_tr_b16 v[222:223], v14 offset:8704\n"
  "s_waitcnt lgkmcnt(10)\n"
  "v_mfma_f32_16x16x32_bf16 v[52:55], v[208:211], v[164:167], v[52:55]\n"
  "v_exp_f32 v188, v188\n"
  "v_mfma_f32_16x16x32_bf16 v[84:87], v[208:211], v[180:183], v[84:87]\n"
  "v_exp_f32 v189, v189\n"
  "ds_read_b64_tr_b16 v[224:225], v13 offset:13056\n"
  "ds_read_b64_tr_b16 v[226:227], v13 offset:8960\n"
  "s_waitcnt lgkmcnt(10)\n"
  "v_mfma_f32_16x16x32_bf16 v[56:59], v[212:215], v[164:167], v[56:59]\n"
  "v_exp_f32 v190, v190\n"
  "v_mfma_f32_16x16x32_bf16 v[88:91], v[212:215], v[180:183], v[88:91]\n"
  "v_exp_f32 v191, v191\n"
  "ds_read_b64_tr_b16 v[228:229], v14 offset:13056\n"
  "ds_read_b64_tr_b16 v[230:231], v14 offset:8960\n"
  "s_waitcnt lgkmcnt(6)\n"
  "v_mfma_f32_16x16x32_bf16 v[60:63], v[216:219], v[164:167], v[60:63]\n"
  "v_exp_f32 v176, v176\n"
  "v_mfma_f32_16x16x32_bf16 v[92:95], v[216:219], v[180:183], v[92:95]\n"
  "v_exp_f32 v177, v177\n"
  "ds_read_b64_tr_b16 v[200:201], v13 offset:4096\n"
  "ds_read_b64_tr_b16 v[202:203], v13 offset:0\n"
  "s_waitcnt lgkmcnt(6)\n"
  "v_mfma_f32_16x16x32_bf16 v[64:67], v[220:223], v[164:167], v[64:67]\n"
  "v_exp_f32 v178, v178\n"
  "s_setprio 1\n"
  "v_mfma_f32_16x16x32_bf16 v[96:99], v[220:223], v[180:183], v[96:99]\n"
  "v_exp_f32 v179, v179\n"
  "ds_read_b64_tr_b16 v[204:205], v14 offset:4096\n"
  "ds_read_b64_tr_b16 v[206:207], v14 offset:0\n"
  "s_waitcnt lgkmcnt(6)\n"
  "v_mfma_f32_16x16x32_bf16 v[68:71], v[224:227], v[164:167], v[68:71]\n"
  "v_cvt_pk_bf16_f32 v172, v172, v173\n"
  "v_cvt_pk_bf16_f32 v173, v174, v175\n"
  "v_mfma_f32_16x16x32_bf16 v[100:103], v[224:227], v[180:183], v[100:103]\n"
  "v_cvt_pk_bf16_f32 v174, v176, v177\n"
  "v_cvt_pk_bf16_f32 v175, v178, v179\n"
  "v_mfma_f32_16x16x32_bf16 v[108:111], v[224:227], v[232:235], v[108:111]\n"
  "v_exp_f32 v192, v192\n"
  "ds_read_b64_tr_b16 v[208:209], v13 offset:4352\n"
  "ds_read_b64_tr_b16 v[210:211], v13 offset:256\n"
  "s_waitcnt lgkmcnt(6)\n"
  "v_mfma_f32_16x16x32_bf16 v[72:75], v[228:231], v[164:167], v[72:75]\n"
  "v_exp_f32 v193, v193\n"
  "v_mfma_f32_16x16x32_bf16 v[104:107], v[228:231], v[180:183], v[104:107]\n"
  "v_exp_f32 v194, v194\n"
  "v_mfma_f32_16x16x32_bf16 v[112:115], v[228:231], v[232:235], v[112:115]\n"
  "v_exp_f32 v195, v195\n"
  "ds_read_b64_tr_b16 v[212:213], v14 offset:4352\n"
  "ds_read_b64_tr_b16 v[214:215], v14 offset:256\n"
  "s_waitcnt lgkmcnt(6)\n"
  "v_mfma_f32_16x16x32_bf16 v[44:47], v[200:203], v[172:175], v[44:47]\n"
  "v_cvt_pk_bf16_f32 v188, v188, v189\n"
  "v_cvt_pk_bf16_f32 v189, v190, v191\n"
  "v_mfma_f32_16x16x32_bf16 v[36:39], v[32:35], v[172:175], v[36:39]\n"
  "v_cvt_pk_bf16_f32 v190, v192, v193\n"
  "v_cvt_pk_bf16_f32 v191, v194, v195\n"
  "s_nop 1\n"
  "v_mfma_f32_16x16x32_bf16 v[76:79], v[200:203], v[188:191], v[76:79]\n"
  "s_setprio 0\n"
  "v_mfma_f32_16x16x32_bf16 v[40:43], v[32:35], v[188:191], v[40:43]\n"
  "ds_read_b64_tr_b16 v[216:217], v13 offset:4608\n"
  "ds_read_b64_tr_b16 v[218:219], v13 offset:512\n"
  "s_waitcnt lgkmcnt(6)\n"
  "v_mfma_f32_16x16x32_bf16 v[48:51], v[204:207], v[172:175], v[48:51]\n"
  "v_mfma_f32_16x16x32_bf16 v[80:83], v[204:207], v[188:191], v[80:83]\n"
  "ds_read_b64_tr_b16 v[220:221], v14 offset:4608\n"
  "ds_read_b64_tr_b16 v[222:223], v14 offset:512\n"
  "s_waitcnt lgkmcnt(6)\n"
  "v_mfma_f32_16x16x32_bf16 v[52:55], v[208:211], v[172:175], v[52:55]\n"
  "v_mfma_f32_16x16x32_bf16 v[84:87], v[208:211], v[188:191], v[84:87]\n"
  "ds_read_b64_tr_b16 v[224:225], v13 offset:4864\n"
  "ds_read_b64_tr_b16 v[226:227], v13 offset:768\n"
  "s_waitcnt lgkmcnt(6)\n"
  "v_mfma_f32_16x16x32_bf16 v[56:59], v[212:215], v[172:175], v[56:59]\n"
  "v_mfma_f32_16x16x32_bf16 v[88:91], v[212:215], v[188:191], v[88:91]\n"
  "ds_read_b64_tr_b16 v[228:229], v14 offset:4864\n"
  "ds_read_b64_tr_b16 v[230:231], v14 offset:768\n"
  "s_waitcnt lgkmcnt(6)\n"
  "v_mfma_f32_16x16x32_bf16 v[60:63], v[216:219], v[172:175], v[60:63]\n"
  "v_mfma_f32_16x16x32_bf16 v[92:95], v[216:219], v[188:191], v[92:95]\n"
  "s_waitcnt lgkmcnt(4)\n"
  "v_mfma_f32_16x16x32_bf16 v[64:67], v[220:223], v[172:175], v[64:67]\n"
  "v_mfma_f32_16x16x32_bf16 v[96:99], v[220:223], v[188:191], v[96:99]\n"
  "s_setprio 3\n"
  "s_waitcnt vmcnt(8) lgkmcnt(0)\n"
  "s_barrier\n"
  "ds_read_b128 v[200:203], v240 offset:12288\n"
  "v_mfma_f32_16x16x32_bf16 v[68:71], v[224:227], v[172:175], v[68:71]\n"
  "ds_read_b128 v[204:207], v240 offset:12544\n"
  "s_add_u32 m0, s51, 16384\n"
  "s_add_u32 s40, s50, 32768\n"
  "buffer_load_dwordx4 v10, %[srdv], s40 offen offset:0 nt lds\n"
  "v_mfma_f32_16x16x32_bf16 v[100:103], v[224:227], v[188:191], v[100:103]\n"
  "ds_read_b128 v[208:211], v240 offset:12800\n"
  "buffer_load_dwordx4 v11, %[srdv], s40 offen offset:1024 nt lds\n"
  "v_mfma_f32_16x16x32_bf16 v[108:111], v[224:227], v[236:239], v[108:111]\n"
  "ds_read_b128 v[212:215], v240 offset:13056\n"
  "buffer_load_dwordx4 v10, %[srdv], s40 offen offset:2048 nt lds\n"
  "v_mfma_f32_16x16x32_bf16 v[72:75], v[228:231], v[172:175], v[72:75]\n"
  "buffer_load_dwordx4 v11, %[srdv], s40 offen offset:3072 nt lds\n"
  "v_mfma_f32_16x16x32_bf16 v[104:107], v[228:231], v[188:191], v[104:107]\n"
  "v_mfma_f32_16x16x32_bf16 v[112:115], v[228:231], v[236:239], v[112:115]\n"
  "s_waitcnt lgkmcnt(3)\n"
  "v_mfma_f32_16x16x32_bf16 v[196:199], v[200:203], v[148:151], v[28:31]\n"
  "ds_read_b128 v[216:219], v240 offset:8192\n"
  "v_mfma_f32_16x16x32_bf16 v[164:167], v[200:203], v[128:131], v[20:23]\n"
  "s_setprio 2\n"
  "v_mfma_f32_16x16x32_bf16 v[180:183], v[200:203], v[144:147], v[24:27]\n"
  "ds_read_b128 v[220:223], v240 offset:8448\n"
  "s_waitcnt lgkmcnt(4)\n"
  "v_mfma_f32_16x16x32_bf16 v[196:199], v[204:207], v[152:155], v[196:199]\n"
  "v_mfma_f32_16x16x32_bf16 v[164:167], v[204:207], v[124:127], v[164:167]\n"
  "ds_read_b128 v[224:227], v240 offset:8704\n"
  "v_mfma_f32_16x16x32_bf16 v[180:183], v[204:207], v[140:143], v[180:183]\n"
  "s_waitcnt lgkmcnt(4)\n"
  "v_mfma_f32_16x16x32_bf16 v[196:199], v[208:211], v[156:159], v[196:199]\n"
  "ds_read_b128 v[228:231], v240 offset:8960\n"
  "v_mfma_f32_16x16x32_bf16 v[164:167], v[208:211], v[120:123], v[164:167]\n"
  "v_mfma_f32_16x16x32_bf16 v[180:183], v[208:211], v[136:139], v[180:183]\n"
  "s_waitcnt lgkmcnt(4)\n"
  "v_mfma_f32_16x16x32_bf16 v[196:199], v[212:215], v[160:163], v[196:199]\n"
  "v_mfma_f32_16x16x32_bf16 v[164:167], v[212:215], v[116:119], v[164:167]\n"
  "s_setprio 1\n"
  "v_mfma_f32_16x16x32_bf16 v[180:183], v[212:215], v[132:135], v[180:183]\n"
  "s_waitcnt lgkmcnt(3)\n"
  "v_mfma_f32_16x16x32_bf16 v[168:171], v[216:219], v[128:131], v[20:23]\n"
  "ds_read_b128 v[200:203], v240 offset:4096\n"
  "v_mfma_f32_16x16x32_bf16 v[184:187], v[216:219], v[144:147], v[24:27]\n"
  "s_nop 0\n"
  "v_exp_f32 v196, v196\n"
  "ds_read_b128 v[204:207], v240 offset:4352\n"
  "s_waitcnt lgkmcnt(4)\n"
  "v_mfma_f32_16x16x32_bf16 v[168:171], v[220:223], v[124:127], v[168:171]\n"
  "v_exp_f32 v197, v197\n"
  "ds_read_b128 v[208:211], v240 offset:4608\n"
  "v_mfma_f32_16x16x32_bf16 v[184:187], v[220:223], v[140:143], v[184:187]\n"
  "v_exp_f32 v198, v198\n"
  "ds_read_b128 v[212:215], v240 offset:4864\n"
  "s_waitcnt lgkmcnt(5)\n"
  "v_mfma_f32_16x16x32_bf16 v[168:171], v[224:227], v[120:123], v[168:171]\n"
  "v_exp_f32 v199, v199\n"
  "v_mfma_f32_16x16x32_bf16 v[184:187], v[224:227], v[136:139], v[184:187]\n"
  "v_add_f32 v18, v18, v196\n"
  "v_add_f32 v19, v19, v197\n"
  "s_waitcnt lgkmcnt(4)\n"
  "v_mfma_f32_16x16x32_bf16 v[168:171], v[228:231], v[116:119], v[168:171]\n"
  "v_cvt_pk_bf16_f32 v244, v196, v197\n"
  "v_cvt_pk_bf16_f32 v245, v198, v199\n"
  "ds_write_b64 v15, v[244:245] offset:0\n"
  "s_setprio 0\n"
  "v_mfma_f32_16x16x32_bf16 v[184:187], v[228:231], v[132:135], v[184:187]\n"
  "v_add_f32 v18, v18, v198\n"
  "v_add_f32 v19, v19, v199\n"
  "s_waitcnt lgkmcnt(4)\n"
  "v_mfma_f32_16x16x32_bf16 v[172:175], v[200:203], v[128:131], v[20:23]\n"
  "v_exp_f32 v164, v164\n"
  "ds_read_b128 v[216:219], v240 offset:0\n"
  "v_mfma_f32_16x16x32_bf16 v[188:191], v[200:203], v[144:147], v[24:27]\n"
  "v_exp_f32 v165, v165\n"
  "ds_read_b128 v[220:223], v240 offset:256\n"
  "s_waitcnt lgkmcnt(5)\n"
  "v_mfma_f32_16x16x32_bf16 v[172:175], v[204:207], v[124:127], v[172:175]\n"
  "v_exp_f32 v166, v166\n"
  "ds_read_b128 v[224:227], v240 offset:512\n"
  "v_mfma_f32_16x16x32_bf16 v[188:191], v[204:207], v[140:143], v[188:191]\n"
  "v_exp_f32 v167, v167\n"
  "ds_read_b128 v[228:231], v240 offset:768\n"
  "s_waitcnt lgkmcnt(6)\n"
  "v_mfma_f32_16x16x32_bf16 v[172:175], v[208:211], v[120:123], v[172:175]\n"
  "v_exp_f32 v180, v180\n"
  "v_mfma_f32_16x16x32_bf16 v[188:191], v[208:211], v[136:139], v[188:191]\n"
  "v_exp_f32 v181, v181\n"
  "s_waitcnt lgkmcnt(5)\n"
  "v_mfma_f32_16x16x32_bf16 v[172:175], v[212:215], v[116:119], v[172:175]\n"
  "v_exp_f32 v182, v182\n"
  "v_mfma_f32_16x16x32_bf16 v[188:191], v[212:215], v[132:135], v[188:191]\n"
  "v_exp_f32 v183, v183\n"
  "s_waitcnt vmcnt(8) lgkmcnt(0)\n"
  "s_barrier\n"
  "ds_read_b64_tr_b16 v[200:201], v241 offset:12288\n"
  "ds_read_b64_tr_b16 v[202:203], v241 offset:8192\n"
  "s_setprio 3\n"
  "v_mfma_f32_16x16x32_bf16 v[176:179], v[216:219], v[128:131], v[20:23]\n"
  "v_exp_f32 v168, v168\n"
  "ds_read_b64_tr_b16 v[204:205], v242 offset:12288\n"
  "ds_read_b64_tr_b16 v[206:207], v242 offset:8192\n"
  "s_add_u32 m0, s51, 65536\n"
  "s_add_u32 s40, s50, 49152\n"
  "buffer_load_dwordx4 v10, %[srdk], s40 offen offset:0 nt lds\n"
  "v_mfma_f32_16x16x32_bf16 v[192:195], v[216:219], v[144:147], v[24:27]\n"
  "v_exp_f32 v169, v169\n"
  "ds_read_b64_tr_b16 v[208:209], v241 offset:12544\n"
  "ds_read_b64_tr_b16 v[210:211], v241 offset:8448\n"
  "v_mfma_f32_16x16x32_bf16 v[176:179], v[220:223], v[124:127], v[176:179]\n"
  "v_exp_f32 v170, v170\n"
  "ds_read_b64_tr_b16 v[212:213], v242 offset:12544\n"
  "ds_read_b64_tr_b16 v[214:215], v242 offset:8448\n"
  "buffer_load_dwordx4 v11, %[srdk], s40 offen offset:1024 nt lds\n"
  "v_mfma_f32_16x16x32_bf16 v[192:195], v[220:223], v[140:143], v[192:195]\n"
  "v_exp_f32 v171, v171\n"
  "ds_read_b64 v[232:233], v16 offset:1536\n"
  "ds_read_b64 v[234:235], v16 offset:1024\n"
  "v_mfma_f32_16x16x32_bf16 v[176:179], v[224:227], v[120:123], v[176:179]\n"
  "v_cvt_pk_bf16_f32 v164, v164, v165\n"
  "v_cvt_pk_bf16_f32 v165, v166, v167\n"
  "ds_read_b64 v[236:237], v16 offset:512\n"
  "ds_read_b64 v[238:239], v16 offset:0\n"
  "buffer_load_dwordx4 v10, %[srdk], s40 offen offset:2048 nt lds\n"
  "v_mfma_f32_16x16x32_bf16 v[192:195], v[224:227], v[136:139], v[192:195]\n"
  "v_cvt_pk_bf16_f32 v166, v168, v169\n"
  "v_cvt_pk_bf16_f32 v167, v170, v171\n"
  "v_mfma_f32_16x16x32_bf16 v[176:179], v[228:231], v[116:119], v[176:179]\n"
  "v_exp_f32 v184, v184\n"
  "buffer_load_dwordx4 v11, %[srdk], s40 offen offset:3072 nt lds\n"
  "v_mfma_f32_16x16x32_bf16 v[192:195], v[228:231], v[132:135], v[192:195]\n"
  "v_exp_f32 v185, v185\n"
  "s_waitcnt lgkmcnt(10)\n"
  "v_mfma_f32_16x16x32_bf16 v[44:47], v[200:203], v[164:167], v[44:47]\n"
  "v_exp_f32 v186, v186\n"
  "v_mfma_f32_16x16x32_bf16 v[36:39], v[32:35], v[164:167], v[36:39]\n"
  "v_exp_f32 v187, v187\n"
  "s_setprio 2\n"
  "v_cvt_pk_bf16_f32 v180, v180, v181\n"
  "v_cvt_pk_bf16_f32 v181, v182, v183\n"
  "v_cvt_pk_bf16_f32 v182, v184, v185\n"
  "v_cvt_pk_bf16_f32 v183, v186, v187\n"
  "s_nop 1\n"
  "v_mfma_f32_16x16x32_bf16 v[76:79], v[200:203], v[180:183], v[76:79]\n"
  "v_exp_f32 v172, v172\n"
  "v_mfma_f32_16x16x32_bf16 v[40:43], v[32:35], v[180:183], v[40:43]\n"
  "v_exp_f32 v173, v173\n"
  "ds_read_b64_tr_b16 v[216:217], v241 offset:12800\n"
  "ds_read_b64_tr_b16 v[218:219], v241 offset:8704\n"
  "s_waitcnt lgkmcnt(10)\n"
  "v_mfma_f32_16x16x32_bf16 v[48:51], v[204:207], v[164:167], v[48:51]\n"
  "v_exp_f32 v174, v174\n"
  "v_mfma_f32_16x16x32_bf16 v[80:83], v[204:207], v[180:183], v[80:83]\n"
  "v_exp_f32 v175, v175\n"
  "ds_read_b64_tr_b16 v[220:221], v242 offset:12800\n"
  "ds_read_b64_tr_b16 v[222:223], v242 offset:8704\n"
  "s_waitcnt lgkmcnt(10)\n"
  "v_mfma_f32_16x16x32_bf16 v[52:55], v[208:211], v[164:167], v[52:55]\n"
  "v_exp_f32 v188, v188\n"
  "v_mfma_f32_16x16x32_bf16 v[84:87], v[208:211], v[180:183], v[84:87]\n"
  "v_exp_f32 v189, v189\n"
  "ds_read_b64_tr_b16 v[224:225], v241 offset:13056\n"
  "ds_read_b64_tr_b16 v[226:227], v241 offset:8960\n"
  "s_waitcnt lgkmcnt(10)\n"
  "v_mfma_f32_16x16x32_bf16 v[56:59], v[212:215], v[164:167], v[56:59]\n"
  "v_exp_f32 v190, v190\n"
  "v_mfma_f32_16x16x32_bf16 v[88:91], v[212:215], v[180:183], v[88:91]\n"
  "v_exp_f32 v191, v191\n"
  "ds_read_b64_tr_b16 v[228:229], v242 offset:13056\n"
  "ds_read_b64_tr_b16 v[230:231], v242 offset:8960\n"
  "s_waitcnt lgkmcnt(6)\n"
  "v_mfma_f32_16x16x32_bf16 v[60:63], v[216:219], v[164:167], v[60:63]\n"
  "v_exp_f32 v176, v176\n"
  "v_mfma_f32_16x16x32_bf16 v[92:95], v[216:219], v[180:183], v[92:95]\n"
  "v_exp_f32 v177, v177\n"
  "ds_read_b64_tr_b16 v[200:201], v241 offset:4096\n"
  "ds_read_b64_tr_b16 v[202:203], v241 offset:0\n"
  "s_waitcnt lgkmcnt(6)\n"
  "v_mfma_f32_16x16x32_bf16 v[64:67], v[220:223], v[164:167], v[64:67]\n"
  "v_exp_f32 v178, v178\n"
  "s_setprio 1\n"
  "v_mfma_f32_16x16x32_bf16 v[96:99], v[220:223], v[180:183], v[96:99]\n"
  "v_exp_f32 v179, v179\n"
  "ds_read_b64_tr_b16 v[204:205], v242 offset:4096\n"
  "ds_read_b64_tr_b16 v[206:207], v242 offset:0\n"
  "s_waitcnt lgkmcnt(6)\n"
  "v_mfma_f32_16x16x32_bf16 v[68:71], v[224:227], v[164:167], v[68:71]\n"
  "v_cvt_pk_bf16_f32 v172, v172, v173\n"
  "v_cvt_pk_bf16_f32 v173, v174, v175\n"
  "v_mfma_f32_16x16x32_bf16 v[100:103], v[224:227], v[180:183], v[100:103]\n"
  "v_cvt_pk_bf16_f32 v174, v176, v177\n"
  "v_cvt_pk_bf16_f32 v175, v178, v179\n"
  "v_mfma_f32_16x16x32_bf16 v[108:111], v[224:227], v[232:235], v[108:111]\n"
  "v_exp_f32 v192, v192\n"
  "ds_read_b64_tr_b16 v[208:209], v241 offset:4352\n"
  "ds_read_b64_tr_b16 v[210:211], v241 offset:256\n"
  "s_waitcnt lgkmcnt(6)\n"
  "v_mfma_f32_16x16x32_bf16 v[72:75], v[228:231], v[164:167], v[72:75]\n"
  "v_exp_f32 v193, v193\n"
  "v_mfma_f32_16x16x32_bf16 v[104:107], v[228:231], v[180:183], v[104:107]\n"
  "v_exp_f32 v194, v194\n"
  "v_mfma_f32_16x16x32_bf16 v[112:115], v[228:231], v[232:235], v[112:115]\n"
  "v_exp_f32 v195, v195\n"
  "ds_read_b64_tr_b16 v[212:213], v242 offset:4352\n"
  "ds_read_b64_tr_b16 v[214:215], v242 offset:256\n"
  "s_waitcnt lgkmcnt(6)\n"
  "v_mfma_f32_16x16x32_bf16 v[44:47], v[200:203], v[172:175], v[44:47]\n"
  "v_cvt_pk_bf16_f32 v188, v188, v189\n"
  "v_cvt_pk_bf16_f32 v189, v190, v191\n"
  "v_mfma_f32_16x16x32_bf16 v[36:39], v[32:35], v[172:175], v[36:39]\n"
  "v_cvt_pk_bf16_f32 v190, v192, v193\n"
  "v_cvt_pk_bf16_f32 v191, v194, v195\n"
  "s_nop 1\n"
  "v_mfma_f32_16x16x32_bf16 v[76:79], v[200:203], v[188:191], v[76:79]\n"
  "s_setprio 0\n"
  "v_mfma_f32_16x16x32_bf16 v[40:43], v[32:35], v[188:191], v[40:43]\n"
  "ds_read_b64_tr_b16 v[216:217], v241 offset:4608\n"
  "ds_read_b64_tr_b16 v[218:219], v241 offset:512\n"
  "s_waitcnt lgkmcnt(6)\n"
  "v_mfma_f32_16x16x32_bf16 v[48:51], v[204:207], v[172:175], v[48:51]\n"
  "v_mfma_f32_16x16x32_bf16 v[80:83], v[204:207], v[188:191], v[80:83]\n"
  "ds_read_b64_tr_b16 v[220:221], v242 offset:4608\n"
  "ds_read_b64_tr_b16 v[222:223], v242 offset:512\n"
  "s_waitcnt lgkmcnt(6)\n"
  "v_mfma_f32_16x16x32_bf16 v[52:55], v[208:211], v[172:175], v[52:55]\n"
  "v_mfma_f32_16x16x32_bf16 v[84:87], v[208:211], v[188:191], v[84:87]\n"
  "ds_read_b64_tr_b16 v[224:225], v241 offset:4864\n"
  "ds_read_b64_tr_b16 v[226:227], v241 offset:768\n"
  "s_waitcnt lgkmcnt(6)\n"
  "v_mfma_f32_16x16x32_bf16 v[56:59], v[212:215], v[172:175], v[56:59]\n"
  "v_mfma_f32_16x16x32_bf16 v[88:91], v[212:215], v[188:191], v[88:91]\n"
  "ds_read_b64_tr_b16 v[228:229], v242 offset:4864\n"
  "ds_read_b64_tr_b16 v[230:231], v242 offset:768\n"
  "s_waitcnt lgkmcnt(6)\n"
  "v_mfma_f32_16x16x32_bf16 v[60:63], v[216:219], v[172:175], v[60:63]\n"
  "v_mfma_f32_16x16x32_bf16 v[92:95], v[216:219], v[188:191], v[92:95]\n"
  "s_waitcnt lgkmcnt(4)\n"
  "v_mfma_f32_16x16x32_bf16 v[64:67], v[220:223], v[172:175], v[64:67]\n"
  "v_mfma_f32_16x16x32_bf16 v[96:99], v[220:223], v[188:191], v[96:99]\n"
  "s_setprio 3\n"
  "s_waitcnt vmcnt(8) lgkmcnt(0)\n"
  "s_barrier\n"
  "ds_read_b128 v[200:203], v12 offset:12288\n"
  "v_mfma_f32_16x16x32_bf16 v[68:71], v[224:227], v[172:175], v[68:71]\n"
  "ds_read_b128 v[204:207], v12 offset:12544\n"
  "s_add_u32 m0, s51, 81920\n"
  "s_add_u32 s40, s50, 49152\n"
  "buffer_load_dwordx4 v10, %[srdv], s40 offen offset:0 nt lds\n"
  "v_mfma_f32_16x16x32_bf16 v[100:103], v[224:227], v[188:191], v[100:103]\n"
  "ds_read_b128 v[208:211], v12 offset:12800\n"
  "buffer_load_dwordx4 v11, %[srdv], s40 offen offset:1024 nt lds\n"
  "v_mfma_f32_16x16x32_bf16 v[108:111], v[224:227], v[236:239], v[108:111]\n"
  "ds_read_b128 v[212:215], v12 offset:13056\n"
  "buffer_load_dwordx4 v10, %[srdv], s40 offen offset:2048 nt lds\n"
  "v_mfma_f32_16x16x32_bf16 v[72:75], v[228:231], v[172:175], v[72:75]\n"
  "buffer_load_dwordx4 v11, %[srdv], s40 offen offset:3072 nt lds\n"
  "v_mfma_f32_16x16x32_bf16 v[104:107], v[228:231], v[188:191], v[104:107]\n"
  "v_mfma_f32_16x16x32_bf16 v[112:115], v[228:231], v[236:239], v[112:115]\n"
  "s_add_u32 s50, s50, 32768\n"
  "s_sub_u32 s41, s41, 1\n"
  "s_cmp_lg_u32 s41, 0\n"
  "s_cbranch_scc1 P1_LOOP3\n"
  "v_lshrrev_b32 v3, 2, v1\n"
  "v_mul_u32_u24 v5, 1280, v3\n"
  "v_bfe_u32 v3, v1, 4, 1\n"
  "v_lshlrev_b32 v3, 1, v3\n"
  "v_and_b32 v7, 3, v1\n"
  "v_xor_b32 v3, v3, v7\n"
  "v_lshl_add_u32 v252, v3, 4, v5\n"
  "s_lshl_b32 s38, %[wave], 1\n"
  "s_and_b32 s39, s38, 3\n"
  "s_mul_i32 s39, s39, 20480\n"
  "s_lshr_b32 s40, s38, 2\n"
  "s_lshl_b32 s40, s40, 6\n"
  "s_add_u32 s52, s39, s40\n"
  "s_add_u32 s38, s38, 1\n"
  "s_and_b32 s39, s38, 3\n"
  "s_mul_i32 s39, s39, 20480\n"
  "s_lshr_b32 s40, s38, 2\n"
  "s_lshl_b32 s40, s40, 6\n"
  "s_add_u32 s53, s39, s40\n"
  "s_lshr_b32 s38, %[wave], 1\n"
  "s_add_u32 s38, s38, 16\n"
  "s_and_b32 s39, s38, 3\n"
  "s_mul_i32 s39, s39, 20480\n"
  "s_lshr_b32 s40, s38, 2\n"
  "s_lshl_b32 s40, s40, 6\n"
  "s_add_u32 s54, s39, s40\n"
  "s_lshl_b32 s44, s38, 10\n"
  "s_add_u32 s44, s44, %[ldsb]\n"
  "s_lshl_b32 s43, %[wave], 11\n"
  "s_add_u32 s43, s43, %[ldsb]\n"
  "s_and_b32 s38, %[wave], 1\n"
  "s_cmp_eq_u32 s38, 0\n"
  "s_cselect_b32 s48, -1, 0\n"
  "s_cselect_b32 s49, 0, -1\n"
  "s_mul_i32 s42, %[jx], 327680\n"
  "s_waitcnt lgkmcnt(3)\n"
  "v_mfma_f32_16x16x32_bf16 v[196:199], v[200:203], v[148:151], v[28:31]\n"
  "ds_read_b128 v[216:219], v12 offset:8192\n"
  "v_mfma_f32_16x16x32_bf16 v[164:167], v[200:203], v[128:131], v[20:23]\n"
  "s_setprio 2\n"
  "v_mfma_f32_16x16x32_bf16 v[180:183], v[200:203], v[144:147], v[24:27]\n"
  "ds_read_b128 v[220:223], v12 offset:8448\n"
  "s_waitcnt lgkmcnt(4)\n"
  "v_mfma_f32_16x16x32_bf16 v[196:199], v[204:207], v[152:155], v[196:199]\n"
  "v_mfma_f32_16x16x32_bf16 v[164:167], v[204:207], v[124:127], v[164:167]\n"
  "ds_read_b128 v[224:227], v12 offset:8704\n"
  "v_mfma_f32_16x16x32_bf16 v[180:183], v[204:207], v[140:143], v[180:183]\n"
  "s_waitcnt lgkmcnt(4)\n"
  "v_mfma_f32_16x16x32_bf16 v[196:199], v[208:211], v[156:159], v[196:199]\n"
  "ds_read_b128 v[228:231], v12 offset:8960\n"
  "v_mfma_f32_16x16x32_bf16 v[164:167], v[208:211], v[120:123], v[164:167]\n"
  "v_mfma_f32_16x16x32_bf16 v[180:183], v[208:211], v[136:139], v[180:183]\n"
  "s_waitcnt lgkmcnt(4)\n"
  "v_mfma_f32_16x16x32_bf16 v[196:199], v[212:215], v[160:163], v[196:199]\n"
  "v_mfma_f32_16x16x32_bf16 v[164:167], v[212:215], v[116:119], v[164:167]\n"
  "s_setprio 1\n"
  "v_mfma_f32_16x16x32_bf16 v[180:183], v[212:215], v[132:135], v[180:183]\n"
  "s_waitcnt lgkmcnt(3)\n"
  "v_mfma_f32_16x16x32_bf16 v[168:171], v[216:219], v[128:131], v[20:23]\n"
  "ds_read_b128 v[200:203], v12 offset:4096\n"
  "v_mfma_f32_16x16x32_bf16 v[184:187], v[216:219], v[144:147], v[24:27]\n"
  "s_nop 0\n"
  "v_exp_f32 v196, v196\n"
  "ds_read_b128 v[204:207], v12 offset:4352\n"
  "s_waitcnt lgkmcnt(4)\n"
  "v_mfma_f32_16x16x32_bf16 v[168:171], v[220:223], v[124:127], v[168:171]\n"
  "v_exp_f32 v197, v197\n"
  "ds_read_b128 v[208:211], v12 offset:4608\n"
  "v_mfma_f32_16x16x32_bf16 v[184:187], v[220:223], v[140:143], v[184:187]\n"
  "v_exp_f32 v198, v198\n"
  "ds_read_b128 v[212:215], v12 offset:4864\n"
  "s_waitcnt lgkmcnt(5)\n"
  "v_mfma_f32_16x16x32_bf16 v[168:171], v[224:227], v[120:123], v[168:171]\n"
  "v_exp_f32 v199, v199\n"
  "v_mfma_f32_16x16x32_bf16 v[184:187], v[224:227], v[136:139], v[184:187]\n"
  "v_add_f32 v18, v18, v196\n"
  "v_add_f32 v19, v19, v197\n"
  "s_waitcnt lgkmcnt(4)\n"
  "v_mfma_f32_16x16x32_bf16 v[168:171], v[228:231], v[116:119], v[168:171]\n"
  "v_cvt_pk_bf16_f32 v244, v196, v197\n"
  "v_cvt_pk_bf16_f32 v245, v198, v199\n"
  "ds_write_b64 v15, v[244:245] offset:0\n"
  "s_setprio 0\n"
  "v_mfma_f32_16x16x32_bf16 v[184:187], v[228:231], v[132:135], v[184:187]\n"
  "v_add_f32 v18, v18, v198\n"
  "v_add_f32 v19, v19, v199\n"
  "s_waitcnt lgkmcnt(4)\n"
  "v_mfma_f32_16x16x32_bf16 v[172:175], v[200:203], v[128:131], v[20:23]\n"
  "v_exp_f32 v164, v164\n"
  "ds_read_b128 v[216:219], v12 offset:0\n"
  "v_mfma_f32_16x16x32_bf16 v[188:191], v[200:203], v[144:147], v[24:27]\n"
  "v_exp_f32 v165, v165\n"
  "ds_read_b128 v[220:223], v12 offset:256\n"
  "s_waitcnt lgkmcnt(5)\n"
  "v_mfma_f32_16x16x32_bf16 v[172:175], v[204:207], v[124:127], v[172:175]\n"
  "v_exp_f32 v166, v166\n"
  "ds_read_b128 v[224:227], v12 offset:512\n"
  "v_mfma_f32_16x16x32_bf16 v[188:191], v[204:207], v[140:143], v[188:191]\n"
  "v_exp_f32 v167, v167\n"
  "ds_read_b128 v[228:231], v12 offset:768\n"
  "s_waitcnt lgkmcnt(6)\n"
  "v_mfma_f32_16x16x32_bf16 v[172:175], v[208:211], v[120:123], v[172:175]\n"
  "v_exp_f32 v180, v180\n"
  "v_mfma_f32_16x16x32_bf16 v[188:191], v[208:211], v[136:139], v[188:191]\n"
  "v_exp_f32 v181, v181\n"
  "s_waitcnt lgkmcnt(5)\n"
  "v_mfma_f32_16x16x32_bf16 v[172:175], v[212:215], v[116:119], v[172:175]\n"
  "v_exp_f32 v182, v182\n"
  "v_mfma_f32_16x16x32_bf16 v[188:191], v[212:215], v[132:135], v[188:191]\n"
  "v_exp_f32 v183, v183\n"
  "s_waitcnt vmcnt(8) lgkmcnt(0)\n"
  "s_barrier\n"
  "ds_read_b64_tr_b16 v[200:201], v13 offset:12288\n"
  "ds_read_b64_tr_b16 v[202:203], v13 offset:8192\n"
  "s_setprio 3\n"
  "v_mfma_f32_16x16x32_bf16 v[176:179], v[216:219], v[128:131], v[20:23]\n"
  "v_exp_f32 v168, v168\n"
  "ds_read_b64_tr_b16 v[204:205], v14 offset:12288\n"
  "ds_read_b64_tr_b16 v[206:207], v14 offset:8192\n"
  "v_mfma_f32_16x16x32_bf16 v[192:195], v[216:219], v[144:147], v[24:27]\n"
  "v_exp_f32 v169, v169\n"
  "ds_read_b64_tr_b16 v[208:209], v13 offset:12544\n"
  "ds_read_b64_tr_b16 v[210:211], v13 offset:8448\n"
  "v_mfma_f32_16x16x32_bf16 v[176:179], v[220:223], v[124:127], v[176:179]\n"
  "v_exp_f32 v170, v170\n"
  "ds_read_b64_tr_b16 v[212:213], v14 offset:12544\n"
  "ds_read_b64_tr_b16 v[214:215], v14 offset:8448\n"
  "v_mfma_f32_16x16x32_bf16 v[192:195], v[220:223], v[140:143], v[192:195]\n"
  "v_exp_f32 v171, v171\n"
  "ds_read_b64 v[232:233], v16 offset:1536\n"
  "ds_read_b64 v[234:235], v16 offset:1024\n"
  "v_mfma_f32_16x16x32_bf16 v[176:179], v[224:227], v[120:123], v[176:179]\n"
  "v_cvt_pk_bf16_f32 v164, v164, v165\n"
  "v_cvt_pk_bf16_f32 v165, v166, v167\n"
  "ds_read_b64 v[236:237], v16 offset:512\n"
  "ds_read_b64 v[238:239], v16 offset:0\n"
  "v_mfma_f32_16x16x32_bf16 v[192:195], v[224:227], v[136:139], v[192:195]\n"
  "v_cvt_pk_bf16_f32 v166, v168, v169\n"
  "v_cvt_pk_bf16_f32 v167, v170, v171\n"
  "v_mfma_f32_16x16x32_bf16 v[176:179], v[228:231], v[116:119], v[176:179]\n"
  "v_exp_f32 v184, v184\n"
  "v_mfma_f32_16x16x32_bf16 v[192:195], v[228:231], v[132:135], v[192:195]\n"
  "v_exp_f32 v185, v185\n"
  "s_waitcnt lgkmcnt(10)\n"
  "v_mfma_f32_16x16x32_bf16 v[44:47], v[200:203], v[164:167], v[44:47]\n"
  "v_exp_f32 v186, v186\n"
  "v_mfma_f32_16x16x32_bf16 v[36:39], v[32:35], v[164:167], v[36:39]\n"
  "v_exp_f32 v187, v187\n"
  "s_setprio 2\n"
  "v_cvt_pk_bf16_f32 v180, v180, v181\n"
  "v_cvt_pk_bf16_f32 v181, v182, v183\n"
  "v_cvt_pk_bf16_f32 v182, v184, v185\n"
  "v_cvt_pk_bf16_f32 v183, v186, v187\n"
  "s_nop 1\n"
  "v_mfma_f32_16x16x32_bf16 v[76:79], v[200:203], v[180:183], v[76:79]\n"
  "v_exp_f32 v172, v172\n"
  "v_mfma_f32_16x16x32_bf16 v[40:43], v[32:35], v[180:183], v[40:43]\n"
  "v_exp_f32 v173, v173\n"
  "ds_read_b64_tr_b16 v[216:217], v13 offset:12800\n"
  "ds_read_b64_tr_b16 v[218:219], v13 offset:8704\n"
  "s_waitcnt lgkmcnt(10)\n"
  "v_mfma_f32_16x16x32_bf16 v[48:51], v[204:207], v[164:167], v[48:51]\n"
  "v_exp_f32 v174, v174\n"
  "v_mfma_f32_16x16x32_bf16 v[80:83], v[204:207], v[180:183], v[80:83]\n"
  "v_exp_f32 v175, v175\n"
  "ds_read_b64_tr_b16 v[220:221], v14 offset:12800\n"
  "ds_read_b64_tr_b16 v[222:223], v14 offset:8704\n"
  "s_waitcnt lgkmcnt(10)\n"
  "v_mfma_f32_16x16x32_bf16 v[52:55], v[208:211], v[164:167], v[52:55]\n"
  "v_exp_f32 v188, v188\n"
  "v_mfma_f32_16x16x32_bf16 v[84:87], v[208:211], v[180:183], v[84:87]\n"
  "v_exp_f32 v189, v189\n"
  "ds_read_b64_tr_b16 v[224:225], v13 offset:13056\n"
  "ds_read_b64_tr_b16 v[226:227], v13 offset:8960\n"
  "s_waitcnt lgkmcnt(10)\n"
  "v_mfma_f32_16x16x32_bf16 v[56:59], v[212:215], v[164:167], v[56:59]\n"
  "v_exp_f32 v190, v190\n"
  "v_mfma_f32_16x16x32_bf16 v[88:91], v[212:215], v[180:183], v[88:91]\n"
  "v_exp_f32 v191, v191\n"
  "ds_read_b64_tr_b16 v[228:229], v14 offset:13056\n"
  "ds_read_b64_tr_b16 v[230:231], v14 offset:8960\n"
  "s_waitcnt lgkmcnt(6)\n"
  "v_mfma_f32_16x16x32_bf16 v[60:63], v[216:219], v[164:167], v[60:63]\n"
  "v_exp_f32 v176, v176\n"
  "v_mfma_f32_16x16x32_bf16 v[92:95], v[216:219], v[180:183], v[92:95]\n"
  "v_exp_f32 v177, v177\n"
  "ds_read_b64_tr_b16 v[200:201], v13 offset:4096\n"
  "ds_read_b64_tr_b16 v[202:203], v13 offset:0\n"
  "s_waitcnt lgkmcnt(6)\n"
  "v_mfma_f32_16x16x32_bf16 v[64:67], v[220:223], v[164:167], v[64:67]\n"
  "v_exp_f32 v178, v178\n"
  "s_setprio 1\n"
  "v_mfma_f32_16x16x32_bf16 v[96:99], v[220:223], v[180:183], v[96:99]\n"
  "v_exp_f32 v179, v179\n"
  "ds_read_b64_tr_b16 v[204:205], v14 offset:4096\n"
  "ds_read_b64_tr_b16 v[206:207], v14 offset:0\n"
  "s_waitcnt lgkmcnt(6)\n"
  "v_mfma_f32_16x16x32_bf16 v[68:71], v[224:227], v[164:167], v[68:71]\n"
  "v_cvt_pk_bf16_f32 v172, v172, v173\n"
  "v_cvt_pk_bf16_f32 v173, v174, v175\n"
  "v_mfma_f32_16x16x32_bf16 v[100:103], v[224:227], v[180:183], v[100:103]\n"
  "v_cvt_pk_bf16_f32 v174, v176, v177\n"
  "v_cvt_pk_bf16_f32 v175, v178, v179\n"
  "v_mfma_f32_16x16x32_bf16 v[108:111], v[224:227], v[232:235], v[108:111]\n"
  "v_exp_f32 v192, v192\n"
  "ds_read_b64_tr_b16 v[208:209], v13 offset:4352\n"
  "ds_read_b64_tr_b16 v[210:211], v13 offset:256\n"
  "s_waitcnt lgkmcnt(6)\n"
  "v_mfma_f32_16x16x32_bf16 v[72:75], v[228:231], v[164:167], v[72:75]\n"
  "v_exp_f32 v193, v193\n"
  "v_mfma_f32_16x16x32_bf16 v[104:107], v[228:231], v[180:183], v[104:107]\n"
  "v_exp_f32 v194, v194\n"
  "v_mfma_f32_16x16x32_bf16 v[112:115], v[228:231], v[232:235], v[112:115]\n"
  "v_exp_f32 v195, v195\n"
  "ds_read_b64_tr_b16 v[212:213], v14 offset:4352\n"
  "ds_read_b64_tr_b16 v[214:215], v14 offset:256\n"
  "s_waitcnt lgkmcnt(6)\n"
  "v_mfma_f32_16x16x32_bf16 v[44:47], v[200:203], v[172:175], v[44:47]\n"
  "v_cvt_pk_bf16_f32 v188, v188, v189\n"
  "v_cvt_pk_bf16_f32 v189, v190, v191\n"
  "v_mfma_f32_16x16x32_bf16 v[36:39], v[32:35], v[172:175], v[36:39]\n"
  "v_cvt_pk_bf16_f32 v190, v192, v193\n"
  "v_cvt_pk_bf16_f32 v191, v194, v195\n"
  "s_nop 1\n"
  "v_mfma_f32_16x16x32_bf16 v[76:79], v[200:203], v[188:191], v[76:79]\n"
  "s_setprio 0\n"
  "v_mfma_f32_16x16x32_bf16 v[40:43], v[32:35], v[188:191], v[40:43]\n"
  "ds_read_b64_tr_b16 v[216:217], v13 offset:4608\n"
  "ds_read_b64_tr_b16 v[218:219], v13 offset:512\n"
  "s_waitcnt lgkmcnt(6)\n"
  "v_mfma_f32_16x16x32_bf16 v[48:51], v[204:207], v[172:175], v[48:51]\n"
  "v_mfma_f32_16x16x32_bf16 v[80:83], v[204:207], v[188:191], v[80:83]\n"
  "ds_read_b64_tr_b16 v[220:221], v14 offset:4608\n"
  "ds_read_b64_tr_b16 v[222:223], v14 offset:512\n"
  "s_waitcnt lgkmcnt(6)\n"
  "v_mfma_f32_16x16x32_bf16 v[52:55], v[208:211], v[172:175], v[52:55]\n"
  "v_mfma_f32_16x16x32_bf16 v[84:87], v[208:211], v[188:191], v[84:87]\n"
  "ds_read_b64_tr_b16 v[224:225], v13 offset:4864\n"
  "ds_read_b64_tr_b16 v[226:227], v13 offset:768\n"
  "s_waitcnt lgkmcnt(6)\n"
  "v_mfma_f32_16x16x32_bf16 v[56:59], v[212:215], v[172:175], v[56:59]\n"
  "v_mfma_f32_16x16x32_bf16 v[88:91], v[212:215], v[188:191], v[88:91]\n"
  "ds_read_b64_tr_b16 v[228:229], v14 offset:4864\n"
  "ds_read_b64_tr_b16 v[230:231], v14 offset:768\n"
  "s_waitcnt lgkmcnt(6)\n"
  "v_mfma_f32_16x16x32_bf16 v[60:63], v[216:219], v[172:175], v[60:63]\n"
  "v_mfma_f32_16x16x32_bf16 v[92:95], v[216:219], v[188:191], v[92:95]\n"
  "s_waitcnt lgkmcnt(4)\n"
  "v_mfma_f32_16x16x32_bf16 v[64:67], v[220:223], v[172:175], v[64:67]\n"
  "v_mfma_f32_16x16x32_bf16 v[96:99], v[220:223], v[188:191], v[96:99]\n"
  "s_setprio 3\n"
  "s_waitcnt vmcnt(4) lgkmcnt(0)\n"
  "s_barrier\n"
  "ds_read_b128 v[200:203], v240 offset:12288\n"
  "v_mfma_f32_16x16x32_bf16 v[68:71], v[224:227], v[172:175], v[68:71]\n"
  "ds_read_b128 v[204:207], v240 offset:12544\n"
  "s_add_u32 m0, s43, 0\n"
  "s_add_u32 s40, s42, s52\n"
  "buffer_load_dwordx4 v252, %[srdwo], s40 offen offset:0 lds\n"
  "v_mfma_f32_16x16x32_bf16 v[100:103], v[224:227], v[188:191], v[100:103]\n"
  "ds_read_b128 v[208:211], v240 offset:12800\n"
  "s_add_u32 m0, s43, 1024\n"
  "s_add_u32 s40, s42, s53\n"
  "buffer_load_dwordx4 v252, %[srdwo], s40 offen offset:0 lds\n"
  "v_mfma_f32_16x16x32_bf16 v[108:111], v[224:227], v[236:239], v[108:111]\n"
  "ds_read_b128 v[212:215], v240 offset:13056\n"
  "s_add_u32 m0, s44, 0\n"
  "s_add_u32 s40, s42, s54\n"
  "s_mov_b64 exec, s[48:49]\n"
  "buffer_load_dwordx4 v252, %[srdwo], s40 offen offset:0 lds\n"
  "s_mov_b64 exec, -1\n"
  "s_add_u32 s42, s42, 81920\n"
  "s_cmp_ge_u32 s42, 1310720\n"
  "s_cselect_b32 s40, 1310720, 0\n"
  "s_sub_u32 s42, s42, s40\n"
  "v_mfma_f32_16x16x32_bf16 v[72:75], v[228:231], v[172:175], v[72:75]\n"
  "s_add_u32 m0, s43, 38912\n"
  "s_add_u32 s40, s42, s52\n"
  "buffer_load_dwordx4 v252, %[srdwo], s40 offen offset:0 lds\n"
  "v_mfma_f32_16x16x32_bf16 v[104:107], v[228:231], v[188:191], v[104:107]\n"
  "s_add_u32 m0, s43, 39936\n"
  "s_add_u32 s40, s42, s53\n"
  "buffer_load_dwordx4 v252, %[srdwo], s40 offen offset:0 lds\n"
  "v_mfma_f32_16x16x32_bf16 v[112:115], v[228:231], v[236:239], v[112:115]\n"
  "s_add_u32 m0, s44, 38912\n"
  "s_add_u32 s40, s42, s54\n"
  "s_mov_b64 exec, s[48:49]\n"
  "buffer_load_dwordx4 v252, %[srdwo], s40 offen offset:0 lds\n"
  "s_mov_b64 exec, -1\n"
  "s_add_u32 s42, s42, 81920\n"
  "s_cmp_ge_u32 s42, 1310720\n"
  "s_cselect_b32 s40, 1310720, 0\n"
  "s_sub_u32 s42, s42, s40\n"
  "s_waitcnt lgkmcnt(3)\n"
  "v_mfma_f32_16x16x32_bf16 v[196:199], v[200:203], v[148:151], v[28:31]\n"
  "ds_read_b128 v[216:219], v240 offset:8192\n"
  "v_mfma_f32_16x16x32_bf16 v[164:167], v[200:203], v[128:131], v[20:23]\n"
  "s_setprio 2\n"
  "v_mfma_f32_16x16x32_bf16 v[180:183], v[200:203], v[144:147], v[24:27]\n"
  "ds_read_b128 v[220:223], v240 offset:8448\n"
  "s_waitcnt lgkmcnt(4)\n"
  "v_mfma_f32_16x16x32_bf16 v[196:199], v[204:207], v[152:155], v[196:199]\n"
  "v_mfma_f32_16x16x32_bf16 v[164:167], v[204:207], v[124:127], v[164:167]\n"
  "ds_read_b128 v[224:227], v240 offset:8704\n"
  "v_mfma_f32_16x16x32_bf16 v[180:183], v[204:207], v[140:143], v[180:183]\n"
  "s_waitcnt lgkmcnt(4)\n"
  "v_mfma_f32_16x16x32_bf16 v[196:199], v[208:211], v[156:159], v[196:199]\n"
  "ds_read_b128 v[228:231], v240 offset:8960\n"
  "v_mfma_f32_16x16x32_bf16 v[164:167], v[208:211], v[120:123], v[164:167]\n"
  "v_mfma_f32_16x16x32_bf16 v[180:183], v[208:211], v[136:139], v[180:183]\n"
  "s_waitcnt lgkmcnt(4)\n"
  "v_mfma_f32_16x16x32_bf16 v[196:199], v[212:215], v[160:163], v[196:199]\n"
  "v_mfma_f32_16x16x32_bf16 v[164:167], v[212:215], v[116:119], v[164:167]\n"
  "s_setprio 1\n"
  "v_mfma_f32_16x16x32_bf16 v[180:183], v[212:215], v[132:135], v[180:183]\n"
  "s_waitcnt lgkmcnt(3)\n"
  "v_mfma_f32_16x16x32_bf16 v[168:171], v[216:219], v[128:131], v[20:23]\n"
  "ds_read_b128 v[200:203], v240 offset:4096\n"
  "v_mfma_f32_16x16x32_bf16 v[184:187], v[216:219], v[144:147], v[24:27]\n"
  "s_nop 0\n"
  "v_exp_f32 v196, v196\n"
  "ds_read_b128 v[204:207], v240 offset:4352\n"
  "s_waitcnt lgkmcnt(4)\n"
  "v_mfma_f32_16x16x32_bf16 v[168:171], v[220:223], v[124:127], v[168:171]\n"
  "v_exp_f32 v197, v197\n"
  "ds_read_b128 v[208:211], v240 offset:4608\n"
  "v_mfma_f32_16x16x32_bf16 v[184:187], v[220:223], v[140:143], v[184:187]\n"
  "v_exp_f32 v198, v198\n"
  "ds_read_b128 v[212:215], v240 offset:4864\n"
  "s_waitcnt lgkmcnt(5)\n"
  "v_mfma_f32_16x16x32_bf16 v[168:171], v[224:227], v[120:123], v[168:171]\n"
  "v_exp_f32 v199, v199\n"
  "v_mfma_f32_16x16x32_bf16 v[184:187], v[224:227], v[136:139], v[184:187]\n"
  "v_add_f32 v18, v18, v196\n"
  "v_add_f32 v19, v19, v197\n"
  "s_waitcnt lgkmcnt(4)\n"
  "v_mfma_f32_16x16x32_bf16 v[168:171], v[228:231], v[116:119], v[168:171]\n"
  "v_cvt_pk_bf16_f32 v244, v196, v197\n"
  "v_cvt_pk_bf16_f32 v245, v198, v199\n"
  "ds_write_b64 v15, v[244:245] offset:0\n"
  "s_setprio 0\n"
  "v_mfma_f32_16x16x32_bf16 v[184:187], v[228:231], v[132:135], v[184:187]\n"
  "v_add_f32 v18, v18, v198\n"
  "v_add_f32 v19, v19, v199\n"
  "s_waitcnt lgkmcnt(4)\n"
  "v_mfma_f32_16x16x32_bf16 v[172:175], v[200:203], v[128:131], v[20:23]\n"
  "v_exp_f32 v164, v164\n"
  "ds_read_b128 v[216:219], v240 offset:0\n"
  "v_mfma_f32_16x16x32_bf16 v[188:191], v[200:203], v[144:147], v[24:27]\n"
  "v_exp_f32 v165, v165\n"
  "ds_read_b128 v[220:223], v240 offset:256\n"
  "s_waitcnt lgkmcnt(5)\n"
  "v_mfma_f32_16x16x32_bf16 v[172:175], v[204:207], v[124:127], v[172:175]\n"
  "v_exp_f32 v166, v166\n"
  "ds_read_b128 v[224:227], v240 offset:512\n"
  "v_mfma_f32_16x16x32_bf16 v[188:191], v[204:207], v[140:143], v[188:191]\n"
  "v_exp_f32 v167, v167\n"
  "ds_read_b128 v[228:231], v240 offset:768\n"
  "s_waitcnt lgkmcnt(6)\n"
  "v_mfma_f32_16x16x32_bf16 v[172:175], v[208:211], v[120:123], v[172:175]\n"
  "v_exp_f32 v180, v180\n"
  "v_mfma_f32_16x16x32_bf16 v[188:191], v[208:211], v[136:139], v[188:191]\n"
  "v_exp_f32 v181, v181\n"
  "s_waitcnt lgkmcnt(5)\n"
  "v_mfma_f32_16x16x32_bf16 v[172:175], v[212:215], v[116:119], v[172:175]\n"
  "v_exp_f32 v182, v182\n"
  "v_mfma_f32_16x16x32_bf16 v[188:191], v[212:215], v[132:135], v[188:191]\n"
  "v_exp_f32 v183, v183\n"
  "s_waitcnt vmcnt(6) lgkmcnt(0)\n"
  "s_barrier\n"
  "ds_read_b64_tr_b16 v[200:201], v241 offset:12288\n"
  "ds_read_b64_tr_b16 v[202:203], v241 offset:8192\n"
  "s_setprio 3\n"
  "v_mfma_f32_16x16x32_bf16 v[176:179], v[216:219], v[128:131], v[20:23]\n"
  "v_exp_f32 v168, v168\n"
  "ds_read_b64_tr_b16 v[204:205], v242 offset:12288\n"
  "ds_read_b64_tr_b16 v[206:207], v242 offset:8192\n"
  "v_mfma_f32_16x16x32_bf16 v[192:195], v[216:219], v[144:147], v[24:27]\n"
  "v_exp_f32 v169, v169\n"
  "ds_read_b64_tr_b16 v[208:209], v241 offset:12544\n"
  "ds_read_b64_tr_b16 v[210:211], v241 offset:8448\n"
  "v_mfma_f32_16x16x32_bf16 v[176:179], v[220:223], v[124:127], v[176:179]\n"
  "v_exp_f32 v170, v170\n"
  "ds_read_b64_tr_b16 v[212:213], v242 offset:12544\n"
  "ds_read_b64_tr_b16 v[214:215], v242 offset:8448\n"
  "v_mfma_f32_16x16x32_bf16 v[192:195], v[220:223], v[140:143], v[192:195]\n"
  "v_exp_f32 v171, v171\n"
  "ds_read_b64 v[232:233], v16 offset:1536\n"
  "ds_read_b64 v[234:235], v16 offset:1024\n"
  "v_mfma_f32_16x16x32_bf16 v[176:179], v[224:227], v[120:123], v[176:179]\n"
  "v_cvt_pk_bf16_f32 v164, v164, v165\n"
  "v_cvt_pk_bf16_f32 v165, v166, v167\n"
  "ds_read_b64 v[236:237], v16 offset:512\n"
  "ds_read_b64 v[238:239], v16 offset:0\n"
  "v_mfma_f32_16x16x32_bf16 v[192:195], v[224:227], v[136:139], v[192:195]\n"
  "v_cvt_pk_bf16_f32 v166, v168, v169\n"
  "v_cvt_pk_bf16_f32 v167, v170, v171\n"
  "v_mfma_f32_16x16x32_bf16 v[176:179], v[228:231], v[116:119], v[176:179]\n"
  "v_exp_f32 v184, v184\n"
  "v_mfma_f32_16x16x32_bf16 v[192:195], v[228:231], v[132:135], v[192:195]\n"
  "v_exp_f32 v185, v185\n"
  "s_waitcnt lgkmcnt(10)\n"
  "v_mfma_f32_16x16x32_bf16 v[44:47], v[200:203], v[164:167], v[44:47]\n"
  "v_exp_f32 v186, v186\n"
  "v_mfma_f32_16x16x32_bf16 v[36:39], v[32:35], v[164:167], v[36:39]\n"
  "v_exp_f32 v187, v187\n"
  "s_setprio 2\n"
  "v_cvt_pk_bf16_f32 v180, v180, v181\n"
  "v_cvt_pk_bf16_f32 v181, v182, v183\n"
  "v_cvt_pk_bf16_f32 v182, v184, v185\n"
  "v_cvt_pk_bf16_f32 v183, v186, v187\n"
  "s_nop 1\n"
  "v_mfma_f32_16x16x32_bf16 v[76:79], v[200:203], v[180:183], v[76:79]\n"
  "v_exp_f32 v172, v172\n"
  "v_mfma_f32_16x16x32_bf16 v[40:43], v[32:35], v[180:183], v[40:43]\n"
  "v_exp_f32 v173, v173\n"
  "ds_read_b64_tr_b16 v[216:217], v241 offset:12800\n"
  "ds_read_b64_tr_b16 v[218:219], v241 offset:8704\n"
  "s_waitcnt lgkmcnt(10)\n"
  "v_mfma_f32_16x16x32_bf16 v[48:51], v[204:207], v[164:167], v[48:51]\n"
  "v_exp_f32 v174, v174\n"
  "v_mfma_f32_16x16x32_bf16 v[80:83], v[204:207], v[180:183], v[80:83]\n"
  "v_exp_f32 v175, v175\n"
  "ds_read_b64_tr_b16 v[220:221], v242 offset:12800\n"
  "ds_read_b64_tr_b16 v[222:223], v242 offset:8704\n"
  "s_waitcnt lgkmcnt(10)\n"
  "v_mfma_f32_16x16x32_bf16 v[52:55], v[208:211], v[164:167], v[52:55]\n"
  "v_exp_f32 v188, v188\n"
  "v_mfma_f32_16x16x32_bf16 v[84:87], v[208:211], v[180:183], v[84:87]\n"
  "v_exp_f32 v189, v189\n"
  "ds_read_b64_tr_b16 v[224:225], v241 offset:13056\n"
  "ds_read_b64_tr_b16 v[226:227], v241 offset:8960\n"
  "s_waitcnt lgkmcnt(10)\n"
  "v_mfma_f32_16x16x32_bf16 v[56:59], v[212:215], v[164:167], v[56:59]\n"
  "v_exp_f32 v190, v190\n"
  "v_mfma_f32_16x16x32_bf16 v[88:91], v[212:215], v[180:183], v[88:91]\n"
  "v_exp_f32 v191, v191\n"
  "ds_read_b64_tr_b16 v[228:229], v242 offset:13056\n"
  "ds_read_b64_tr_b16 v[230:231], v242 offset:8960\n"
  "s_waitcnt lgkmcnt(6)\n"
  "v_mfma_f32_16x16x32_bf16 v[60:63], v[216:219], v[164:167], v[60:63]\n"
  "v_exp_f32 v176, v176\n"
  "v_mfma_f32_16x16x32_bf16 v[92:95], v[216:219], v[180:183], v[92:95]\n"
  "v_exp_f32 v177, v177\n"
  "ds_read_b64_tr_b16 v[200:201], v241 offset:4096\n"
  "ds_read_b64_tr_b16 v[202:203], v241 offset:0\n"
  "s_waitcnt lgkmcnt(6)\n"
  "v_mfma_f32_16x16x32_bf16 v[64:67], v[220:223], v[164:167], v[64:67]\n"
  "v_exp_f32 v178, v178\n"
  "s_setprio 1\n"
  "v_mfma_f32_16x16x32_bf16 v[96:99], v[220:223], v[180:183], v[96:99]\n"
  "v_exp_f32 v179, v179\n"
  "ds_read_b64_tr_b16 v[204:205], v242 offset:4096\n"
  "ds_read_b64_tr_b16 v[206:207], v242 offset:0\n"
  "s_waitcnt lgkmcnt(6)\n"
  "v_mfma_f32_16x16x32_bf16 v[68:71], v[224:227], v[164:167], v[68:71]\n"
  "v_cvt_pk_bf16_f32 v172, v172, v173\n"
  "v_cvt_pk_bf16_f32 v173, v174, v175\n"
  "v_mfma_f32_16x16x32_bf16 v[100:103], v[224:227], v[180:183], v[100:103]\n"
  "v_cvt_pk_bf16_f32 v174, v176, v177\n"
  "v_cvt_pk_bf16_f32 v175, v178, v179\n"
  "v_mfma_f32_16x16x32_bf16 v[108:111], v[224:227], v[232:235], v[108:111]\n"
  "v_exp_f32 v192, v192\n"
  "ds_read_b64_tr_b16 v[208:209], v241 offset:4352\n"
  "ds_read_b64_tr_b16 v[210:211], v241 offset:256\n"
  "s_waitcnt lgkmcnt(6)\n"
  "v_mfma_f32_16x16x32_bf16 v[72:75], v[228:231], v[164:167], v[72:75]\n"
  "v_exp_f32 v193, v193\n"
  "v_mfma_f32_16x16x32_bf16 v[104:107], v[228:231], v[180:183], v[104:107]\n"
  "v_exp_f32 v194, v194\n"
  "v_mfma_f32_16x16x32_bf16 v[112:115], v[228:231], v[232:235], v[112:115]\n"
  "v_exp_f32 v195, v195\n"
  "ds_read_b64_tr_b16 v[212:213], v242 offset:4352\n"
  "ds_read_b64_tr_b16 v[214:215], v242 offset:256\n"
  "s_waitcnt lgkmcnt(6)\n"
  "v_mfma_f32_16x16x32_bf16 v[44:47], v[200:203], v[172:175], v[44:47]\n"
  "v_cvt_pk_bf16_f32 v188, v188, v189\n"
  "v_cvt_pk_bf16_f32 v189, v190, v191\n"
  "v_mfma_f32_16x16x32_bf16 v[36:39], v[32:35], v[172:175], v[36:39]\n"
  "v_cvt_pk_bf16_f32 v190, v192, v193\n"
  "v_cvt_pk_bf16_f32 v191, v194, v195\n"
  "s_nop 1\n"
  "v_mfma_f32_16x16x32_bf16 v[76:79], v[200:203], v[188:191], v[76:79]\n"
  "v_lshrrev_b32 v3, 2, v1\n"
  "v_lshlrev_b32 v3, 6, v3\n"
  "s_setprio 0\n"
  "v_mfma_f32_16x16x32_bf16 v[40:43], v[32:35], v[188:191], v[40:43]\n"
  "v_bfe_u32 v5, v1, 1, 1\n"
  "v_lshl_add_u32 v3, v5, 4, v3\n"
  "ds_read_b64_tr_b16 v[216:217], v241 offset:4608\n"
  "ds_read_b64_tr_b16 v[218:219], v241 offset:512\n"
  "s_waitcnt lgkmcnt(6)\n"
  "v_mfma_f32_16x16x32_bf16 v[48:51], v[204:207], v[172:175], v[48:51]\n"
  "v_and_b32 v5, 1, v1\n"
  "v_lshl_add_u32 v3, v5, 3, v3\n"
  "v_mfma_f32_16x16x32_bf16 v[80:83], v[204:207], v[188:191], v[80:83]\n"
  "v_bfe_u32 v5, v1, 4, 1\n"
  "v_add_u32 v3, %[ldsb], v3\n"
  "ds_read_b64_tr_b16 v[220:221], v242 offset:4608\n"
  "ds_read_b64_tr_b16 v[222:223], v242 offset:512\n"
  "s_waitcnt lgkmcnt(6)\n"
  "v_mfma_f32_16x16x32_bf16 v[52:55], v[208:211], v[172:175], v[52:55]\n"
  "s_add_u32 s38, s37, 0\n"
  "s_cmp_ge_u32 s38, 5\n"
  "s_cselect_b32 s39, 5, 0\n"
  "s_sub_u32 s38, s38, s39\n"
  "s_mul_i32 s39, s36, 5\n"
  "s_add_u32 s39, s39, s38\n"
  "s_and_b32 s38, s39, 1\n"
  "s_lshr_b32 s39, s39, 1\n"
  "s_lshl_b32 s39, s39, 12\n"
  "v_xor_b32 v7, s38, v5\n"
  "v_lshl_add_u32 v7, v7, 5, v3\n"
  "v_mfma_f32_16x16x32_bf16 v[84:87], v[208:211], v[188:191], v[84:87]\n"
  "v_add_u32 v20, s39, v7\n"
  "v_add_u32 v25, 77824, v20\n"
  "ds_read_b64_tr_b16 v[224:225], v241 offset:4864\n"
  "ds_read_b64_tr_b16 v[226:227], v241 offset:768\n"
  "s_waitcnt lgkmcnt(6)\n"
  "v_mfma_f32_16x16x32_bf16 v[56:59], v[212:215], v[172:175], v[56:59]\n"
  "s_add_u32 s38, s37, 1\n"
  "s_cmp_ge_u32 s38, 5\n"
  "s_cselect_b32 s39, 5, 0\n"
  "s_sub_u32 s38, s38, s39\n"
  "s_mul_i32 s39, s36, 5\n"
  "s_add_u32 s39, s39, s38\n"
  "s_and_b32 s38, s39, 1\n"
  "s_lshr_b32 s39, s39, 1\n"
  "s_lshl_b32 s39, s39, 12\n"
  "v_xor_b32 v7, s38, v5\n"
  "v_lshl_add_u32 v7, v7, 5, v3\n"
  "v_mfma_f32_16x16x32_bf16 v[88:91], v[212:215], v[188:191], v[88:91]\n"
  "v_add_u32 v21, s39, v7\n"
  "v_add_u32 v26, 77824, v21\n"
  "ds_read_b64_tr_b16 v[228:229], v242 offset:4864\n"
  "ds_read_b64_tr_b16 v[230:231], v242 offset:768\n"
  "s_waitcnt lgkmcnt(6)\n"
  "v_mfma_f32_16x16x32_bf16 v[60:63], v[216:219], v[172:175], v[60:63]\n"
  "s_add_u32 s38, s37, 2\n"
  "s_cmp_ge_u32 s38, 5\n"
  "s_cselect_b32 s39, 5, 0\n"
  "s_sub_u32 s38, s38, s39\n"
  "s_mul_i32 s39, s36, 5\n"
  "s_add_u32 s39, s39, s38\n"
  "s_and_b32 s38, s39, 1\n"
  "s_lshr_b32 s39, s39, 1\n"
  "s_lshl_b32 s39, s39, 12\n"
  "v_xor_b32 v7, s38, v5\n"
  "v_lshl_add_u32 v7, v7, 5, v3\n"
  "v_mfma_f32_16x16x32_bf16 v[92:95], v[216:219], v[188:191], v[92:95]\n"
  "v_add_u32 v22, s39, v7\n"
  "v_add_u32 v27, 77824, v22\n"
  "s_waitcnt lgkmcnt(4)\n"
  "v_mfma_f32_16x16x32_bf16 v[64:67], v[220:223], v[172:175], v[64:67]\n"
  "s_add_u32 s38, s37, 3\n"
  "s_cmp_ge_u32 s38, 5\n"
  "s_cselect_b32 s39, 5, 0\n"
  "s_sub_u32 s38, s38, s39\n"
  "s_mul_i32 s39, s36, 5\n"
  "s_add_u32 s39, s39, s38\n"
  "s_and_b32 s38, s39, 1\n"
  "s_lshr_b32 s39, s39, 1\n"
  "s_lshl_b32 s39, s39, 12\n"
  "v_xor_b32 v7, s38, v5\n"
  "v_lshl_add_u32 v7, v7, 5, v3\n"
  "v_mfma_f32_16x16x32_bf16 v[96:99], v[220:223], v[188:191], v[96:99]\n"
  "v_add_u32 v23, s39, v7\n"
  "v_add_u32 v28, 77824, v23\n"
  "s_setprio 3\n"
  "s_waitcnt lgkmcnt(2)\n"
  "v_mfma_f32_16x16x32_bf16 v[68:71], v[224:227], v[172:175], v[68:71]\n"
  "s_add_u32 s38, s37, 4\n"
  "s_cmp_ge_u32 s38, 5\n"
  "s_cselect_b32 s39, 5, 0\n"
  "s_sub_u32 s38, s38, s39\n"
  "s_mul_i32 s39, s36, 5\n"
  "s_add_u32 s39, s39, s38\n"
  "s_and_b32 s38, s39, 1\n"
  "s_lshr_b32 s39, s39, 1\n"
  "s_lshl_b32 s39, s39, 12\n"
  "v_xor_b32 v7, s38, v5\n"
  "v_lshl_add_u32 v7, v7, 5, v3\n"
  "v_mfma_f32_16x16x32_bf16 v[100:103], v[224:227], v[188:191], v[100:103]\n"
  "v_add_u32 v24, s39, v7\n"
  "v_add_u32 v29, 77824, v24\n"
  "v_mfma_f32_16x16x32_bf16 v[108:111], v[224:227], v[236:239], v[108:111]\n"
  "s_mov_b32 s38, 4\n"
  "s_mul_i32 s39, s36, 5\n"
  "s_add_u32 s39, s39, s38\n"
  "s_and_b32 s38, s39, 1\n"
  "s_lshr_b32 s39, s39, 1\n"
  "s_lshl_b32 s39, s39, 12\n"
  "v_xor_b32 v7, s38, v5\n"
  "v_lshl_add_u32 v7, v7, 5, v3\n"
  "s_waitcnt lgkmcnt(0)\n"
  "v_mfma_f32_16x16x32_bf16 v[72:75], v[228:231], v[172:175], v[72:75]\n"
  "v_add_u32 v30, s39, v7\n"
  "v_add_u32 v31, 77824, v30\n"
  "v_mfma_f32_16x16x32_bf16 v[104:107], v[228:231], v[188:191], v[104:107]\n"
  "s_lshl_b32 s38, s37, 12\n"
  "s_add_u32 s38, s38, 20480\n"
  "s_add_u32 s38, s38, %[ldsb]\n"
  "v_add_u32 v244, s38, v17\n"
  "v_add_u32 v245, 77824, v244\n"
  "v_mfma_f32_16x16x32_bf16 v[112:115], v[228:231], v[236:239], v[112:115]\n"
  "s_add_u32 s38, %[ldsb], 36864\n"
  "v_add_u32 v246, s38, v17\n"
  "v_add_u32 v247, 77824, v246\n"
  "s_mul_i32 s38, s36, 77824\n"
  "s_add_u32 s38, s38, 20480\n"
  "s_add_u32 s38, s38, %[ldsb]\n"
  "s_lshl_b32 s39, s37, 12\n"
  "s_add_u32 s39, s39, s38\n"
  "v_add_u32 v13, s39, v17\n"
  "s_mul_i32 s38, s36, 77824\n"
  "s_add_u32 s38, s38, 36864\n"
  "s_add_u32 s38, s38, %[ldsb]\n"
  "s_lshr_b32 s39, s37, 1\n"
  "s_mul_i32 s39, s39, 38912\n"
  "s_add_u32 s38, s38, s39\n"
  "s_and_b32 s39, s37, 1\n"
  "s_lshl_b32 s39, s39, 10\n"
  "s_add_u32 s38, s38, s39\n"
  "v_add_u32 v14, s38, v17\n"
  "v_and_b32 v7, 15, v1\n"
  "v_mul_u32_u24 v7, 2560, v7\n"
  "v_lshrrev_b32 v5, 4, v1\n"
  "v_lshl_add_u32 v15, v5, 3, v7\n"
  "v_mov_b64 v[248:249], 0\n"
  "v_max3_f32 v248, abs(v44), abs(v45), v248\n"
  "v_max3_f32 v249, abs(v46), abs(v47), v249\n"
  "v_max3_f32 v248, abs(v76), abs(v77), v248\n"
  "v_max3_f32 v249, abs(v78), abs(v79), v249\n"
  "v_max3_f32 v248, abs(v48), abs(v49), v248\n"
  "v_max3_f32 v249, abs(v50), abs(v51), v249\n"
  "v_max3_f32 v248, abs(v80), abs(v81), v248\n"
  "v_max3_f32 v249, abs(v82), abs(v83), v249\n"
  "v_max3_f32 v248, abs(v52), abs(v53), v248\n"
  "v_max3_f32 v249, abs(v54), abs(v55), v249\n"
  "v_max3_f32 v248, abs(v84), abs(v85), v248\n"
  "v_max3_f32 v249, abs(v86), abs(v87), v249\n"
  "v_max3_f32 v248, abs(v56), abs(v57), v248\n"
  "v_max3_f32 v249, abs(v58), abs(v59), v249\n"
  "v_max3_f32 v248, abs(v88), abs(v89), v248\n"
  "v_max3_f32 v249, abs(v90), abs(v91), v249\n"
  "v_max3_f32 v248, abs(v60), abs(v61), v248\n"
  "v_max3_f32 v249, abs(v62), abs(v63), v249\n"
  "v_max3_f32 v248, abs(v92), abs(v93), v248\n"
  "v_max3_f32 v249, abs(v94), abs(v95), v249\n"
  "v_max3_f32 v248, abs(v64), abs(v65), v248\n"
  "v_max3_f32 v249, abs(v66), abs(v67), v249\n"
  "v_max3_f32 v248, abs(v96), abs(v97), v248\n"
  "v_max3_f32 v249, abs(v98), abs(v99), v249\n"
  "v_max3_f32 v248, abs(v68), abs(v69), v248\n"
  "v_max3_f32 v249, abs(v70), abs(v71), v249\n"
  "v_max3_f32 v248, abs(v100), abs(v101), v248\n"
  "v_max3_f32 v249, abs(v102), abs(v103), v249\n"
  "v_max3_f32 v248, abs(v72), abs(v73), v248\n"
  "v_max3_f32 v249, abs(v74), abs(v75), v249\n"
  "v_max3_f32 v248, abs(v104), abs(v105), v248\n"
  "v_max3_f32 v249, abs(v106), abs(v107), v249\n"
  "v_max3_f32 v248, abs(v108), abs(v109), v248\n"
  "v_max3_f32 v249, abs(v110), abs(v111), v249\n"
  "v_max3_f32 v248, abs(v112), abs(v113), v248\n"
  "v_max3_f32 v249, abs(v114), abs(v115), v249\n"
  "P1_EPI:\n"
  "s_waitcnt lgkmcnt(0)\n"
  "s_nop 7\n"
  "s_nop 3\n"
  "v_add_f32 v18, v18, v19\n"
  "v_max_f32 v248, v248, v249\n"
  "v_mov_b32 v2, v18\n"
  "v_add_f32 v5, v36, v40\n"
  "v_mov_b32 v6, 0x7e800000\n"
  "v_permlane32_swap_b32 v18, v2\n"
  "v_cmp_lt_f32 s[58:59], v248, v6\n"
  "v_add_f32 v18, v18, v2\n"
  "v_lshlrev_b32 v6, 2, v1\n"
  "v_mov_b32 v2, v18\n"
  "s_lshl_b32 s38, %[wave], 8\n"
  "s_add_u32 s38, s38, 155648\n"
  "s_add_u32 s38, s38, %[ldsb]\n"
  "v_permlane16_swap_b32 v18, v2\n"
  "v_add_u32 v7, s38, v6\n"
  "v_add_f32 v18, v18, v2\n"
  "v_mov_b32 v8, 0x5e800000\n"
  "v_add_f32 v5, v5, v18\n"
  "ds_write_b32 v7, v18 offset:0\n"
  "v_cmp_lt_f32 vcc, v5, v8\n"
  "v_rcp_f32 v2, v36\n"
  "v_rcp_f32 v4, v40\n"
  "s_lshl_b32 s38, %[wave], 2\n"
  "s_add_u32 s38, s38, 157696\n"
  "s_add_u32 s38, s38, %[ldsb]\n"
  "v_mov_b32 v7, s38\n"
  "s_nop 1\n"
  "s_and_b64 vcc, vcc, s[58:59]\n"
  "s_cmp_eq_u64 vcc, -1\n"
  "s_cselect_b32 s57, 0, 1\n"
  "v_mov_b32 v8, s57\n"
  "ds_write_b32 v7, v8 offset:0\n"
  "s_waitcnt lgkmcnt(0)\n"
  "s_barrier\n"
  "s_lshl_b32 s38, s36, 10\n"
  "s_add_u32 s38, s38, 155648\n"
  "s_add_u32 s38, s38, %[ldsb]\n"
  "v_add_u32 v7, s38, v6\n"
  "ds_read_b32 v248, v7 offset:0\n"
  "ds_read_b32 v249, v7 offset:256\n"
  "ds_read_b32 v250, v7 offset:512\n"
  "ds_read_b32 v251, v7 offset:768\n"
  "s_add_u32 s38, %[ldsb], 157696\n"
  "v_mov_b32 v7, s38\n"
  "ds_read_b128 v[164:167], v7 offset:0\n"
  "ds_read_b128 v[168:171], v7 offset:16\n"
  "s_waitcnt lgkmcnt(4)\n"
  "v_add_f32 v248, v248, v249\n"
  "s_waitcnt lgkmcnt(2)\n"
  "v_add_f32 v250, v250, v251\n"
  "v_add_f32 v18, v248, v250\n"
  "s_waitcnt lgkmcnt(1)\n"
  "v_or3_b32 v8, v164, v165, v166\n"
  "s_waitcnt lgkmcnt(0)\n"
  "v_or3_b32 v8, v8, v167, v168\n"
  "v_or3_b32 v8, v8, v169, v170\n"
  "v_or_b32 v8, v8, v171\n"
  "v_rcp_f32 v6, v18\n"
  "v_readfirstlane_b32 s57, v8\n"
  "s_nop 4\n"
  "s_andn2_b32 s57, s57, s35\n"
  "s_cmp_lg_u32 s57, 0\n"
  "s_cbranch_scc1 P1_SLOW\n"
  "s_add_u32 m0, s43, 77824\n"
  "s_add_u32 s40, s42, s52\n"
  "buffer_load_dwordx4 v252, %[srdwo], s40 offen offset:0 lds\n"
  "s_add_u32 m0, s43, 78848\n"
  "s_add_u32 s40, s42, s53\n"
  "buffer_load_dwordx4 v252, %[srdwo], s40 offen offset:0 lds\n"
  "s_add_u32 m0, s44, 77824\n"
  "s_add_u32 s40, s42, s54\n"
  "s_mov_b64 exec, s[48:49]\n"
  "buffer_load_dwordx4 v252, %[srdwo], s40 offen offset:0 lds\n"
  "s_mov_b64 exec, -1\n"
  "s_add_u32 s42, s42, 81920\n"
  "s_cmp_ge_u32 s42, 1310720\n"
  "s_cselect_b32 s40, 1310720, 0\n"
  "s_sub_u32 s42, s42, s40\n"
  "s_add_u32 m0, s43, 116736\n"
  "s_add_u32 s40, s42, s52\n"
  "buffer_load_dwordx4 v252, %[srdwo], s40 offen offset:0 lds\n"
  "s_add_u32 m0, s43, 117760\n"
  "s_add_u32 s40, s42, s53\n"
  "buffer_load_dwordx4 v252, %[srdwo], s40 offen offset:0 lds\n"
  "s_add_u32 m0, s44, 116736\n"
  "s_add_u32 s40, s42, s54\n"
  "s_mov_b64 exec, s[48:49]\n"
  "buffer_load_dwordx4 v252, %[srdwo], s40 offen offset:0 lds\n"
  "s_mov_b64 exec, -1\n"
  "s_add_u32 s42, s42, 81920\n"
  "s_cmp_ge_u32 s42, 1310720\n"
  "s_cselect_b32 s40, 1310720, 0\n"
  "s_sub_u32 s42, s42, s40\n"
  "s_mul_i32 s41, %[jx], 73728\n"
  "s_mul_i32 s38, s36, 36864\n"
  "s_add_u32 s41, s41, s38\n"
  "s_lshr_b32 s38, s37, 1\n"
  "s_mul_i32 s38, s38, 18432\n"
  "s_add_u32 s57, s41, s38\n"
  "s_and_b32 s38, s37, 1\n"
  "s_lshl_b32 s38, s38, 10\n"
  "s_add_u32 s57, s57, s38\n"
  "s_add_u32 s57, s57, 16384\n"
  "s_lshl_b32 s38, s37, 12\n"
  "s_add_u32 s41, s41, s38\n"
  "s_add_u32 s56, s41, 18432\n"
  "v_pk_mul_f32 v[44:45], v[44:45], v[2:3] op_sel_hi:[1,0]\n"
  "v_pk_mul_f32 v[46:47], v[46:47], v[2:3] op_sel_hi:[1,0]\n"
  "v_pk_mul_f32 v[48:49], v[48:49], v[2:3] op_sel_hi:[1,0]\n"
  "v_pk_mul_f32 v[50:51], v[50:51], v[2:3] op_sel_hi:[1,0]\n"
  "v_cvt_pk_bf16_f32 v164, v44, v45\n"
  "v_cvt_pk_bf16_f32 v165, v46, v47\n"
  "v_cvt_pk_bf16_f32 v166, v48, v49\n"
  "v_cvt_pk_bf16_f32 v167, v50, v51\n"
  "ds_write_b128 v13, v[164:167] offset:0\n"
  "buffer_store_dwordx4 v[164:167], v17, %[srdo], s41 offen offset:0 sc1\n"
  "v_pk_mul_f32 v[52:53], v[52:53], v[2:3] op_sel_hi:[1,0]\n"
  "v_pk_mul_f32 v[54:55], v[54:55], v[2:3] op_sel_hi:[1,0]\n"
  "v_pk_mul_f32 v[56:57], v[56:57], v[2:3] op_sel_hi:[1,0]\n"
  "v_pk_mul_f32 v[58:59], v[58:59], v[2:3] op_sel_hi:[1,0]\n"
  "v_cvt_pk_bf16_f32 v168, v52, v53\n"
  "v_cvt_pk_bf16_f32 v169, v54, v55\n"
  "v_cvt_pk_bf16_f32 v170, v56, v57\n"
  "v_cvt_pk_bf16_f32 v171, v58, v59\n"
  "ds_write_b128 v13, v[168:171] offset:1024\n"
  "buffer_store_dwordx4 v[168:171], v17, %[srdo], s41 offen offset:1024 sc1\n"
  "v_pk_mul_f32 v[60:61], v[60:61], v[2:3] op_sel_hi:[1,0]\n"
  "v_pk_mul_f32 v[62:63], v[62:63], v[2:3] op_sel_hi:[1,0]\n"
  "v_pk_mul_f32 v[64:65], v[64:65], v[2:3] op_sel_hi:[1,0]\n"
  "v_pk_mul_f32 v[66:67], v[66:67], v[2:3] op_sel_hi:[1,0]\n"
  "v_cvt_pk_bf16_f32 v172, v60, v61\n"
  "v_cvt_pk_bf16_f32 v173, v62, v63\n"
  "v_cvt_pk_bf16_f32 v174, v64, v65\n"
  "v_cvt_pk_bf16_f32 v175, v66, v67\n"
  "ds_write_b128 v13, v[172:175] offset:38912\n"
  "buffer_store_dwordx4 v[172:175], v17, %[srdo], s56 offen offset:0 sc1\n"
  "v_pk_mul_f32 v[68:69], v[68:69], v[2:3] op_sel_hi:[1,0]\n"
  "v_pk_mul_f32 v[70:71], v[70:71], v[2:3] op_sel_hi:[1,0]\n"
  "v_pk_mul_f32 v[72:73], v[72:73], v[2:3] op_sel_hi:[1,0]\n"
  "v_pk_mul_f32 v[74:75], v[74:75], v[2:3] op_sel_hi:[1,0]\n"
  "v_cvt_pk_bf16_f32 v176, v68, v69\n"
  "v_cvt_pk_bf16_f32 v177, v70, v71\n"
  "v_cvt_pk_bf16_f32 v178, v72, v73\n"
  "v_cvt_pk_bf16_f32 v179, v74, v75\n"
  "ds_write_b128 v13, v[176:179] offset:39936\n"
  "buffer_store_dwordx4 v[176:179], v17, %[srdo], s56 offen offset:1024 sc1\n"
  "v_pk_mul_f32 v[76:77], v[76:77], v[4:5] op_sel_hi:[1,0]\n"
  "v_pk_mul_f32 v[78:79], v[78:79], v[4:5] op_sel_hi:[1,0]\n"
  "v_pk_mul_f32 v[80:81], v[80:81], v[4:5] op_sel_hi:[1,0]\n"
  "v_pk_mul_f32 v[82:83], v[82:83], v[4:5] op_sel_hi:[1,0]\n"
  "v_cvt_pk_bf16_f32 v180, v76, v77\n"
  "v_cvt_pk_bf16_f32 v181, v78, v79\n"
  "v_cvt_pk_bf16_f32 v182, v80, v81\n"
  "v_cvt_pk_bf16_f32 v183, v82, v83\n"
  "ds_write_b128 v13, v[180:183] offset:2048\n"
  "buffer_store_dwordx4 v[180:183], v17, %[srdo], s41 offen offset:2048 sc1\n"
  "v_pk_mul_f32 v[84:85], v[84:85], v[4:5] op_sel_hi:[1,0]\n"
  "v_pk_mul_f32 v[86:87], v[86:87], v[4:5] op_sel_hi:[1,0]\n"
  "v_pk_mul_f32 v[88:89], v[88:89], v[4:5] op_sel_hi:[1,0]\n"
  "v_pk_mul_f32 v[90:91], v[90:91], v[4:5] op_sel_hi:[1,0]\n"
  "v_cvt_pk_bf16_f32 v184, v84, v85\n"
  "v_cvt_pk_bf16_f32 v185, v86, v87\n"
  "v_cvt_pk_bf16_f32 v186, v88, v89\n"
  "v_cvt_pk_bf16_f32 v187, v90, v91\n"
  "ds_write_b128 v13, v[184:187] offset:3072\n"
  "buffer_store_dwordx4 v[184:187], v17, %[srdo], s41 offen offset:3072 sc1\n"
  "v_pk_mul_f32 v[92:93], v[92:93], v[4:5] op_sel_hi:[1,0]\n"
  "v_pk_mul_f32 v[94:95], v[94:95], v[4:5] op_sel_hi:[1,0]\n"
  "v_pk_mul_f32 v[96:97], v[96:97], v[4:5] op_sel_hi:[1,0]\n"
  "v_pk_mul_f32 v[98:99], v[98:99], v[4:5] op_sel_hi:[1,0]\n"
  "v_cvt_pk_bf16_f32 v188, v92, v93\n"
  "v_cvt_pk_bf16_f32 v189, v94, v95\n"
  "v_cvt_pk_bf16_f32 v190, v96, v97\n"
  "v_cvt_pk_bf16_f32 v191, v98, v99\n"
  "ds_write_b128 v13, v[188:191] offset:40960\n"
  "buffer_store_dwordx4 v[188:191], v17, %[srdo], s56 offen offset:2048 sc1\n"
  "v_pk_mul_f32 v[100:101], v[100:101], v[4:5] op_sel_hi:[1,0]\n"
  "v_pk_mul_f32 v[102:103], v[102:103], v[4:5] op_sel_hi:[1,0]\n"
  "v_pk_mul_f32 v[104:105], v[104:105], v[4:5] op_sel_hi:[1,0]\n"
  "v_pk_mul_f32 v[106:107], v[106:107], v[4:5] op_sel_hi:[1,0]\n"
  "v_cvt_pk_bf16_f32 v192, v100, v101\n"
  "v_cvt_pk_bf16_f32 v193, v102, v103\n"
  "v_cvt_pk_bf16_f32 v194, v104, v105\n"
  "v_cvt_pk_bf16_f32 v195, v106, v107\n"
  "ds_write_b128 v13, v[192:195] offset:41984\n"
  "buffer_store_dwordx4 v[192:195], v17, %[srdo], s56 offen offset:3072 sc1\n"
  "v_pk_mul_f32 v[108:109], v[108:109], v[6:7] op_sel_hi:[1,0]\n"
  "v_pk_mul_f32 v[110:111], v[110:111], v[6:7] op_sel_hi:[1,0]\n"
  "v_pk_mul_f32 v[112:113], v[112:113], v[6:7] op_sel_hi:[1,0]\n"
  "v_pk_mul_f32 v[114:115], v[114:115], v[6:7] op_sel_hi:[1,0]\n"
  "v_cvt_pk_bf16_f32 v196, v108, v109\n"
  "v_cvt_pk_bf16_f32 v197, v110, v111\n"
  "v_cvt_pk_bf16_f32 v198, v112, v113\n"
  "v_cvt_pk_bf16_f32 v199, v114, v115\n"
  "ds_write_b128 v14, v[196:199] offset:0\n"
  "buffer_store_dwordx4 v[196:199], v17, %[srdo], s57 offen offset:0 sc1\n"
  "s_lshl_b32 s45, %[jx], 2\n"
  "s_add_u32 s45, s45, 4\n"
  "s_and_b32 s45, s45, 15\n"
  "s_mul_i32 s45, s45, 18432\n"
  "s_lshl_b32 s56, %[wave], 11\n"
  "s_add_u32 s46, s56, 20480\n"
  "s_add_u32 s46, s46, %[ldsb]\n"
  "s_lshr_b32 s38, %[wave], 2\n"
  "s_add_u32 s38, s38, 16\n"
  "s_lshl_b32 s55, s38, 10\n"
  "s_add_u32 s47, s55, 20480\n"
  "s_add_u32 s47, s47, %[ldsb]\n"
  "s_lshl_b32 s38, s37, 4\n"
  "s_lshl_b64 s[50:51], 0xffff, s38\n"
  "s_cmp_eq_u32 s37, s36\n"
  "s_cselect_b64 vcc, -1, 0\n"
  "s_waitcnt vmcnt(18) lgkmcnt(0)\n"
  "s_barrier\n"
  "ds_read_b64_tr_b16 v[56:57], v20 offset:0\n"
  "ds_read_b64_tr_b16 v[58:59], v20 offset:1024\n"
  "ds_read_b128 v[44:47], v244 offset:0\n"
  "ds_read_b128 v[48:51], v244 offset:2048\n"
  "ds_read_b128 v[52:55], v246 offset:0\n"
  "ds_read_b64_tr_b16 v[60:61], v21 offset:0\n"
  "ds_read_b64_tr_b16 v[62:63], v21 offset:1024\n"
  "ds_read_b64_tr_b16 v[64:65], v22 offset:0\n"
  "ds_read_b64_tr_b16 v[66:67], v22 offset:1024\n"
  "ds_read_b64_tr_b16 v[68:69], v23 offset:0\n"
  "ds_read_b64_tr_b16 v[70:71], v23 offset:1024\n"
  "ds_read_b64_tr_b16 v[72:73], v24 offset:0\n"
  "ds_read_b64_tr_b16 v[74:75], v24 offset:1024\n"
  "s_cbranch_vccz P2_C1\n"
  "ds_read_b64_tr_b16 v[76:77], v30 offset:0\n"
  "ds_read_b64_tr_b16 v[78:79], v30 offset:1024\n"
  "P2_C1:\n"
  "ds_read_b64_tr_b16 v[92:93], v20 offset:2048\n"
  "ds_read_b64_tr_b16 v[94:95], v20 offset:3072\n"
  "ds_read_b128 v[80:83], v244 offset:1024\n"
  "ds_read_b128 v[84:87], v244 offset:3072\n"
  "ds_read_b128 v[88:91], v246 offset:1024\n"
  "ds_read_b64_tr_b16 v[96:97], v21 offset:2048\n"
  "ds_read_b64_tr_b16 v[98:99], v21 offset:3072\n"
  "ds_read_b64_tr_b16 v[100:101], v22 offset:2048\n"
  "ds_read_b64_tr_b16 v[102:103], v22 offset:3072\n"
  "ds_read_b64_tr_b16 v[104:105], v23 offset:2048\n"
  "ds_read_b64_tr_b16 v[106:107], v23 offset:3072\n"
  "ds_read_b64_tr_b16 v[108:109], v24 offset:2048\n"
  "ds_read_b64_tr_b16 v[110:111], v24 offset:3072\n"
  "s_cbranch_vccz P2_C2\n"
  "ds_read_b64_tr_b16 v[112:113], v30 offset:2048\n"
  "ds_read_b64_tr_b16 v[114:115], v30 offset:3072\n"
  "P2_C2:\n"
  "s_waitcnt vmcnt(0) lgkmcnt(0)\n"
  "s_barrier\n"
  "s_lshl_b32 s38, %[grp], 4\n"
  "s_lshl_b32 s39, %[hg], 2\n"
  "s_add_u32 s38, s38, s39\n"
  "s_lshl_b32 s57, s38, 7\n"
  "s_add_u32 s57, s57, 2048\n"
  "s_lshl_b32 s39, %[jx], 7\n"
  "s_add_u32 s39, s39, s57\n"
  "s_cmp_eq_u32 %[wave], 4\n"
  "s_cbranch_scc0 P2_F1P_S\n"
  "v_mov_b32 v241, s34\n"
  "v_mov_b32 v240, 0\n"
  "s_mov_b64 exec, 1\n"
  "buffer_store_dword v241, v240, %[srdf], s39 offen offset:0 sc1\n"
  "s_mov_b64 exec, -1\n"
  "P2_F1P_S:\n"
  "s_nop 7\n"
  "s_nop 3\n"
  "v_and_b32 v240, 3, v1\n"
  "v_lshlrev_b32 v240, 7, v240\n"
  "v_mov_b32 v241, s34\n"
  "v_mfma_f32_16x16x32_bf16 v[116:119], v[56:59], v[44:47], 0\n"
  "s_setprio 3\n"
  "ds_read_b64_tr_b16 v[176:177], v20 offset:38912\n"
  "ds_read_b64_tr_b16 v[178:179], v20 offset:39936\n"
  "v_mfma_f32_16x16x32_bf16 v[136:139], v[56:59], v[48:51], 0\n"
  "ds_read_b128 v[164:167], v244 offset:38912\n"
  "s_add_u32 m0, s43, 0\n"
  "s_add_u32 s40, s42, s52\n"
  "buffer_load_dwordx4 v252, %[srdwo], s40 offen offset:0 lds\n"
  "v_mfma_f32_16x16x32_bf16 v[156:159], v[56:59], v[52:55], 0\n"
  "ds_read_b128 v[168:171], v244 offset:40960\n"
  "ds_read_b128 v[172:175], v246 offset:38912\n"
  "v_mfma_f32_16x16x32_bf16 v[120:123], v[60:63], v[44:47], 0\n"
  "ds_read_b64_tr_b16 v[180:181], v21 offset:38912\n"
  "v_mfma_f32_16x16x32_bf16 v[140:143], v[60:63], v[48:51], 0\n"
  "ds_read_b64_tr_b16 v[182:183], v21 offset:39936\n"
  "v_mfma_f32_16x16x32_bf16 v[124:127], v[64:67], v[44:47], 0\n"
  "ds_read_b64_tr_b16 v[184:185], v22 offset:38912\n"
  "ds_read_b64_tr_b16 v[186:187], v22 offset:39936\n"
  "v_mfma_f32_16x16x32_bf16 v[144:147], v[64:67], v[48:51], 0\n"
  "s_setprio 2\n"
  "ds_read_b64_tr_b16 v[188:189], v23 offset:38912\n"
  "v_mfma_f32_16x16x32_bf16 v[128:131], v[68:71], v[44:47], 0\n"
  "ds_read_b64_tr_b16 v[190:191], v23 offset:39936\n"
  "ds_read_b64_tr_b16 v[192:193], v24 offset:38912\n"
  "v_mfma_f32_16x16x32_bf16 v[148:151], v[68:71], v[48:51], 0\n"
  "ds_read_b64_tr_b16 v[194:195], v24 offset:39936\n"
  "s_add_u32 m0, s43, 1024\n"
  "s_add_u32 s40, s42, s53\n"
  "buffer_load_dwordx4 v252, %[srdwo], s40 offen offset:0 lds\n"
  "v_mfma_f32_16x16x32_bf16 v[132:135], v[72:75], v[44:47], 0\n"
  "s_cbranch_vccz P2_C3\n"
  "ds_read_b64_tr_b16 v[196:197], v30 offset:38912\n"
  "ds_read_b64_tr_b16 v[198:199], v30 offset:39936\n"
  "P2_C3:\n"
  "v_mfma_f32_16x16x32_bf16 v[152:155], v[72:75], v[48:51], 0\n"
  "ds_read_b64_tr_b16 v[212:213], v20 offset:40960\n"
  "ds_read_b64_tr_b16 v[214:215], v20 offset:41984\n"
  "s_cbranch_vccz P2_C4\n"
  "v_mfma_f32_16x16x32_bf16 v[160:163], v[76:79], v[52:55], 0\n"
  "P2_C4:\n"
  "ds_read_b128 v[200:203], v244 offset:39936\n"
  "v_mfma_f32_16x16x32_bf16 v[116:119], v[92:95], v[80:83], v[116:119]\n"
  "s_setprio 1\n"
  "ds_read_b128 v[204:207], v244 offset:41984\n"
  "ds_read_b128 v[208:211], v246 offset:39936\n"
  "v_mfma_f32_16x16x32_bf16 v[136:139], v[92:95], v[84:87], v[136:139]\n"
  "ds_read_b64_tr_b16 v[216:217], v21 offset:40960\n"
  "v_mfma_f32_16x16x32_bf16 v[156:159], v[92:95], v[88:91], v[156:159]\n"
  "ds_read_b64_tr_b16 v[218:219], v21 offset:41984\n"
  "v_mfma_f32_16x16x32_bf16 v[120:123], v[96:99], v[80:83], v[120:123]\n"
  "ds_read_b64_tr_b16 v[220:221], v22 offset:40960\n"
  "ds_read_b64_tr_b16 v[222:223], v22 offset:41984\n"
  "s_add_u32 m0, s44, 0\n"
  "s_add_u32 s40, s42, s54\n"
  "s_mov_b64 exec, s[48:49]\n"
  "buffer_load_dwordx4 v252, %[srdwo], s40 offen offset:0 lds\n"
  "s_mov_b64 exec, -1\n"
  "s_add_u32 s42, s42, 81920\n"
  "s_cmp_ge_u32 s42, 1310720\n"
  "s_cselect_b32 s40, 1310720, 0\n"
  "s_sub_u32 s42, s42, s40\n"
  "v_mfma_f32_16x16x32_bf16 v[140:143], v[96:99], v[84:87], v[140:143]\n"
  "ds_read_b64_tr_b16 v[224:225], v23 offset:40960\n"
  "v_mfma_f32_16x16x32_bf16 v[124:127], v[100:103], v[80:83], v[124:127]\n"
  "ds_read_b64_tr_b16 v[226:227], v23 offset:41984\n"
  "ds_read_b64_tr_b16 v[228:229], v24 offset:40960\n"
  "v_mfma_f32_16x16x32_bf16 v[144:147], v[100:103], v[84:87], v[144:147]\n"
  "s_setprio 0\n"
  "ds_read_b64_tr_b16 v[230:231], v24 offset:41984\n"
  "v_mfma_f32_16x16x32_bf16 v[128:131], v[104:107], v[80:83], v[128:131]\n"
  "s_cbranch_vccz P2_C5\n"
  "ds_read_b64_tr_b16 v[232:233], v30 offset:40960\n"
  "ds_read_b64_tr_b16 v[234:235], v30 offset:41984\n"
  "P2_C5:\n"
  "v_mfma_f32_16x16x32_bf16 v[148:151], v[104:107], v[84:87], v[148:151]\n"
  "v_mfma_f32_16x16x32_bf16 v[132:135], v[108:111], v[80:83], v[132:135]\n"
  "v_mfma_f32_16x16x32_bf16 v[152:155], v[108:111], v[84:87], v[152:155]\n"
  "s_cbranch_vccz P2_C6\n"
  "v_mfma_f32_16x16x32_bf16 v[160:163], v[112:115], v[88:91], v[160:163]\n"
  "P2_C6:\n"
  "s_waitcnt vmcnt(15) lgkmcnt(0)\n"
  "s_barrier\n"
  "v_mfma_f32_16x16x32_bf16 v[116:119], v[176:179], v[164:167], v[116:119]\n"
  "s_setprio 3\n"
  "s_add_u32 m0, s43, 38912\n"
  "s_add_u32 s40, s42, s52\n"
  "buffer_load_dwordx4 v252, %[srdwo], s40 offen offset:0 lds\n"
  "ds_read_b64_tr_b16 v[56:57], v25 offset:0\n"
  "ds_read_b64_tr_b16 v[58:59], v25 offset:1024\n"
  "v_mfma_f32_16x16x32_bf16 v[136:139], v[176:179], v[168:171], v[136:139]\n"
  "s_add_u32 m0, s43, 39936\n"
  "s_add_u32 s40, s42, s53\n"
  "buffer_load_dwordx4 v252, %[srdwo], s40 offen offset:0 lds\n"
  "ds_read_b128 v[44:47], v245 offset:0\n"
  "v_mfma_f32_16x16x32_bf16 v[156:159], v[176:179], v[172:175], v[156:159]\n"
  "s_add_u32 m0, s44, 38912\n"
  "s_add_u32 s40, s42, s54\n"
  "s_mov_b64 exec, s[48:49]\n"
  "buffer_load_dwordx4 v252, %[srdwo], s40 offen offset:0 lds\n"
  "s_mov_b64 exec, -1\n"
  "s_add_u32 s42, s42, 81920\n"
  "s_cmp_ge_u32 s42, 1310720\n"
  "s_cselect_b32 s40, 1310720, 0\n"
  "s_sub_u32 s42, s42, s40\n"
  "ds_read_b128 v[48:51], v245 offset:2048\n"
  "ds_read_b128 v[52:55], v247 offset:0\n"
  "v_mfma_f32_16x16x32_bf16 v[120:123], v[180:183], v[164:167], v[120:123]\n"
  "buffer_load_dword v242, v240, %[srdf], s57 offen offset:0 sc1\n"
  "ds_read_b64_tr_b16 v[60:61], v26 offset:0\n"
  "v_mfma_f32_16x16x32_bf16 v[140:143], v[180:183], v[168:171], v[140:143]\n"
  "ds_read_b64_tr_b16 v[62:63], v26 offset:1024\n"
  "v_mfma_f32_16x16x32_bf16 v[124:127], v[184:187], v[164:167], v[124:127]\n"
  "ds_read_b64_tr_b16 v[64:65], v27 offset:0\n"
  "ds_read_b64_tr_b16 v[66:67], v27 offset:1024\n"
  "v_mfma_f32_16x16x32_bf16 v[144:147], v[184:187], v[168:171], v[144:147]\n"
  "s_setprio 2\n"
  "ds_read_b64_tr_b16 v[68:69], v28 offset:0\n"
  "v_mfma_f32_16x16x32_bf16 v[128:131], v[188:191], v[164:167], v[128:131]\n"
  "ds_read_b64_tr_b16 v[70:71], v28 offset:1024\n"
  "ds_read_b64_tr_b16 v[72:73], v29 offset:0\n"
  "v_mfma_f32_16x16x32_bf16 v[148:151], v[188:191], v[168:171], v[148:151]\n"
  "ds_read_b64_tr_b16 v[74:75], v29 offset:1024\n"
  "v_mfma_f32_16x16x32_bf16 v[132:135], v[192:195], v[164:167], v[132:135]\n"
  "s_cbranch_vccz P2_C7\n"
  "ds_read_b64_tr_b16 v[76:77], v31 offset:0\n"
  "ds_read_b64_tr_b16 v[78:79], v31 offset:1024\n"
  "P2_C7:\n"
  "v_mfma_f32_16x16x32_bf16 v[152:155], v[192:195], v[168:171], v[152:155]\n"
  "ds_read_b64_tr_b16 v[92:93], v25 offset:2048\n"
  "ds_read_b64_tr_b16 v[94:95], v25 offset:3072\n"
  "s_cbranch_vccz P2_C8\n"
  "v_mfma_f32_16x16x32_bf16 v[160:163], v[196:199], v[172:175], v[160:163]\n"
  "P2_C8:\n"
  "ds_read_b128 v[80:83], v245 offset:1024\n"
  "v_mfma_f32_16x16x32_bf16 v[116:119], v[212:215], v[200:203], v[116:119]\n"
  "s_setprio 1\n"
  "ds_read_b128 v[84:87], v245 offset:3072\n"
  "ds_read_b128 v[88:91], v247 offset:1024\n"
  "v_mfma_f32_16x16x32_bf16 v[136:139], v[212:215], v[204:207], v[136:139]\n"
  "ds_read_b64_tr_b16 v[96:97], v26 offset:2048\n"
  "v_mfma_f32_16x16x32_bf16 v[156:159], v[212:215], v[208:211], v[156:159]\n"
  "ds_read_b64_tr_b16 v[98:99], v26 offset:3072\n"
  "v_mfma_f32_16x16x32_bf16 v[120:123], v[216:219], v[200:203], v[120:123]\n"
  "ds_read_b64_tr_b16 v[100:101], v27 offset:2048\n"
  "ds_read_b64_tr_b16 v[102:103], v27 offset:3072\n"
  "v_mfma_f32_16x16x32_bf16 v[140:143], v[216:219], v[204:207], v[140:143]\n"
  "s_waitcnt vmcnt(0)\n"
  "s_mov_b32 s41, 0x20000\n"
  "s_branch P2_F1W_C\n"
  "P2_F1W_L:\n"
  "s_sleep 1\n"
  "buffer_load_dword v242, v240, %[srdf], s57 offen offset:0 sc1\n"
  "s_waitcnt vmcnt(0)\n"
  "P2_F1W_C:\n"
  "v_cmp_eq_u32 s[58:59], v242, v241\n"
  "s_nop 4\n"
  "s_cmp_eq_u64 s[58:59], -1\n"
  "s_cbranch_scc1 P2_F1W_S\n"
  "s_sub_u32 s41, s41, 1\n"
  "s_cmp_lg_u32 s41, 0\n"
  "s_cbranch_scc1 P2_F1W_L\n"
  "P2_F1W_S:\n"
  "s_nop 3\n"
  "ds_read_b64_tr_b16 v[104:105], v28 offset:2048\n"
  "v_mfma_f32_16x16x32_bf16 v[124:127], v[220:223], v[200:203], v[124:127]\n"
  "s_add_u32 m0, s46, 0\n"
  "s_add_u32 s39, s45, s56\n"
  "buffer_load_dwordx4 v17, %[srdo], s39 offen offset:0 sc1 lds\n"
  "ds_read_b64_tr_b16 v[106:107], v28 offset:3072\n"
  "ds_read_b64_tr_b16 v[108:109], v29 offset:2048\n"
  "v_mfma_f32_16x16x32_bf16 v[144:147], v[220:223], v[204:207], v[144:147]\n"
  "s_setprio 0\n"
  "ds_read_b64_tr_b16 v[110:111], v29 offset:3072\n"
  "v_mfma_f32_16x16x32_bf16 v[128:131], v[224:227], v[200:203], v[128:131]\n"
  "buffer_load_dwordx4 v17, %[srdo], s39 offen offset:1024 sc1 lds\n"
  "s_cbranch_vccz P2_C9\n"
  "ds_read_b64_tr_b16 v[112:113], v31 offset:2048\n"
  "ds_read_b64_tr_b16 v[114:115], v31 offset:3072\n"
  "P2_C9:\n"
  "v_mfma_f32_16x16x32_bf16 v[148:151], v[224:227], v[204:207], v[148:151]\n"
  "v_mfma_f32_16x16x32_bf16 v[132:135], v[228:231], v[200:203], v[132:135]\n"
  "s_add_u32 m0, s47, 0\n"
  "s_add_u32 s39, s45, s55\n"
  "s_mov_b64 exec, s[50:51]\n"
  "buffer_load_dwordx4 v17, %[srdo], s39 offen offset:0 sc1 lds\n"
  "s_mov_b64 exec, -1\n"
  "s_add_u32 s45, s45, 18432\n"
  "s_cmp_ge_u32 s45, 294912\n"
  "s_cselect_b32 s40, 294912, 0\n"
  "s_sub_u32 s45, s45, s40\n"
  "v_mfma_f32_16x16x32_bf16 v[152:155], v[228:231], v[204:207], v[152:155]\n"
  "s_cbranch_vccz P2_C10\n"
  "v_mfma_f32_16x16x32_bf16 v[160:163], v[232:235], v[208:211], v[160:163]\n"
  "P2_C10:\n"
  "s_waitcnt vmcnt(19) lgkmcnt(0)\n"
  "s_barrier\n"
  "v_mfma_f32_16x16x32_bf16 v[116:119], v[56:59], v[44:47], v[116:119]\n"
  "s_setprio 3\n"
  "ds_read_b64_tr_b16 v[176:177], v25 offset:38912\n"
  "ds_read_b64_tr_b16 v[178:179], v25 offset:39936\n"
  "v_mfma_f32_16x16x32_bf16 v[136:139], v[56:59], v[48:51], v[136:139]\n"
  "ds_read_b128 v[164:167], v245 offset:38912\n"
  "s_add_u32 m0, s46, 38912\n"
  "s_add_u32 s39, s45, s56\n"
  "buffer_load_dwordx4 v17, %[srdo], s39 offen offset:0 sc1 lds\n"
  "v_mfma_f32_16x16x32_bf16 v[156:159], v[56:59], v[52:55], v[156:159]\n"
  "ds_read_b128 v[168:171], v245 offset:40960\n"
  "ds_read_b128 v[172:175], v247 offset:38912\n"
  "v_mfma_f32_16x16x32_bf16 v[120:123], v[60:63], v[44:47], v[120:123]\n"
  "ds_read_b64_tr_b16 v[180:181], v26 offset:38912\n"
  "buffer_load_dwordx4 v17, %[srdo], s39 offen offset:1024 sc1 lds\n"
  "v_mfma_f32_16x16x32_bf16 v[140:143], v[60:63], v[48:51], v[140:143]\n"
  "ds_read_b64_tr_b16 v[182:183], v26 offset:39936\n"
  "v_mfma_f32_16x16x32_bf16 v[124:127], v[64:67], v[44:47], v[124:127]\n"
  "ds_read_b64_tr_b16 v[184:185], v27 offset:38912\n"
  "ds_read_b64_tr_b16 v[186:187], v27 offset:39936\n"
  "s_add_u32 m0, s47, 38912\n"
  "s_add_u32 s39, s45, s55\n"
  "s_mov_b64 exec, s[50:51]\n"
  "buffer_load_dwordx4 v17, %[srdo], s39 offen offset:0 sc1 lds\n"
  "s_mov_b64 exec, -1\n"
  "s_add_u32 s45, s45, 18432\n"
  "s_cmp_ge_u32 s45, 294912\n"
  "s_cselect_b32 s40, 294912, 0\n"
  "s_sub_u32 s45, s45, s40\n"
  "v_mfma_f32_16x16x32_bf16 v[144:147], v[64:67], v[48:51], v[144:147]\n"
  "s_setprio 2\n"
  "ds_read_b64_tr_b16 v[188:189], v28 offset:38912\n"
  "v_mfma_f32_16x16x32_bf16 v[128:131], v[68:71], v[44:47], v[128:131]\n"
  "ds_read_b64_tr_b16 v[190:191], v28 offset:39936\n"
  "ds_read_b64_tr_b16 v[192:193], v29 offset:38912\n"
  "v_mfma_f32_16x16x32_bf16 v[148:151], v[68:71], v[48:51], v[148:151]\n"
  "ds_read_b64_tr_b16 v[194:195], v29 offset:39936\n"
  "s_add_u32 m0, s46, 77824\n"
  "s_add_u32 s39, s45, s56\n"
  "buffer_load_dwordx4 v17, %[srdo], s39 offen offset:0 sc1 lds\n"
  "v_mfma_f32_16x16x32_bf16 v[132:135], v[72:75], v[44:47], v[132:135]\n"
  "s_cbranch_vccz P2_C11\n"
  "ds_read_b64_tr_b16 v[196:197], v31 offset:38912\n"
  "ds_read_b64_tr_b16 v[198:199], v31 offset:39936\n"
  "P2_C11:\n"
  "v_mfma_f32_16x16x32_bf16 v[152:155], v[72:75], v[48:51], v[152:155]\n"
  "ds_read_b64_tr_b16 v[212:213], v25 offset:40960\n"
  "ds_read_b64_tr_b16 v[214:215], v25 offset:41984\n"
  "buffer_load_dwordx4 v17, %[srdo], s39 offen offset:1024 sc1 lds\n"
  "s_cbranch_vccz P2_C12\n"
  "v_mfma_f32_16x16x32_bf16 v[160:163], v[76:79], v[52:55], v[160:163]\n"
  "P2_C12:\n"
  "ds_read_b128 v[200:203], v245 offset:39936\n"
  "v_mfma_f32_16x16x32_bf16 v[116:119], v[92:95], v[80:83], v[116:119]\n"
  "s_setprio 1\n"
  "ds_read_b128 v[204:207], v245 offset:41984\n"
  "ds_read_b128 v[208:211], v247 offset:39936\n"
  "s_add_u32 m0, s47, 77824\n"
  "s_add_u32 s39, s45, s55\n"
  "s_mov_b64 exec, s[50:51]\n"
  "buffer_load_dwordx4 v17, %[srdo], s39 offen offset:0 sc1 lds\n"
  "s_mov_b64 exec, -1\n"
  "s_add_u32 s45, s45, 18432\n"
  "s_cmp_ge_u32 s45, 294912\n"
  "s_cselect_b32 s40, 294912, 0\n"
  "s_sub_u32 s45, s45, s40\n"
  "v_mfma_f32_16x16x32_bf16 v[136:139], v[92:95], v[84:87], v[136:139]\n"
  "ds_read_b64_tr_b16 v[216:217], v26 offset:40960\n"
  "v_mfma_f32_16x16x32_bf16 v[156:159], v[92:95], v[88:91], v[156:159]\n"
  "ds_read_b64_tr_b16 v[218:219], v26 offset:41984\n"
  "v_mfma_f32_16x16x32_bf16 v[120:123], v[96:99], v[80:83], v[120:123]\n"
  "ds_read_b64_tr_b16 v[220:221], v27 offset:40960\n"
  "ds_read_b64_tr_b16 v[222:223], v27 offset:41984\n"
  "s_add_u32 m0, s43, 77824\n"
  "s_add_u32 s40, s42, s52\n"
  "buffer_load_dwordx4 v252, %[srdwo], s40 offen offset:0 lds\n"
  "v_mfma_f32_16x16x32_bf16 v[140:143], v[96:99], v[84:87], v[140:143]\n"
  "ds_read_b64_tr_b16 v[224:225], v28 offset:40960\n"
  "v_mfma_f32_16x16x32_bf16 v[124:127], v[100:103], v[80:83], v[124:127]\n"
  "ds_read_b64_tr_b16 v[226:227], v28 offset:41984\n"
  "ds_read_b64_tr_b16 v[228:229], v29 offset:40960\n"
  "s_add_u32 m0, s43, 78848\n"
  "s_add_u32 s40, s42, s53\n"
  "buffer_load_dwordx4 v252, %[srdwo], s40 offen offset:0 lds\n"
  "v_mfma_f32_16x16x32_bf16 v[144:147], v[100:103], v[84:87], v[144:147]\n"
  "s_setprio 0\n"
  "ds_read_b64_tr_b16 v[230:231], v29 offset:41984\n"
  "v_mfma_f32_16x16x32_bf16 v[128:131], v[104:107], v[80:83], v[128:131]\n"
  "s_cbranch_vccz P2_C13\n"
  "ds_read_b64_tr_b16 v[232:233], v31 offset:40960\n"
  "ds_read_b64_tr_b16 v[234:235], v31 offset:41984\n"
  "P2_C13:\n"
  "s_add_u32 m0, s44, 77824\n"
  "s_add_u32 s40, s42, s54\n"
  "s_mov_b64 exec, s[48:49]\n"
  "buffer_load_dwordx4 v252, %[srdwo], s40 offen offset:0 lds\n"
  "s_mov_b64 exec, -1\n"
  "s_add_u32 s42, s42, 81920\n"
  "s_cmp_ge_u32 s42, 1310720\n"
  "s_cselect_b32 s40, 1310720, 0\n"
  "s_sub_u32 s42, s42, s40\n"
  "v_mfma_f32_16x16x32_bf16 v[148:151], v[104:107], v[84:87], v[148:151]\n"
  "v_mfma_f32_16x16x32_bf16 v[132:135], v[108:111], v[80:83], v[132:135]\n"
  "v_mfma_f32_16x16x32_bf16 v[152:155], v[108:111], v[84:87], v[152:155]\n"
  "s_cbranch_vccz P2_C14\n"
  "v_mfma_f32_16x16x32_bf16 v[160:163], v[112:115], v[88:91], v[160:163]\n"
  "P2_C14:\n"
  "s_waitcnt vmcnt(9) lgkmcnt(0)\n"
  "s_barrier\n"
  "v_mfma_f32_16x16x32_bf16 v[116:119], v[176:179], v[164:167], v[116:119]\n"
  "s_setprio 3\n"
  "ds_read_b64_tr_b16 v[56:57], v20 offset:0\n"
  "ds_read_b64_tr_b16 v[58:59], v20 offset:1024\n"
  "v_mfma_f32_16x16x32_bf16 v[136:139], v[176:179], v[168:171], v[136:139]\n"
  "ds_read_b128 v[44:47], v244 offset:0\n"
  "s_add_u32 m0, s46, 116736\n"
  "s_add_u32 s39, s45, s56\n"
  "buffer_load_dwordx4 v17, %[srdo], s39 offen offset:0 sc1 lds\n"
  "v_mfma_f32_16x16x32_bf16 v[156:159], v[176:179], v[172:175], v[156:159]\n"
  "ds_read_b128 v[48:51], v244 offset:2048\n"
  "ds_read_b128 v[52:55], v246 offset:0\n"
  "v_mfma_f32_16x16x32_bf16 v[120:123], v[180:183], v[164:167], v[120:123]\n"
  "ds_read_b64_tr_b16 v[60:61], v21 offset:0\n"
  "v_mfma_f32_16x16x32_bf16 v[140:143], v[180:183], v[168:171], v[140:143]\n"
  "ds_read_b64_tr_b16 v[62:63], v21 offset:1024\n"
  "buffer_load_dwordx4 v17, %[srdo], s39 offen offset:1024 sc1 lds\n"
  "v_mfma_f32_16x16x32_bf16 v[124:127], v[184:187], v[164:167], v[124:127]\n"
  "ds_read_b64_tr_b16 v[64:65], v22 offset:0\n"
  "ds_read_b64_tr_b16 v[66:67], v22 offset:1024\n"
  "v_mfma_f32_16x16x32_bf16 v[144:147], v[184:187], v[168:171], v[144:147]\n"
  "s_setprio 2\n"
  "ds_read_b64_tr_b16 v[68:69], v23 offset:0\n"
  "v_mfma_f32_16x16x32_bf16 v[128:131], v[188:191], v[164:167], v[128:131]\n"
  "ds_read_b64_tr_b16 v[70:71], v23 offset:1024\n"
  "ds_read_b64_tr_b16 v[72:73], v24 offset:0\n"
  "v_mfma_f32_16x16x32_bf16 v[148:151], v[188:191], v[168:171], v[148:151]\n"
  "ds_read_b64_tr_b16 v[74:75], v24 offset:1024\n"
  "s_add_u32 m0, s47, 116736\n"
  "s_add_u32 s39, s45, s55\n"
  "s_mov_b64 exec, s[50:51]\n"
  "buffer_load_dwordx4 v17, %[srdo], s39 offen offset:0 sc1 lds\n"
  "s_mov_b64 exec, -1\n"
  "s_add_u32 s45, s45, 18432\n"
  "s_cmp_ge_u32 s45, 294912\n"
  "s_cselect_b32 s40, 294912, 0\n"
  "s_sub_u32 s45, s45, s40\n"
  "v_mfma_f32_16x16x32_bf16 v[132:135], v[192:195], v[164:167], v[132:135]\n"
  "s_cbranch_vccz P2_C15\n"
  "ds_read_b64_tr_b16 v[76:77], v30 offset:0\n"
  "ds_read_b64_tr_b16 v[78:79], v30 offset:1024\n"
  "P2_C15:\n"
  "v_mfma_f32_16x16x32_bf16 v[152:155], v[192:195], v[168:171], v[152:155]\n"
  "ds_read_b64_tr_b16 v[92:93], v20 offset:2048\n"
  "ds_read_b64_tr_b16 v[94:95], v20 offset:3072\n"
  "s_cbranch_vccz P2_C16\n"
  "v_mfma_f32_16x16x32_bf16 v[160:163], v[196:199], v[172:175], v[160:163]\n"
  "P2_C16:\n"
  "ds_read_b128 v[80:83], v244 offset:1024\n"
  "s_add_u32 m0, s43, 116736\n"
  "s_add_u32 s40, s42, s52\n"
  "buffer_load_dwordx4 v252, %[srdwo], s40 offen offset:0 lds\n"
  "v_mfma_f32_16x16x32_bf16 v[116:119], v[212:215], v[200:203], v[116:119]\n"
  "s_setprio 1\n"
  "ds_read_b128 v[84:87], v244 offset:3072\n"
  "ds_read_b128 v[88:91], v246 offset:1024\n"
  "v_mfma_f32_16x16x32_bf16 v[136:139], v[212:215], v[204:207], v[136:139]\n"
  "ds_read_b64_tr_b16 v[96:97], v21 offset:2048\n"
  "v_mfma_f32_16x16x32_bf16 v[156:159], v[212:215], v[208:211], v[156:159]\n"
  "ds_read_b64_tr_b16 v[98:99], v21 offset:3072\n"
  "v_mfma_f32_16x16x32_bf16 v[120:123], v[216:219], v[200:203], v[120:123]\n"
  "ds_read_b64_tr_b16 v[100:101], v22 offset:2048\n"
  "ds_read_b64_tr_b16 v[102:103], v22 offset:3072\n"
  "s_add_u32 m0, s43, 117760\n"
  "s_add_u32 s40, s42, s53\n"
  "buffer_load_dwordx4 v252, %[srdwo], s40 offen offset:0 lds\n"
  "v_mfma_f32_16x16x32_bf16 v[140:143], v[216:219], v[204:207], v[140:143]\n"
  "ds_read_b64_tr_b16 v[104:105], v23 offset:2048\n"
  "v_mfma_f32_16x16x32_bf16 v[124:127], v[220:223], v[200:203], v[124:127]\n"
  "ds_read_b64_tr_b16 v[106:107], v23 offset:3072\n"
  "ds_read_b64_tr_b16 v[108:109], v24 offset:2048\n"
  "v_mfma_f32_16x16x32_bf16 v[144:147], v[220:223], v[204:207], v[144:147]\n"
  "s_setprio 0\n"
  "ds_read_b64_tr_b16 v[110:111], v24 offset:3072\n"
  "s_add_u32 m0, s44, 116736\n"
  "s_add_u32 s40, s42, s54\n"
  "s_mov_b64 exec, s[48:49]\n"
  "buffer_load_dwordx4 v252, %[srdwo], s40 offen offset:0 lds\n"
  "s_mov_b64 exec, -1\n"
  "s_add_u32 s42, s42, 81920\n"
  "s_cmp_ge_u32 s42, 1310720\n"
  "s_cselect_b32 s40, 1310720, 0\n"
  "s_sub_u32 s42, s42, s40\n"
  "v_mfma_f32_16x16x32_bf16 v[128:131], v[224:227], v[200:203], v[128:131]\n"
  "s_cbranch_vccz P2_C17\n"
  "ds_read_b64_tr_b16 v[112:113], v30 offset:2048\n"
  "ds_read_b64_tr_b16 v[114:115], v30 offset:3072\n"
  "P2_C17:\n"
  "v_mfma_f32_16x16x32_bf16 v[148:151], v[224:227], v[204:207], v[148:151]\n"
  "v_mfma_f32_16x16x32_bf16 v[132:135], v[228:231], v[200:203], v[132:135]\n"
  "v_mfma_f32_16x16x32_bf16 v[152:155], v[228:231], v[204:207], v[152:155]\n"
  "s_cbranch_vccz P2_C18\n"
  "v_mfma_f32_16x16x32_bf16 v[160:163], v[232:235], v[208:211], v[160:163]\n"
  "P2_C18:\n"
  "s_waitcnt vmcnt(12) lgkmcnt(0)\n"
  "s_barrier\n"
  "v_mfma_f32_16x16x32_bf16 v[116:119], v[56:59], v[44:47], v[116:119]\n"
  "s_setprio 3\n"
  "ds_read_b64_tr_b16 v[176:177], v20 offset:38912\n"
  "ds_read_b64_tr_b16 v[178:179], v20 offset:39936\n"
  "v_mfma_f32_16x16x32_bf16 v[136:139], v[56:59], v[48:51], v[136:139]\n"
  "ds_read_b128 v[164:167], v244 offset:38912\n"
  "s_add_u32 m0, s46, 0\n"
  "s_add_u32 s39, s45, s56\n"
  "buffer_load_dwordx4 v17, %[srdo], s39 offen offset:0 sc1 lds\n"
  "v_mfma_f32_16x16x32_bf16 v[156:159], v[56:59], v[52:55], v[156:159]\n"
  "ds_read_b128 v[168:171], v244 offset:40960\n"
  "ds_read_b128 v[172:175], v246 offset:38912\n"
  "v_mfma_f32_16x16x32_bf16 v[120:123], v[60:63], v[44:47], v[120:123]\n"
  "ds_read_b64_tr_b16 v[180:181], v21 offset:38912\n"
  "v_mfma_f32_16x16x32_bf16 v[140:143], v[60:63], v[48:51], v[140:143]\n"
  "ds_read_b64_tr_b16 v[182:183], v21 offset:39936\n"
  "buffer_load_dwordx4 v17, %[srdo], s39 offen offset:1024 sc1 lds\n"
  "v_mfma_f32_16x16x32_bf16 v[124:127], v[64:67], v[44:47], v[124:127]\n"
  "ds_read_b64_tr_b16 v[184:185], v22 offset:38912\n"
  "ds_read_b64_tr_b16 v[186:187], v22 offset:39936\n"
  "v_mfma_f32_16x16x32_bf16 v[144:147], v[64:67], v[48:51], v[144:147]\n"
  "s_setprio 2\n"
  "ds_read_b64_tr_b16 v[188:189], v23 offset:38912\n"
  "v_mfma_f32_16x16x32_bf16 v[128:131], v[68:71], v[44:47], v[128:131]\n"
  "ds_read_b64_tr_b16 v[190:191], v23 offset:39936\n"
  "ds_read_b64_tr_b16 v[192:193], v24 offset:38912\n"
  "v_mfma_f32_16x16x32_bf16 v[148:151], v[68:71], v[48:51], v[148:151]\n"
  "ds_read_b64_tr_b16 v[194:195], v24 offset:39936\n"
  "s_add_u32 m0, s47, 0\n"
  "s_add_u32 s39, s45, s55\n"
  "s_mov_b64 exec, s[50:51]\n"
  "buffer_load_dwordx4 v17, %[srdo], s39 offen offset:0 sc1 lds\n"
  "s_mov_b64 exec, -1\n"
  "s_add_u32 s45, s45, 18432\n"
  "s_cmp_ge_u32 s45, 294912\n"
  "s_cselect_b32 s40, 294912, 0\n"
  "s_sub_u32 s45, s45, s40\n"
  "v_mfma_f32_16x16x32_bf16 v[132:135], v[72:75], v[44:47], v[132:135]\n"
  "s_cbranch_vccz P2_C19\n"
  "ds_read_b64_tr_b16 v[196:197], v30 offset:38912\n"
  "ds_read_b64_tr_b16 v[198:199], v30 offset:39936\n"
  "P2_C19:\n"
  "v_mfma_f32_16x16x32_bf16 v[152:155], v[72:75], v[48:51], v[152:155]\n"
  "ds_read_b64_tr_b16 v[212:213], v20 offset:40960\n"
  "ds_read_b64_tr_b16 v[214:215], v20 offset:41984\n"
  "s_cbranch_vccz P2_C20\n"
  "v_mfma_f32_16x16x32_bf16 v[160:163], v[76:79], v[52:55], v[160:163]\n"
  "P2_C20:\n"
  "ds_read_b128 v[200:203], v244 offset:39936\n"
  "s_add_u32 m0, s43, 0\n"
  "s_add_u32 s40, s42, s52\n"
  "buffer_load_dwordx4 v252, %[srdwo], s40 offen offset:0 lds\n"
  "v_mfma_f32_16x16x32_bf16 v[116:119], v[92:95], v[80:83], v[116:119]\n"
  "s_setprio 1\n"
  "ds_read_b128 v[204:207], v244 offset:41984\n"
  "ds_read_b128 v[208:211], v246 offset:39936\n"
  "v_mfma_f32_16x16x32_bf16 v[136:139], v[92:95], v[84:87], v[136:139]\n"
  "ds_read_b64_tr_b16 v[216:217], v21 offset:40960\n"
  "v_mfma_f32_16x16x32_bf16 v[156:159], v[92:95], v[88:91], v[156:159]\n"
  "ds_read_b64_tr_b16 v[218:219], v21 offset:41984\n"
  "v_mfma_f32_16x16x32_bf16 v[120:123], v[96:99], v[80:83], v[120:123]\n"
  "ds_read_b64_tr_b16 v[220:221], v22 offset:40960\n"
  "ds_read_b64_tr_b16 v[222:223], v22 offset:41984\n"
  "s_add_u32 m0, s43, 1024\n"
  "s_add_u32 s40, s42, s53\n"
  "buffer_load_dwordx4 v252, %[srdwo], s40 offen offset:0 lds\n"
  "v_mfma_f32_16x16x32_bf16 v[140:143], v[96:99], v[84:87], v[140:143]\n"
  "ds_read_b64_tr_b16 v[224:225], v23 offset:40960\n"
  "v_mfma_f32_16x16x32_bf16 v[124:127], v[100:103], v[80:83], v[124:127]\n"
  "ds_read_b64_tr_b16 v[226:227], v23 offset:41984\n"
  "ds_read_b64_tr_b16 v[228:229], v24 offset:40960\n"
  "v_mfma_f32_16x16x32_bf16 v[144:147], v[100:103], v[84:87], v[144:147]\n"
  "s_setprio 0\n"
  "ds_read_b64_tr_b16 v[230:231], v24 offset:41984\n"
  "s_add_u32 m0, s44, 0\n"
  "s_add_u32 s40, s42, s54\n"
  "s_mov_b64 exec, s[48:49]\n"
  "buffer_load_dwordx4 v252, %[srdwo], s40 offen offset:0 lds\n"
  "s_mov_b64 exec, -1\n"
  "s_add_u32 s42, s42, 81920\n"
  "s_cmp_ge_u32 s42, 1310720\n"
  "s_cselect_b32 s40, 1310720, 0\n"
  "s_sub_u32 s42, s42, s40\n"
  "v_mfma_f32_16x16x32_bf16 v[128:131], v[104:107], v[80:83], v[128:131]\n"
  "s_cbranch_vccz P2_C21\n"
  "ds_read_b64_tr_b16 v[232:233], v30 offset:40960\n"
  "ds_read_b64_tr_b16 v[234:235], v30 offset:41984\n"
  "P2_C21:\n"
  "v_mfma_f32_16x16x32_bf16 v[148:151], v[104:107], v[84:87], v[148:151]\n"
  "v_mfma_f32_16x16x32_bf16 v[132:135], v[108:111], v[80:83], v[132:135]\n"
  "v_mfma_f32_16x16x32_bf16 v[152:155], v[108:111], v[84:87], v[152:155]\n"
  "s_cbranch_vccz P2_C22\n"
  "v_mfma_f32_16x16x32_bf16 v[160:163], v[112:115], v[88:91], v[160:163]\n"
  "P2_C22:\n"
  "s_waitcnt vmcnt(12) lgkmcnt(0)\n"
  "s_barrier\n"
  "v_mfma_f32_16x16x32_bf16 v[116:119], v[176:179], v[164:167], v[116:119]\n"
  "s_setprio 3\n"
  "ds_read_b64_tr_b16 v[56:57], v25 offset:0\n"
  "ds_read_b64_tr_b16 v[58:59], v25 offset:1024\n"
  "v_mfma_f32_16x16x32_bf16 v[136:139], v[176:179], v[168:171], v[136:139]\n"
  "ds_read_b128 v[44:47], v245 offset:0\n"
  "s_add_u32 m0, s46, 38912\n"
  "s_add_u32 s39, s45, s56\n"
  "buffer_load_dwordx4 v17, %[srdo], s39 offen offset:0 sc1 lds\n"
  "v_mfma_f32_16x16x32_bf16 v[156:159], v[176:179], v[172:175], v[156:159]\n"
  "ds_read_b128 v[48:51], v245 offset:2048\n"
  "ds_read_b128 v[52:55], v247 offset:0\n"
  "v_mfma_f32_16x16x32_bf16 v[120:123], v[180:183], v[164:167], v[120:123]\n"
  "ds_read_b64_tr_b16 v[60:61], v26 offset:0\n"
  "v_mfma_f32_16x16x32_bf16 v[140:143], v[180:183], v[168:171], v[140:143]\n"
  "ds_read_b64_tr_b16 v[62:63], v26 offset:1024\n"
  "buffer_load_dwordx4 v17, %[srdo], s39 offen offset:1024 sc1 lds\n"
  "v_mfma_f32_16x16x32_bf16 v[124:127], v[184:187], v[164:167], v[124:127]\n"
  "ds_read_b64_tr_b16 v[64:65], v27 offset:0\n"
  "ds_read_b64_tr_b16 v[66:67], v27 offset:1024\n"
  "v_mfma_f32_16x16x32_bf16 v[144:147], v[184:187], v[168:171], v[144:147]\n"
  "s_setprio 2\n"
  "ds_read_b64_tr_b16 v[68:69], v28 offset:0\n"
  "v_mfma_f32_16x16x32_bf16 v[128:131], v[188:191], v[164:167], v[128:131]\n"
  "ds_read_b64_tr_b16 v[70:71], v28 offset:1024\n"
  "ds_read_b64_tr_b16 v[72:73], v29 offset:0\n"
  "v_mfma_f32_16x16x32_bf16 v[148:151], v[188:191], v[168:171], v[148:151]\n"
  "ds_read_b64_tr_b16 v[74:75], v29 offset:1024\n"
  "s_add_u32 m0, s47, 38912\n"
  "s_add_u32 s39, s45, s55\n"
  "s_mov_b64 exec, s[50:51]\n"
  "buffer_load_dwordx4 v17, %[srdo], s39 offen offset:0 sc1 lds\n"
  "s_mov_b64 exec, -1\n"
  "s_add_u32 s45, s45, 18432\n"
  "s_cmp_ge_u32 s45, 294912\n"
  "s_cselect_b32 s40, 294912, 0\n"
  "s_sub_u32 s45, s45, s40\n"
  "v_mfma_f32_16x16x32_bf16 v[132:135], v[192:195], v[164:167], v[132:135]\n"
  "s_cbranch_vccz P2_C23\n"
  "ds_read_b64_tr_b16 v[76:77], v31 offset:0\n"
  "ds_read_b64_tr_b16 v[78:79], v31 offset:1024\n"
  "P2_C23:\n"
  "v_mfma_f32_16x16x32_bf16 v[152:155], v[192:195], v[168:171], v[152:155]\n"
  "ds_read_b64_tr_b16 v[92:93], v25 offset:2048\n"
  "ds_read_b64_tr_b16 v[94:95], v25 offset:3072\n"
  "s_cbranch_vccz P2_C24\n"
  "v_mfma_f32_16x16x32_bf16 v[160:163], v[196:199], v[172:175], v[160:163]\n"
  "P2_C24:\n"
  "ds_read_b128 v[80:83], v245 offset:1024\n"
  "s_add_u32 m0, s43, 38912\n"
  "s_add_u32 s40, s42, s52\n"
  "buffer_load_dwordx4 v252, %[srdwo], s40 offen offset:0 lds\n"
  "v_mfma_f32_16x16x32_bf16 v[116:119], v[212:215], v[200:203], v[116:119]\n"
  "s_setprio 1\n"
  "ds_read_b128 v[84:87], v245 offset:3072\n"
  "ds_read_b128 v[88:91], v247 offset:1024\n"
  "v_mfma_f32_16x16x32_bf16 v[136:139], v[212:215], v[204:207], v[136:139]\n"
  "ds_read_b64_tr_b16 v[96:97], v26 offset:2048\n"
  "v_mfma_f32_16x16x32_bf16 v[156:159], v[212:215], v[208:211], v[156:159]\n"
  "ds_read_b64_tr_b16 v[98:99], v26 offset:3072\n"
  "v_mfma_f32_16x16x32_bf16 v[120:123], v[216:219], v[200:203], v[120:123]\n"
  "ds_read_b64_tr_b16 v[100:101], v27 offset:2048\n"
  "ds_read_b64_tr_b16 v[102:103], v27 offset:3072\n"
  "s_add_u32 m0, s43, 39936\n"
  "s_add_u32 s40, s42, s53\n"
  "buffer_load_dwordx4 v252, %[srdwo], s40 offen offset:0 lds\n"
  "v_mfma_f32_16x16x32_bf16 v[140:143], v[216:219], v[204:207], v[140:143]\n"
  "ds_read_b64_tr_b16 v[104:105], v28 offset:2048\n"
  "v_mfma_f32_16x16x32_bf16 v[124:127], v[220:223], v[200:203], v[124:127]\n"
  "ds_read_b64_tr_b16 v[106:107], v28 offset:3072\n"
  "ds_read_b64_tr_b16 v[108:109], v29 offset:2048\n"
  "v_mfma_f32_16x16x32_bf16 v[144:147], v[220:223], v[204:207], v[144:147]\n"
  "s_setprio 0\n"
  "ds_read_b64_tr_b16 v[110:111], v29 offset:3072\n"
  "s_add_u32 m0, s44, 38912\n"
  "s_add_u32 s40, s42, s54\n"
  "s_mov_b64 exec, s[48:49]\n"
  "buffer_load_dwordx4 v252, %[srdwo], s40 offen offset:0 lds\n"
  "s_mov_b64 exec, -1\n"
  "s_add_u32 s42, s42, 81920\n"
  "s_cmp_ge_u32 s42, 1310720\n"
  "s_cselect_b32 s40, 1310720, 0\n"
  "s_sub_u32 s42, s42, s40\n"
  "v_mfma_f32_16x16x32_bf16 v[128:131], v[224:227], v[200:203], v[128:131]\n"
  "s_cbranch_vccz P2_C25\n"
  "ds_read_b64_tr_b16 v[112:113], v31 offset:2048\n"
  "ds_read_b64_tr_b16 v[114:115], v31 offset:3072\n"
  "P2_C25:\n"
  "v_mfma_f32_16x16x32_bf16 v[148:151], v[224:227], v[204:207], v[148:151]\n"
  "v_mfma_f32_16x16x32_bf16 v[132:135], v[228:231], v[200:203], v[132:135]\n"
  "v_mfma_f32_16x16x32_bf16 v[152:155], v[228:231], v[204:207], v[152:155]\n"
  "s_cbranch_vccz P2_C26\n"
  "v_mfma_f32_16x16x32_bf16 v[160:163], v[232:235], v[208:211], v[160:163]\n"
  "P2_C26:\n"
  "s_waitcnt vmcnt(12) lgkmcnt(0)\n"
  "s_barrier\n"
  "v_mfma_f32_16x16x32_bf16 v[116:119], v[56:59], v[44:47], v[116:119]\n"
  "s_setprio 3\n"
  "ds_read_b64_tr_b16 v[176:177], v25 offset:38912\n"
  "ds_read_b64_tr_b16 v[178:179], v25 offset:39936\n"
  "v_mfma_f32_16x16x32_bf16 v[136:139], v[56:59], v[48:51], v[136:139]\n"
  "ds_read_b128 v[164:167], v245 offset:38912\n"
  "s_add_u32 m0, s46, 77824\n"
  "s_add_u32 s39, s45, s56\n"
  "buffer_load_dwordx4 v17, %[srdo], s39 offen offset:0 sc1 lds\n"
  "v_mfma_f32_16x16x32_bf16 v[156:159], v[56:59], v[52:55], v[156:159]\n"
  "ds_read_b128 v[168:171], v245 offset:40960\n"
  "ds_read_b128 v[172:175], v247 offset:38912\n"
  "v_mfma_f32_16x16x32_bf16 v[120:123], v[60:63], v[44:47], v[120:123]\n"
  "ds_read_b64_tr_b16 v[180:181], v26 offset:38912\n"
  "v_mfma_f32_16x16x32_bf16 v[140:143], v[60:63], v[48:51], v[140:143]\n"
  "ds_read_b64_tr_b16 v[182:183], v26 offset:39936\n"
  "buffer_load_dwordx4 v17, %[srdo], s39 offen offset:1024 sc1 lds\n"
  "v_mfma_f32_16x16x32_bf16 v[124:127], v[64:67], v[44:47], v[124:127]\n"
  "ds_read_b64_tr_b16 v[184:185], v27 offset:38912\n"
  "ds_read_b64_tr_b16 v[186:187], v27 offset:39936\n"
  "v_mfma_f32_16x16x32_bf16 v[144:147], v[64:67], v[48:51], v[144:147]\n"
  "s_setprio 2\n"
  "ds_read_b64_tr_b16 v[188:189], v28 offset:38912\n"
  "v_mfma_f32_16x16x32_bf16 v[128:131], v[68:71], v[44:47], v[128:131]\n"
  "ds_read_b64_tr_b16 v[190:191], v28 offset:39936\n"
  "ds_read_b64_tr_b16 v[192:193], v29 offset:38912\n"
  "v_mfma_f32_16x16x32_bf16 v[148:151], v[68:71], v[48:51], v[148:151]\n"
  "ds_read_b64_tr_b16 v[194:195], v29 offset:39936\n"
  "s_add_u32 m0, s47, 77824\n"
  "s_add_u32 s39, s45, s55\n"
  "s_mov_b64 exec, s[50:51]\n"
  "buffer_load_dwordx4 v17, %[srdo], s39 offen offset:0 sc1 lds\n"
  "s_mov_b64 exec, -1\n"
  "s_add_u32 s45, s45, 18432\n"
  "s_cmp_ge_u32 s45, 294912\n"
  "s_cselect_b32 s40, 294912, 0\n"
  "s_sub_u32 s45, s45, s40\n"
  "v_mfma_f32_16x16x32_bf16 v[132:135], v[72:75], v[44:47], v[132:135]\n"
  "s_cbranch_vccz P2_C27\n"
  "ds_read_b64_tr_b16 v[196:197], v31 offset:38912\n"
  "ds_read_b64_tr_b16 v[198:199], v31 offset:39936\n"
  "P2_C27:\n"
  "v_mfma_f32_16x16x32_bf16 v[152:155], v[72:75], v[48:51], v[152:155]\n"
  "ds_read_b64_tr_b16 v[212:213], v25 offset:40960\n"
  "ds_read_b64_tr_b16 v[214:215], v25 offset:41984\n"
  "s_cbranch_vccz P2_C28\n"
  "v_mfma_f32_16x16x32_bf16 v[160:163], v[76:79], v[52:55], v[160:163]\n"
  "P2_C28:\n"
  "ds_read_b128 v[200:203], v245 offset:39936\n"
  "s_add_u32 m0, s43, 77824\n"
  "s_add_u32 s40, s42, s52\n"
  "buffer_load_dwordx4 v252, %[srdwo], s40 offen offset:0 lds\n"
  "v_mfma_f32_16x16x32_bf16 v[116:119], v[92:95], v[80:83], v[116:119]\n"
  "s_setprio 1\n"
  "ds_read_b128 v[204:207], v245 offset:41984\n"
  "ds_read_b128 v[208:211], v247 offset:39936\n"
  "v_mfma_f32_16x16x32_bf16 v[136:139], v[92:95], v[84:87], v[136:139]\n"
  "ds_read_b64_tr_b16 v[216:217], v26 offset:40960\n"
  "v_mfma_f32_16x16x32_bf16 v[156:159], v[92:95], v[88:91], v[156:159]\n"
  "ds_read_b64_tr_b16 v[218:219], v26 offset:41984\n"
  "v_mfma_f32_16x16x32_bf16 v[120:123], v[96:99], v[80:83], v[120:123]\n"
  "ds_read_b64_tr_b16 v[220:221], v27 offset:40960\n"
  "ds_read_b64_tr_b16 v[222:223], v27 offset:41984\n"
  "s_add_u32 m0, s43, 78848\n"
  "s_add_u32 s40, s42, s53\n"
  "buffer_load_dwordx4 v252, %[srdwo], s40 offen offset:0 lds\n"
  "v_mfma_f32_16x16x32_bf16 v[140:143], v[96:99], v[84:87], v[140:143]\n"
  "ds_read_b64_tr_b16 v[224:225], v28 offset:40960\n"
  "v_mfma_f32_16x16x32_bf16 v[124:127], v[100:103], v[80:83], v[124:127]\n"
  "ds_read_b64_tr_b16 v[226:227], v28 offset:41984\n"
  "ds_read_b64_tr_b16 v[228:229], v29 offset:40960\n"
  "v_mfma_f32_16x16x32_bf16 v[144:147], v[100:103], v[84:87], v[144:147]\n"
  "s_setprio 0\n"
  "ds_read_b64_tr_b16 v[230:231], v29 offset:41984\n"
  "s_add_u32 m0, s44, 77824\n"
  "s_add_u32 s40, s42, s54\n"
  "s_mov_b64 exec, s[48:49]\n"
  "buffer_load_dwordx4 v252, %[srdwo], s40 offen offset:0 lds\n"
  "s_mov_b64 exec, -1\n"
  "s_add_u32 s42, s42, 81920\n"
  "s_cmp_ge_u32 s42, 1310720\n"
  "s_cselect_b32 s40, 1310720, 0\n"
  "s_sub_u32 s42, s42, s40\n"
  "v_mfma_f32_16x16x32_bf16 v[128:131], v[104:107], v[80:83], v[128:131]\n"
  "s_cbranch_vccz P2_C29\n"
  "ds_read_b64_tr_b16 v[232:233], v31 offset:40960\n"
  "ds_read_b64_tr_b16 v[234:235], v31 offset:41984\n"
  "P2_C29:\n"
  "v_mfma_f32_16x16x32_bf16 v[148:151], v[104:107], v[84:87], v[148:151]\n"
  "v_mfma_f32_16x16x32_bf16 v[132:135], v[108:111], v[80:83], v[132:135]\n"
  "v_mfma_f32_16x16x32_bf16 v[152:155], v[108:111], v[84:87], v[152:155]\n"
  "s_cbranch_vccz P2_C30\n"
  "v_mfma_f32_16x16x32_bf16 v[160:163], v[112:115], v[88:91], v[160:163]\n"
  "P2_C30:\n"
  "s_waitcnt vmcnt(12) lgkmcnt(0)\n"
  "s_barrier\n"
  "v_mfma_f32_16x16x32_bf16 v[116:119], v[176:179], v[164:167], v[116:119]\n"
  "s_setprio 3\n"
  "ds_read_b64_tr_b16 v[56:57], v20 offset:0\n"
  "ds_read_b64_tr_b16 v[58:59], v20 offset:1024\n"
  "v_mfma_f32_16x16x32_bf16 v[136:139], v[176:179], v[168:171], v[136:139]\n"
  "ds_read_b128 v[44:47], v244 offset:0\n"
  "s_add_u32 m0, s46, 116736\n"
  "s_add_u32 s39, s45, s56\n"
  "buffer_load_dwordx4 v17, %[srdo], s39 offen offset:0 sc1 lds\n"
  "v_mfma_f32_16x16x32_bf16 v[156:159], v[176:179], v[172:175], v[156:159]\n"
  "ds_read_b128 v[48:51], v244 offset:2048\n"
  "ds_read_b128 v[52:55], v246 offset:0\n"
  "v_mfma_f32_16x16x32_bf16 v[120:123], v[180:183], v[164:167], v[120:123]\n"
  "ds_read_b64_tr_b16 v[60:61], v21 offset:0\n"
  "v_mfma_f32_16x16x32_bf16 v[140:143], v[180:183], v[168:171], v[140:143]\n"
  "ds_read_b64_tr_b16 v[62:63], v21 offset:1024\n"
  "buffer_load_dwordx4 v17, %[srdo], s39 offen offset:1024 sc1 lds\n"
  "v_mfma_f32_16x16x32_bf16 v[124:127], v[184:187], v[164:167], v[124:127]\n"
  "ds_read_b64_tr_b16 v[64:65], v22 offset:0\n"
  "ds_read_b64_tr_b16 v[66:67], v22 offset:1024\n"
  "v_mfma_f32_16x16x32_bf16 v[144:147], v[184:187], v[168:171], v[144:147]\n"
  "s_setprio 2\n"
  "ds_read_b64_tr_b16 v[68:69], v23 offset:0\n"
  "v_mfma_f32_16x16x32_bf16 v[128:131], v[188:191], v[164:167], v[128:131]\n"
  "ds_read_b64_tr_b16 v[70:71], v23 offset:1024\n"
  "ds_read_b64_tr_b16 v[72:73], v24 offset:0\n"
  "v_mfma_f32_16x16x32_bf16 v[148:151], v[188:191], v[168:171], v[148:151]\n"
  "ds_read_b64_tr_b16 v[74:75], v24 offset:1024\n"
  "s_add_u32 m0, s47, 116736\n"
  "s_add_u32 s39, s45, s55\n"
  "s_mov_b64 exec, s[50:51]\n"
  "buffer_load_dwordx4 v17, %[srdo], s39 offen offset:0 sc1 lds\n"
  "s_mov_b64 exec, -1\n"
  "s_add_u32 s45, s45, 18432\n"
  "s_cmp_ge_u32 s45, 294912\n"
  "s_cselect_b32 s40, 294912, 0\n"
  "s_sub_u32 s45, s45, s40\n"
  "v_mfma_f32_16x16x32_bf16 v[132:135], v[192:195], v[164:167], v[132:135]\n"
  "s_cbranch_vccz P2_C31\n"
  "ds_read_b64_tr_b16 v[76:77], v30 offset:0\n"
  "ds_read_b64_tr_b16 v[78:79], v30 offset:1024\n"
  "P2_C31:\n"
  "v_mfma_f32_16x16x32_bf16 v[152:155], v[192:195], v[168:171], v[152:155]\n"
  "ds_read_b64_tr_b16 v[92:93], v20 offset:2048\n"
  "ds_read_b64_tr_b16 v[94:95], v20 offset:3072\n"
  "s_cbranch_vccz P2_C32\n"
  "v_mfma_f32_16x16x32_bf16 v[160:163], v[196:199], v[172:175], v[160:163]\n"
  "P2_C32:\n"
  "ds_read_b128 v[80:83], v244 offset:1024\n"
  "s_add_u32 m0, s43, 116736\n"
  "s_add_u32 s40, s42, s52\n"
  "buffer_load_dwordx4 v252, %[srdwo], s40 offen offset:0 lds\n"
  "v_mfma_f32_16x16x32_bf16 v[116:119], v[212:215], v[200:203], v[116:119]\n"
  "s_setprio 1\n"
  "ds_read_b128 v[84:87], v244 offset:3072\n"
  "ds_read_b128 v[88:91], v246 offset:1024\n"
  "v_mfma_f32_16x16x32_bf16 v[136:139], v[212:215], v[204:207], v[136:139]\n"
  "ds_read_b64_tr_b16 v[96:97], v21 offset:2048\n"
  "v_mfma_f32_16x16x32_bf16 v[156:159], v[212:215], v[208:211], v[156:159]\n"
  "ds_read_b64_tr_b16 v[98:99], v21 offset:3072\n"
  "v_mfma_f32_16x16x32_bf16 v[120:123], v[216:219], v[200:203], v[120:123]\n"
  "ds_read_b64_tr_b16 v[100:101], v22 offset:2048\n"
  "ds_read_b64_tr_b16 v[102:103], v22 offset:3072\n"
  "s_add_u32 m0, s43, 117760\n"
  "s_add_u32 s40, s42, s53\n"
  "buffer_load_dwordx4 v252, %[srdwo], s40 offen offset:0 lds\n"
  "v_mfma_f32_16x16x32_bf16 v[140:143], v[216:219], v[204:207], v[140:143]\n"
  "ds_read_b64_tr_b16 v[104:105], v23 offset:2048\n"
  "v_mfma_f32_16x16x32_bf16 v[124:127], v[220:223], v[200:203], v[124:127]\n"
  "ds_read_b64_tr_b16 v[106:107], v23 offset:3072\n"
  "ds_read_b64_tr_b16 v[108:109], v24 offset:2048\n"
  "v_mfma_f32_16x16x32_bf16 v[144:147], v[220:223], v[204:207], v[144:147]\n"
  "s_setprio 0\n"
  "ds_read_b64_tr_b16 v[110:111], v24 offset:3072\n"
  "s_add_u32 m0, s44, 116736\n"
  "s_add_u32 s40, s42, s54\n"
  "s_mov_b64 exec, s[48:49]\n"
  "buffer_load_dwordx4 v252, %[srdwo], s40 offen offset:0 lds\n"
  "s_mov_b64 exec, -1\n"
  "s_add_u32 s42, s42, 81920\n"
  "s_cmp_ge_u32 s42, 1310720\n"
  "s_cselect_b32 s40, 1310720, 0\n"
  "s_sub_u32 s42, s42, s40\n"
  "v_mfma_f32_16x16x32_bf16 v[128:131], v[224:227], v[200:203], v[128:131]\n"
  "s_cbranch_vccz P2_C33\n"
  "ds_read_b64_tr_b16 v[112:113], v30 offset:2048\n"
  "ds_read_b64_tr_b16 v[114:115], v30 offset:3072\n"
  "P2_C33:\n"
  "v_mfma_f32_16x16x32_bf16 v[148:151], v[224:227], v[204:207], v[148:151]\n"
  "v_mfma_f32_16x16x32_bf16 v[132:135], v[228:231], v[200:203], v[132:135]\n"
  "v_mfma_f32_16x16x32_bf16 v[152:155], v[228:231], v[204:207], v[152:155]\n"
  "s_cbranch_vccz P2_C34\n"
  "v_mfma_f32_16x16x32_bf16 v[160:163], v[232:235], v[208:211], v[160:163]\n"
  "P2_C34:\n"
  "s_waitcnt vmcnt(12) lgkmcnt(0)\n"
  "s_barrier\n"
  "v_mfma_f32_16x16x32_bf16 v[116:119], v[56:59], v[44:47], v[116:119]\n"
  "s_setprio 3\n"
  "ds_read_b64_tr_b16 v[176:177], v20 offset:38912\n"
  "ds_read_b64_tr_b16 v[178:179], v20 offset:39936\n"
  "v_mfma_f32_16x16x32_bf16 v[136:139], v[56:59], v[48:51], v[136:139]\n"
  "ds_read_b128 v[164:167], v244 offset:38912\n"
  "s_add_u32 m0, s46, 0\n"
  "s_add_u32 s39, s45, s56\n"
  "buffer_load_dwordx4 v17, %[srdo], s39 offen offset:0 sc1 lds\n"
  "v_mfma_f32_16x16x32_bf16 v[156:159], v[56:59], v[52:55], v[156:159]\n"
  "ds_read_b128 v[168:171], v244 offset:40960\n"
  "ds_read_b128 v[172:175], v246 offset:38912\n"
  "v_mfma_f32_16x16x32_bf16 v[120:123], v[60:63], v[44:47], v[120:123]\n"
  "ds_read_b64_tr_b16 v[180:181], v21 offset:38912\n"
  "v_mfma_f32_16x16x32_bf16 v[140:143], v[60:63], v[48:51], v[140:143]\n"
  "ds_read_b64_tr_b16 v[182:183], v21 offset:39936\n"
  "buffer_load_dwordx4 v17, %[srdo], s39 offen offset:1024 sc1 lds\n"
  "v_mfma_f32_16x16x32_bf16 v[124:127], v[64:67], v[44:47], v[124:127]\n"
  "ds_read_b64_tr_b16 v[184:185], v22 offset:38912\n"
  "ds_read_b64_tr_b16 v[186:187], v22 offset:39936\n"
  "v_mfma_f32_16x16x32_bf16 v[144:147], v[64:67], v[48:51], v[144:147]\n"
  "s_setprio 2\n"
  "ds_read_b64_tr_b16 v[188:189], v23 offset:38912\n"
  "v_mfma_f32_16x16x32_bf16 v[128:131], v[68:71], v[44:47], v[128:131]\n"
  "ds_read_b64_tr_b16 v[190:191], v23 offset:39936\n"
  "ds_read_b64_tr_b16 v[192:193], v24 offset:38912\n"
  "v_mfma_f32_16x16x32_bf16 v[148:151], v[68:71], v[48:51], v[148:151]\n"
  "ds_read_b64_tr_b16 v[194:195], v24 offset:39936\n"
  "s_add_u32 m0, s47, 0\n"
  "s_add_u32 s39, s45, s55\n"
  "s_mov_b64 exec, s[50:51]\n"
  "buffer_load_dwordx4 v17, %[srdo], s39 offen offset:0 sc1 lds\n"
  "s_mov_b64 exec, -1\n"
  "s_add_u32 s45, s45, 18432\n"
  "s_cmp_ge_u32 s45, 294912\n"
  "s_cselect_b32 s40, 294912, 0\n"
  "s_sub_u32 s45, s45, s40\n"
  "v_mfma_f32_16x16x32_bf16 v[132:135], v[72:75], v[44:47], v[132:135]\n"
  "s_cbranch_vccz P2_C35\n"
  "ds_read_b64_tr_b16 v[196:197], v30 offset:38912\n"
  "ds_read_b64_tr_b16 v[198:199], v30 offset:39936\n"
  "P2_C35:\n"
  "v_mfma_f32_16x16x32_bf16 v[152:155], v[72:75], v[48:51], v[152:155]\n"
  "ds_read_b64_tr_b16 v[212:213], v20 offset:40960\n"
  "ds_read_b64_tr_b16 v[214:215], v20 offset:41984\n"
  "s_cbranch_vccz P2_C36\n"
  "v_mfma_f32_16x16x32_bf16 v[160:163], v[76:79], v[52:55], v[160:163]\n"
  "P2_C36:\n"
  "ds_read_b128 v[200:203], v244 offset:39936\n"
  "s_add_u32 m0, s43, 0\n"
  "s_add_u32 s40, s42, s52\n"
  "buffer_load_dwordx4 v252, %[srdwo], s40 offen offset:0 lds\n"
  "v_mfma_f32_16x16x32_bf16 v[116:119], v[92:95], v[80:83], v[116:119]\n"
  "s_setprio 1\n"
  "ds_read_b128 v[204:207], v244 offset:41984\n"
  "ds_read_b128 v[208:211], v246 offset:39936\n"
  "v_mfma_f32_16x16x32_bf16 v[136:139], v[92:95], v[84:87], v[136:139]\n"
  "ds_read_b64_tr_b16 v[216:217], v21 offset:40960\n"
  "v_mfma_f32_16x16x32_bf16 v[156:159], v[92:95], v[88:91], v[156:159]\n"
  "ds_read_b64_tr_b16 v[218:219], v21 offset:41984\n"
  "v_mfma_f32_16x16x32_bf16 v[120:123], v[96:99], v[80:83], v[120:123]\n"
  "ds_read_b64_tr_b16 v[220:221], v22 offset:40960\n"
  "ds_read_b64_tr_b16 v[222:223], v22 offset:41984\n"
  "s_add_u32 m0, s43, 1024\n"
  "s_add_u32 s40, s42, s53\n"
  "buffer_load_dwordx4 v252, %[srdwo], s40 offen offset:0 lds\n"
  "v_mfma_f32_16x16x32_bf16 v[140:143], v[96:99], v[84:87], v[140:143]\n"
  "ds_read_b64_tr_b16 v[224:225], v23 offset:40960\n"
  "v_mfma_f32_16x16x32_bf16 v[124:127], v[100:103], v[80:83], v[124:127]\n"
  "ds_read_b64_tr_b16 v[226:227], v23 offset:41984\n"
  "ds_read_b64_tr_b16 v[228:229], v24 offset:40960\n"
  "v_mfma_f32_16x16x32_bf16 v[144:147], v[100:103], v[84:87], v[144:147]\n"
  "s_setprio 0\n"
  "ds_read_b64_tr_b16 v[230:231], v24 offset:41984\n"
  "s_add_u32 m0, s44, 0\n"
  "s_add_u32 s40, s42, s54\n"
  "s_mov_b64 exec, s[48:49]\n"
  "buffer_load_dwordx4 v252, %[srdwo], s40 offen offset:0 lds\n"
  "s_mov_b64 exec, -1\n"
  "s_add_u32 s42, s42, 81920\n"
  "s_cmp_ge_u32 s42, 1310720\n"
  "s_cselect_b32 s40, 1310720, 0\n"
  "s_sub_u32 s42, s42, s40\n"
  "v_mfma_f32_16x16x32_bf16 v[128:131], v[104:107], v[80:83], v[128:131]\n"
  "s_cbranch_vccz P2_C37\n"
  "ds_read_b64_tr_b16 v[232:233], v30 offset:40960\n"
  "ds_read_b64_tr_b16 v[234:235], v30 offset:41984\n"
  "P2_C37:\n"
  "v_mfma_f32_16x16x32_bf16 v[148:151], v[104:107], v[84:87], v[148:151]\n"
  "v_mfma_f32_16x16x32_bf16 v[132:135], v[108:111], v[80:83], v[132:135]\n"
  "v_mfma_f32_16x16x32_bf16 v[152:155], v[108:111], v[84:87], v[152:155]\n"
  "s_cbranch_vccz P2_C38\n"
  "v_mfma_f32_16x16x32_bf16 v[160:163], v[112:115], v[88:91], v[160:163]\n"
  "P2_C38:\n"
  "s_waitcnt vmcnt(12) lgkmcnt(0)\n"
  "s_barrier\n"
  "v_mfma_f32_16x16x32_bf16 v[116:119], v[176:179], v[164:167], v[116:119]\n"
  "s_setprio 3\n"
  "ds_read_b64_tr_b16 v[56:57], v25 offset:0\n"
  "ds_read_b64_tr_b16 v[58:59], v25 offset:1024\n"
  "v_mfma_f32_16x16x32_bf16 v[136:139], v[176:179], v[168:171], v[136:139]\n"
  "ds_read_b128 v[44:47], v245 offset:0\n"
  "s_add_u32 m0, s46, 38912\n"
  "s_add_u32 s39, s45, s56\n"
  "buffer_load_dwordx4 v17, %[srdo], s39 offen offset:0 sc1 lds\n"
  "v_mfma_f32_16x16x32_bf16 v[156:159], v[176:179], v[172:175], v[156:159]\n"
  "ds_read_b128 v[48:51], v245 offset:2048\n"
  "ds_read_b128 v[52:55], v247 offset:0\n"
  "v_mfma_f32_16x16x32_bf16 v[120:123], v[180:183], v[164:167], v[120:123]\n"
  "ds_read_b64_tr_b16 v[60:61], v26 offset:0\n"
  "v_mfma_f32_16x16x32_bf16 v[140:143], v[180:183], v[168:171], v[140:143]\n"
  "ds_read_b64_tr_b16 v[62:63], v26 offset:1024\n"
  "buffer_load_dwordx4 v17, %[srdo], s39 offen offset:1024 sc1 lds\n"
  "v_mfma_f32_16x16x32_bf16 v[124:127], v[184:187], v[164:167], v[124:127]\n"
  "ds_read_b64_tr_b16 v[64:65], v27 offset:0\n"
  "ds_read_b64_tr_b16 v[66:67], v27 offset:1024\n"
  "v_mfma_f32_16x16x32_bf16 v[144:147], v[184:187], v[168:171], v[144:147]\n"
  "s_setprio 2\n"
  "ds_read_b64_tr_b16 v[68:69], v28 offset:0\n"
  "v_mfma_f32_16x16x32_bf16 v[128:131], v[188:191], v[164:167], v[128:131]\n"
  "ds_read_b64_tr_b16 v[70:71], v28 offset:1024\n"
  "ds_read_b64_tr_b16 v[72:73], v29 offset:0\n"
  "v_mfma_f32_16x16x32_bf16 v[148:151], v[188:191], v[168:171], v[148:151]\n"
  "ds_read_b64_tr_b16 v[74:75], v29 offset:1024\n"
  "s_add_u32 m0, s47, 38912\n"
  "s_add_u32 s39, s45, s55\n"
  "s_mov_b64 exec, s[50:51]\n"
  "buffer_load_dwordx4 v17, %[srdo], s39 offen offset:0 sc1 lds\n"
  "s_mov_b64 exec, -1\n"
  "s_add_u32 s45, s45, 18432\n"
  "s_cmp_ge_u32 s45, 294912\n"
  "s_cselect_b32 s40, 294912, 0\n"
  "s_sub_u32 s45, s45, s40\n"
  "v_mfma_f32_16x16x32_bf16 v[132:135], v[192:195], v[164:167], v[132:135]\n"
  "s_cbranch_vccz P2_C39\n"
  "ds_read_b64_tr_b16 v[76:77], v31 offset:0\n"
  "ds_read_b64_tr_b16 v[78:79], v31 offset:1024\n"
  "P2_C39:\n"
  "v_mfma_f32_16x16x32_bf16 v[152:155], v[192:195], v[168:171], v[152:155]\n"
  "ds_read_b64_tr_b16 v[92:93], v25 offset:2048\n"
  "ds_read_b64_tr_b16 v[94:95], v25 offset:3072\n"
  "s_cbranch_vccz P2_C40\n"
  "v_mfma_f32_16x16x32_bf16 v[160:163], v[196:199], v[172:175], v[160:163]\n"
  "P2_C40:\n"
  "ds_read_b128 v[80:83], v245 offset:1024\n"
  "s_add_u32 m0, s43, 38912\n"
  "s_add_u32 s40, s42, s52\n"
  "buffer_load_dwordx4 v252, %[srdwo], s40 offen offset:0 lds\n"
  "v_mfma_f32_16x16x32_bf16 v[116:119], v[212:215], v[200:203], v[116:119]\n"
  "s_setprio 1\n"
  "ds_read_b128 v[84:87], v245 offset:3072\n"
  "ds_read_b128 v[88:91], v247 offset:1024\n"
  "v_mfma_f32_16x16x32_bf16 v[136:139], v[212:215], v[204:207], v[136:139]\n"
  "ds_read_b64_tr_b16 v[96:97], v26 offset:2048\n"
  "v_mfma_f32_16x16x32_bf16 v[156:159], v[212:215], v[208:211], v[156:159]\n"
  "ds_read_b64_tr_b16 v[98:99], v26 offset:3072\n"
  "v_mfma_f32_16x16x32_bf16 v[120:123], v[216:219], v[200:203], v[120:123]\n"
  "ds_read_b64_tr_b16 v[100:101], v27 offset:2048\n"
  "ds_read_b64_tr_b16 v[102:103], v27 offset:3072\n"
  "s_add_u32 m0, s43, 39936\n"
  "s_add_u32 s40, s42, s53\n"
  "buffer_load_dwordx4 v252, %[srdwo], s40 offen offset:0 lds\n"
  "v_mfma_f32_16x16x32_bf16 v[140:143], v[216:219], v[204:207], v[140:143]\n"
  "ds_read_b64_tr_b16 v[104:105], v28 offset:2048\n"
  "v_mfma_f32_16x16x32_bf16 v[124:127], v[220:223], v[200:203], v[124:127]\n"
  "ds_read_b64_tr_b16 v[106:107], v28 offset:3072\n"
  "ds_read_b64_tr_b16 v[108:109], v29 offset:2048\n"
  "v_mfma_f32_16x16x32_bf16 v[144:147], v[220:223], v[204:207], v[144:147]\n"
  "s_setprio 0\n"
  "ds_read_b64_tr_b16 v[110:111], v29 offset:3072\n"
  "s_add_u32 m0, s44, 38912\n"
  "s_add_u32 s40, s42, s54\n"
  "s_mov_b64 exec, s[48:49]\n"
  "buffer_load_dwordx4 v252, %[srdwo], s40 offen offset:0 lds\n"
  "s_mov_b64 exec, -1\n"
  "s_add_u32 s42, s42, 81920\n"
  "s_cmp_ge_u32 s42, 1310720\n"
  "s_cselect_b32 s40, 1310720, 0\n"
  "s_sub_u32 s42, s42, s40\n"
  "v_mfma_f32_16x16x32_bf16 v[128:131], v[224:227], v[200:203], v[128:131]\n"
  "s_cbranch_vccz P2_C41\n"
  "ds_read_b64_tr_b16 v[112:113], v31 offset:2048\n"
  "ds_read_b64_tr_b16 v[114:115], v31 offset:3072\n"
  "P2_C41:\n"
  "v_mfma_f32_16x16x32_bf16 v[148:151], v[224:227], v[204:207], v[148:151]\n"
  "v_mfma_f32_16x16x32_bf16 v[132:135], v[228:231], v[200:203], v[132:135]\n"
  "v_mfma_f32_16x16x32_bf16 v[152:155], v[228:231], v[204:207], v[152:155]\n"
  "s_cbranch_vccz P2_C42\n"
  "v_mfma_f32_16x16x32_bf16 v[160:163], v[232:235], v[208:211], v[160:163]\n"
  "P2_C42:\n"
  "s_waitcnt vmcnt(12) lgkmcnt(0)\n"
  "s_barrier\n"
  "v_mfma_f32_16x16x32_bf16 v[116:119], v[56:59], v[44:47], v[116:119]\n"
  "s_setprio 3\n"
  "ds_read_b64_tr_b16 v[176:177], v25 offset:38912\n"
  "ds_read_b64_tr_b16 v[178:179], v25 offset:39936\n"
  "v_mfma_f32_16x16x32_bf16 v[136:139], v[56:59], v[48:51], v[136:139]\n"
  "ds_read_b128 v[164:167], v245 offset:38912\n"
  "s_add_u32 m0, s46, 77824\n"
  "s_add_u32 s39, s45, s56\n"
  "buffer_load_dwordx4 v17, %[srdo], s39 offen offset:0 sc1 lds\n"
  "v_mfma_f32_16x16x32_bf16 v[156:159], v[56:59], v[52:55], v[156:159]\n"
  "ds_read_b128 v[168:171], v245 offset:40960\n"
  "ds_read_b128 v[172:175], v247 offset:38912\n"
  "v_mfma_f32_16x16x32_bf16 v[120:123], v[60:63], v[44:47], v[120:123]\n"
  "ds_read_b64_tr_b16 v[180:181], v26 offset:38912\n"
  "v_mfma_f32_16x16x32_bf16 v[140:143], v[60:63], v[48:51], v[140:143]\n"
  "ds_read_b64_tr_b16 v[182:183], v26 offset:39936\n"
  "buffer_load_dwordx4 v17, %[srdo], s39 offen offset:1024 sc1 lds\n"
  "v_mfma_f32_16x16x32_bf16 v[124:127], v[64:67], v[44:47], v[124:127]\n"
  "ds_read_b64_tr_b16 v[184:185], v27 offset:38912\n"
  "ds_read_b64_tr_b16 v[186:187], v27 offset:39936\n"
  "v_mfma_f32_16x16x32_bf16 v[144:147], v[64:67], v[48:51], v[144:147]\n"
  "s_setprio 2\n"
  "ds_read_b64_tr_b16 v[188:189], v28 offset:38912\n"
  "v_mfma_f32_16x16x32_bf16 v[128:131], v[68:71], v[44:47], v[128:131]\n"
  "ds_read_b64_tr_b16 v[190:191], v28 offset:39936\n"
  "ds_read_b64_tr_b16 v[192:193], v29 offset:38912\n"
  "v_mfma_f32_16x16x32_bf16 v[148:151], v[68:71], v[48:51], v[148:151]\n"
  "ds_read_b64_tr_b16 v[194:195], v29 offset:39936\n"
  "s_add_u32 m0, s47, 77824\n"
  "s_add_u32 s39, s45, s55\n"
  "s_mov_b64 exec, s[50:51]\n"
  "buffer_load_dwordx4 v17, %[srdo], s39 offen offset:0 sc1 lds\n"
  "s_mov_b64 exec, -1\n"
  "s_add_u32 s45, s45, 18432\n"
  "s_cmp_ge_u32 s45, 294912\n"
  "s_cselect_b32 s40, 294912, 0\n"
  "s_sub_u32 s45, s45, s40\n"
  "v_mfma_f32_16x16x32_bf16 v[132:135], v[72:75], v[44:47], v[132:135]\n"
  "s_cbranch_vccz P2_C43\n"
  "ds_read_b64_tr_b16 v[196:197], v31 offset:38912\n"
  "ds_read_b64_tr_b16 v[198:199], v31 offset:39936\n"
  "P2_C43:\n"
  "v_mfma_f32_16x16x32_bf16 v[152:155], v[72:75], v[48:51], v[152:155]\n"
  "ds_read_b64_tr_b16 v[212:213], v25 offset:40960\n"
  "ds_read_b64_tr_b16 v[214:215], v25 offset:41984\n"
  "s_cbranch_vccz P2_C44\n"
  "v_mfma_f32_16x16x32_bf16 v[160:163], v[76:79], v[52:55], v[160:163]\n"
  "P2_C44:\n"
  "ds_read_b128 v[200:203], v245 offset:39936\n"
  "s_add_u32 m0, s43, 77824\n"
  "s_add_u32 s40, s42, s52\n"
  "buffer_load_dwordx4 v252, %[srdwo], s40 offen offset:0 lds\n"
  "v_mfma_f32_16x16x32_bf16 v[116:119], v[92:95], v[80:83], v[116:119]\n"
  "s_setprio 1\n"
  "ds_read_b128 v[204:207], v245 offset:41984\n"
  "ds_read_b128 v[208:211], v247 offset:39936\n"
  "v_mfma_f32_16x16x32_bf16 v[136:139], v[92:95], v[84:87], v[136:139]\n"
  "ds_read_b64_tr_b16 v[216:217], v26 offset:40960\n"
  "v_mfma_f32_16x16x32_bf16 v[156:159], v[92:95], v[88:91], v[156:159]\n"
  "ds_read_b64_tr_b16 v[218:219], v26 offset:41984\n"
  "v_mfma_f32_16x16x32_bf16 v[120:123], v[96:99], v[80:83], v[120:123]\n"
  "ds_read_b64_tr_b16 v[220:221], v27 offset:40960\n"
  "ds_read_b64_tr_b16 v[222:223], v27 offset:41984\n"
  "s_add_u32 m0, s43, 78848\n"
  "s_add_u32 s40, s42, s53\n"
  "buffer_load_dwordx4 v252, %[srdwo], s40 offen offset:0 lds\n"
  "v_mfma_f32_16x16x32_bf16 v[140:143], v[96:99], v[84:87], v[140:143]\n"
  "ds_read_b64_tr_b16 v[224:225], v28 offset:40960\n"
  "v_mfma_f32_16x16x32_bf16 v[124:127], v[100:103], v[80:83], v[124:127]\n"
  "ds_read_b64_tr_b16 v[226:227], v28 offset:41984\n"
  "ds_read_b64_tr_b16 v[228:229], v29 offset:40960\n"
  "v_mfma_f32_16x16x32_bf16 v[144:147], v[100:103], v[84:87], v[144:147]\n"
  "s_setprio 0\n"
  "ds_read_b64_tr_b16 v[230:231], v29 offset:41984\n"
  "s_add_u32 m0, s44, 77824\n"
  "s_add_u32 s40, s42, s54\n"
  "s_mov_b64 exec, s[48:49]\n"
  "buffer_load_dwordx4 v252, %[srdwo], s40 offen offset:0 lds\n"
  "s_mov_b64 exec, -1\n"
  "s_add_u32 s42, s42, 81920\n"
  "s_cmp_ge_u32 s42, 1310720\n"
  "s_cselect_b32 s40, 1310720, 0\n"
  "s_sub_u32 s42, s42, s40\n"
  "v_mfma_f32_16x16x32_bf16 v[128:131], v[104:107], v[80:83], v[128:131]\n"
  "s_cbranch_vccz P2_C45\n"
  "ds_read_b64_tr_b16 v[232:233], v31 offset:40960\n"
  "ds_read_b64_tr_b16 v[234:235], v31 offset:41984\n"
  "P2_C45:\n"
  "v_mfma_f32_16x16x32_bf16 v[148:151], v[104:107], v[84:87], v[148:151]\n"
  "v_mfma_f32_16x16x32_bf16 v[132:135], v[108:111], v[80:83], v[132:135]\n"
  "v_mfma_f32_16x16x32_bf16 v[152:155], v[108:111], v[84:87], v[152:155]\n"
  "s_cbranch_vccz P2_C46\n"
  "v_mfma_f32_16x16x32_bf16 v[160:163], v[112:115], v[88:91], v[160:163]\n"
  "P2_C46:\n"
  "s_waitcnt vmcnt(12) lgkmcnt(0)\n"
  "s_barrier\n"
  "v_mfma_f32_16x16x32_bf16 v[116:119], v[176:179], v[164:167], v[116:119]\n"
  "s_setprio 3\n"
  "ds_read_b64_tr_b16 v[56:57], v20 offset:0\n"
  "ds_read_b64_tr_b16 v[58:59], v20 offset:1024\n"
  "v_mfma_f32_16x16x32_bf16 v[136:139], v[176:179], v[168:171], v[136:139]\n"
  "ds_read_b128 v[44:47], v244 offset:0\n"
  "s_add_u32 m0, s46, 116736\n"
  "s_add_u32 s39, s45, s56\n"
  "buffer_load_dwordx4 v17, %[srdo], s39 offen offset:0 sc1 lds\n"
  "v_mfma_f32_16x16x32_bf16 v[156:159], v[176:179], v[172:175], v[156:159]\n"
  "ds_read_b128 v[48:51], v244 offset:2048\n"
  "ds_read_b128 v[52:55], v246 offset:0\n"
  "v_mfma_f32_16x16x32_bf16 v[120:123], v[180:183], v[164:167], v[120:123]\n"
  "ds_read_b64_tr_b16 v[60:61], v21 offset:0\n"
  "v_mfma_f32_16x16x32_bf16 v[140:143], v[180:183], v[168:171], v[140:143]\n"
  "ds_read_b64_tr_b16 v[62:63], v21 offset:1024\n"
  "buffer_load_dwordx4 v17, %[srdo], s39 offen offset:1024 sc1 lds\n"
  "v_mfma_f32_16x16x32_bf16 v[124:127], v[184:187], v[164:167], v[124:127]\n"
  "ds_read_b64_tr_b16 v[64:65], v22 offset:0\n"
  "ds_read_b64_tr_b16 v[66:67], v22 offset:1024\n"
  "v_mfma_f32_16x16x32_bf16 v[144:147], v[184:187], v[168:171], v[144:147]\n"
  "s_setprio 2\n"
  "ds_read_b64_tr_b16 v[68:69], v23 offset:0\n"
  "v_mfma_f32_16x16x32_bf16 v[128:131], v[188:191], v[164:167], v[128:131]\n"
  "ds_read_b64_tr_b16 v[70:71], v23 offset:1024\n"
  "ds_read_b64_tr_b16 v[72:73], v24 offset:0\n"
  "v_mfma_f32_16x16x32_bf16 v[148:151], v[188:191], v[168:171], v[148:151]\n"
  "ds_read_b64_tr_b16 v[74:75], v24 offset:1024\n"
  "s_add_u32 m0, s47, 116736\n"
  "s_add_u32 s39, s45, s55\n"
  "s_mov_b64 exec, s[50:51]\n"
  "buffer_load_dwordx4 v17, %[srdo], s39 offen offset:0 sc1 lds\n"
  "s_mov_b64 exec, -1\n"
  "s_add_u32 s45, s45, 18432\n"
  "s_cmp_ge_u32 s45, 294912\n"
  "s_cselect_b32 s40, 294912, 0\n"
  "s_sub_u32 s45, s45, s40\n"
  "v_mfma_f32_16x16x32_bf16 v[132:135], v[192:195], v[164:167], v[132:135]\n"
  "s_cbranch_vccz P2_C47\n"
  "ds_read_b64_tr_b16 v[76:77], v30 offset:0\n"
  "ds_read_b64_tr_b16 v[78:79], v30 offset:1024\n"
  "P2_C47:\n"
  "v_mfma_f32_16x16x32_bf16 v[152:155], v[192:195], v[168:171], v[152:155]\n"
  "ds_read_b64_tr_b16 v[92:93], v20 offset:2048\n"
  "ds_read_b64_tr_b16 v[94:95], v20 offset:3072\n"
  "s_cbranch_vccz P2_C48\n"
  "v_mfma_f32_16x16x32_bf16 v[160:163], v[196:199], v[172:175], v[160:163]\n"
  "P2_C48:\n"
  "ds_read_b128 v[80:83], v244 offset:1024\n"
  "s_add_u32 m0, s43, 116736\n"
  "s_add_u32 s40, s42, s52\n"
  "buffer_load_dwordx4 v252, %[srdwo], s40 offen offset:0 lds\n"
  "v_mfma_f32_16x16x32_bf16 v[116:119], v[212:215], v[200:203], v[116:119]\n"
  "s_setprio 1\n"
  "ds_read_b128 v[84:87], v244 offset:3072\n"
  "ds_read_b128 v[88:91], v246 offset:1024\n"
  "v_mfma_f32_16x16x32_bf16 v[136:139], v[212:215], v[204:207], v[136:139]\n"
  "ds_read_b64_tr_b16 v[96:97], v21 offset:2048\n"
  "v_mfma_f32_16x16x32_bf16 v[156:159], v[212:215], v[208:211], v[156:159]\n"
  "ds_read_b64_tr_b16 v[98:99], v21 offset:3072\n"
  "v_mfma_f32_16x16x32_bf16 v[120:123], v[216:219], v[200:203], v[120:123]\n"
  "ds_read_b64_tr_b16 v[100:101], v22 offset:2048\n"
  "ds_read_b64_tr_b16 v[102:103], v22 offset:3072\n"
  "s_add_u32 m0, s43, 117760\n"
  "s_add_u32 s40, s42, s53\n"
  "buffer_load_dwordx4 v252, %[srdwo], s40 offen offset:0 lds\n"
  "v_mfma_f32_16x16x32_bf16 v[140:143], v[216:219], v[204:207], v[140:143]\n"
  "ds_read_b64_tr_b16 v[104:105], v23 offset:2048\n"
  "v_mfma_f32_16x16x32_bf16 v[124:127], v[220:223], v[200:203], v[124:127]\n"
  "ds_read_b64_tr_b16 v[106:107], v23 offset:3072\n"
  "ds_read_b64_tr_b16 v[108:109], v24 offset:2048\n"
  "v_mfma_f32_16x16x32_bf16 v[144:147], v[220:223], v[204:207], v[144:147]\n"
  "s_setprio 0\n"
  "ds_read_b64_tr_b16 v[110:111], v24 offset:3072\n"
  "s_add_u32 m0, s44, 116736\n"
  "s_add_u32 s40, s42, s54\n"
  "s_mov_b64 exec, s[48:49]\n"
  "buffer_load_dwordx4 v252, %[srdwo], s40 offen offset:0 lds\n"
  "s_mov_b64 exec, -1\n"
  "s_add_u32 s42, s42, 81920\n"
  "s_cmp_ge_u32 s42, 1310720\n"
  "s_cselect_b32 s40, 1310720, 0\n"
  "s_sub_u32 s42, s42, s40\n"
  "v_mfma_f32_16x16x32_bf16 v[128:131], v[224:227], v[200:203], v[128:131]\n"
  "s_cbranch_vccz P2_C49\n"
  "ds_read_b64_tr_b16 v[112:113], v30 offset:2048\n"
  "ds_read_b64_tr_b16 v[114:115], v30 offset:3072\n"
  "P2_C49:\n"
  "v_mfma_f32_16x16x32_bf16 v[148:151], v[224:227], v[204:207], v[148:151]\n"
  "v_mfma_f32_16x16x32_bf16 v[132:135], v[228:231], v[200:203], v[132:135]\n"
  "v_mfma_f32_16x16x32_bf16 v[152:155], v[228:231], v[204:207], v[152:155]\n"
  "s_cbranch_vccz P2_C50\n"
  "v_mfma_f32_16x16x32_bf16 v[160:163], v[232:235], v[208:211], v[160:163]\n"
  "P2_C50:\n"
  "s_waitcnt vmcnt(12) lgkmcnt(0)\n"
  "s_barrier\n"
  "v_mfma_f32_16x16x32_bf16 v[116:119], v[56:59], v[44:47], v[116:119]\n"
  "s_setprio 3\n"
  "ds_read_b64_tr_b16 v[176:177], v20 offset:38912\n"
  "ds_read_b64_tr_b16 v[178:179], v20 offset:39936\n"
  "v_mfma_f32_16x16x32_bf16 v[136:139], v[56:59], v[48:51], v[136:139]\n"
  "ds_read_b128 v[164:167], v244 offset:38912\n"
  "v_mfma_f32_16x16x32_bf16 v[156:159], v[56:59], v[52:55], v[156:159]\n"
  "ds_read_b128 v[168:171], v244 offset:40960\n"
  "ds_read_b128 v[172:175], v246 offset:38912\n"
  "v_mfma_f32_16x16x32_bf16 v[120:123], v[60:63], v[44:47], v[120:123]\n"
  "ds_read_b64_tr_b16 v[180:181], v21 offset:38912\n"
  "v_mfma_f32_16x16x32_bf16 v[140:143], v[60:63], v[48:51], v[140:143]\n"
  "ds_read_b64_tr_b16 v[182:183], v21 offset:39936\n"
  "v_mfma_f32_16x16x32_bf16 v[124:127], v[64:67], v[44:47], v[124:127]\n"
  "ds_read_b64_tr_b16 v[184:185], v22 offset:38912\n"
  "ds_read_b64_tr_b16 v[186:187], v22 offset:39936\n"
  "v_mfma_f32_16x16x32_bf16 v[144:147], v[64:67], v[48:51], v[144:147]\n"
  "s_setprio 2\n"
  "ds_read_b64_tr_b16 v[188:189], v23 offset:38912\n"
  "v_mfma_f32_16x16x32_bf16 v[128:131], v[68:71], v[44:47], v[128:131]\n"
  "ds_read_b64_tr_b16 v[190:191], v23 offset:39936\n"
  "ds_read_b64_tr_b16 v[192:193], v24 offset:38912\n"
  "v_mfma_f32_16x16x32_bf16 v[148:151], v[68:71], v[48:51], v[148:151]\n"
  "ds_read_b64_tr_b16 v[194:195], v24 offset:39936\n"
  "v_mfma_f32_16x16x32_bf16 v[132:135], v[72:75], v[44:47], v[132:135]\n"
  "s_cbranch_vccz P2_C51\n"
  "ds_read_b64_tr_b16 v[196:197], v30 offset:38912\n"
  "ds_read_b64_tr_b16 v[198:199], v30 offset:39936\n"
  "P2_C51:\n"
  "v_mfma_f32_16x16x32_bf16 v[152:155], v[72:75], v[48:51], v[152:155]\n"
  "ds_read_b64_tr_b16 v[212:213], v20 offset:40960\n"
  "ds_read_b64_tr_b16 v[214:215], v20 offset:41984\n"
  "s_cbranch_vccz P2_C52\n"
  "v_mfma_f32_16x16x32_bf16 v[160:163], v[76:79], v[52:55], v[160:163]\n"
  "P2_C52:\n"
  "ds_read_b128 v[200:203], v244 offset:39936\n"
  "v_mfma_f32_16x16x32_bf16 v[116:119], v[92:95], v[80:83], v[116:119]\n"
  "s_setprio 1\n"
  "ds_read_b128 v[204:207], v244 offset:41984\n"
  "ds_read_b128 v[208:211], v246 offset:39936\n"
  "v_mfma_f32_16x16x32_bf16 v[136:139], v[92:95], v[84:87], v[136:139]\n"
  "ds_read_b64_tr_b16 v[216:217], v21 offset:40960\n"
  "v_mfma_f32_16x16x32_bf16 v[156:159], v[92:95], v[88:91], v[156:159]\n"
  "ds_read_b64_tr_b16 v[218:219], v21 offset:41984\n"
  "v_mfma_f32_16x16x32_bf16 v[120:123], v[96:99], v[80:83], v[120:123]\n"
  "ds_read_b64_tr_b16 v[220:221], v22 offset:40960\n"
  "ds_read_b64_tr_b16 v[222:223], v22 offset:41984\n"
  "v_mfma_f32_16x16x32_bf16 v[140:143], v[96:99], v[84:87], v[140:143]\n"
  "ds_read_b64_tr_b16 v[224:225], v23 offset:40960\n"
  "v_mfma_f32_16x16x32_bf16 v[124:127], v[100:103], v[80:83], v[124:127]\n"
  "ds_read_b64_tr_b16 v[226:227], v23 offset:41984\n"
  "ds_read_b64_tr_b16 v[228:229], v24 offset:40960\n"
  "v_mfma_f32_16x16x32_bf16 v[144:147], v[100:103], v[84:87], v[144:147]\n"
  "s_setprio 0\n"
  "ds_read_b64_tr_b16 v[230:231], v24 offset:41984\n"
  "v_mfma_f32_16x16x32_bf16 v[128:131], v[104:107], v[80:83], v[128:131]\n"
  "s_cbranch_vccz P2_C53\n"
  "ds_read_b64_tr_b16 v[232:233], v30 offset:40960\n"
  "ds_read_b64_tr_b16 v[234:235], v30 offset:41984\n"
  "P2_C53:\n"
  "v_mfma_f32_16x16x32_bf16 v[148:151], v[104:107], v[84:87], v[148:151]\n"
  "v_mfma_f32_16x16x32_bf16 v[132:135], v[108:111], v[80:83], v[132:135]\n"
  "v_mfma_f32_16x16x32_bf16 v[152:155], v[108:111], v[84:87], v[152:155]\n"
  "s_cbranch_vccz P2_C54\n"
  "v_mfma_f32_16x16x32_bf16 v[160:163], v[112:115], v[88:91], v[160:163]\n"
  "P2_C54:\n"
  "s_waitcnt vmcnt(6) lgkmcnt(0)\n"
  "s_barrier\n"
  "v_mfma_f32_16x16x32_bf16 v[116:119], v[176:179], v[164:167], v[116:119]\n"
  "s_setprio 3\n"
  "ds_read_b64_tr_b16 v[56:57], v25 offset:0\n"
  "ds_read_b64_tr_b16 v[58:59], v25 offset:1024\n"
  "v_mfma_f32_16x16x32_bf16 v[136:139], v[176:179], v[168:171], v[136:139]\n"
  "ds_read_b128 v[44:47], v245 offset:0\n"
  "v_mfma_f32_16x16x32_bf16 v[156:159], v[176:179], v[172:175], v[156:159]\n"
  "ds_read_b128 v[48:51], v245 offset:2048\n"
  "ds_read_b128 v[52:55], v247 offset:0\n"
  "v_mfma_f32_16x16x32_bf16 v[120:123], v[180:183], v[164:167], v[120:123]\n"
  "ds_read_b64_tr_b16 v[60:61], v26 offset:0\n"
  "v_mfma_f32_16x16x32_bf16 v[140:143], v[180:183], v[168:171], v[140:143]\n"
  "ds_read_b64_tr_b16 v[62:63], v26 offset:1024\n"
  "v_mfma_f32_16x16x32_bf16 v[124:127], v[184:187], v[164:167], v[124:127]\n"
  "ds_read_b64_tr_b16 v[64:65], v27 offset:0\n"
  "ds_read_b64_tr_b16 v[66:67], v27 offset:1024\n"
  "v_mfma_f32_16x16x32_bf16 v[144:147], v[184:187], v[168:171], v[144:147]\n"
  "s_setprio 2\n"
  "ds_read_b64_tr_b16 v[68:69], v28 offset:0\n"
  "v_mfma_f32_16x16x32_bf16 v[128:131], v[188:191], v[164:167], v[128:131]\n"
  "ds_read_b64_tr_b16 v[70:71], v28 offset:1024\n"
  "ds_read_b64_tr_b16 v[72:73], v29 offset:0\n"
  "v_mfma_f32_16x16x32_bf16 v[148:151], v[188:191], v[168:171], v[148:151]\n"
  "ds_read_b64_tr_b16 v[74:75], v29 offset:1024\n"
  "v_mfma_f32_16x16x32_bf16 v[132:135], v[192:195], v[164:167], v[132:135]\n"
  "s_cbranch_vccz P2_C55\n"
  "ds_read_b64_tr_b16 v[76:77], v31 offset:0\n"
  "ds_read_b64_tr_b16 v[78:79], v31 offset:1024\n"
  "P2_C55:\n"
  "v_mfma_f32_16x16x32_bf16 v[152:155], v[192:195], v[168:171], v[152:155]\n"
  "ds_read_b64_tr_b16 v[92:93], v25 offset:2048\n"
  "ds_read_b64_tr_b16 v[94:95], v25 offset:3072\n"
  "s_cbranch_vccz P2_C56\n"
  "v_mfma_f32_16x16x32_bf16 v[160:163], v[196:199], v[172:175], v[160:163]\n"
  "P2_C56:\n"
  "ds_read_b128 v[80:83], v245 offset:1024\n"
  "v_mfma_f32_16x16x32_bf16 v[116:119], v[212:215], v[200:203], v[116:119]\n"
  "s_setprio 1\n"
  "ds_read_b128 v[84:87], v245 offset:3072\n"
  "ds_read_b128 v[88:91], v247 offset:1024\n"
  "v_mfma_f32_16x16x32_bf16 v[136:139], v[212:215], v[204:207], v[136:139]\n"
  "ds_read_b64_tr_b16 v[96:97], v26 offset:2048\n"
  "v_mfma_f32_16x16x32_bf16 v[156:159], v[212:215], v[208:211], v[156:159]\n"
  "ds_read_b64_tr_b16 v[98:99], v26 offset:3072\n"
  "v_mfma_f32_16x16x32_bf16 v[120:123], v[216:219], v[200:203], v[120:123]\n"
  "ds_read_b64_tr_b16 v[100:101], v27 offset:2048\n"
  "ds_read_b64_tr_b16 v[102:103], v27 offset:3072\n"
  "v_mfma_f32_16x16x32_bf16 v[140:143], v[216:219], v[204:207], v[140:143]\n"
  "ds_read_b64_tr_b16 v[104:105], v28 offset:2048\n"
  "v_mfma_f32_16x16x32_bf16 v[124:127], v[220:223], v[200:203], v[124:127]\n"
  "ds_read_b64_tr_b16 v[106:107], v28 offset:3072\n"
  "ds_read_b64_tr_b16 v[108:109], v29 offset:2048\n"
  "v_mfma_f32_16x16x32_bf16 v[144:147], v[220:223], v[204:207], v[144:147]\n"
  "s_setprio 0\n"
  "ds_read_b64_tr_b16 v[110:111], v29 offset:3072\n"
  "v_mfma_f32_16x16x32_bf16 v[128:131], v[224:227], v[200:203], v[128:131]\n"
  "s_cbranch_vccz P2_C57\n"
  "ds_read_b64_tr_b16 v[112:113], v31 offset:2048\n"
  "ds_read_b64_tr_b16 v[114:115], v31 offset:3072\n"
  "P2_C57:\n"
  "v_mfma_f32_16x16x32_bf16 v[148:151], v[224:227], v[204:207], v[148:151]\n"
  "v_mfma_f32_16x16x32_bf16 v[132:135], v[228:231], v[200:203], v[132:135]\n"
  "v_mfma_f32_16x16x32_bf16 v[152:155], v[228:231], v[204:207], v[152:155]\n"
  "s_cbranch_vccz P2_C58\n"
  "v_mfma_f32_16x16x32_bf16 v[160:163], v[232:235], v[208:211], v[160:163]\n"
  "P2_C58:\n"
  "s_waitcnt vmcnt(0) lgkmcnt(0)\n"
  "s_barrier\n"
  "v_mfma_f32_16x16x32_bf16 v[116:119], v[56:59], v[44:47], v[116:119]\n"
  "s_setprio 3\n"
  "ds_read_b64_tr_b16 v[176:177], v25 offset:38912\n"
  "ds_read_b64_tr_b16 v[178:179], v25 offset:39936\n"
  "v_mfma_f32_16x16x32_bf16 v[136:139], v[56:59], v[48:51], v[136:139]\n"
  "ds_read_b128 v[164:167], v245 offset:38912\n"
  "v_mfma_f32_16x16x32_bf16 v[156:159], v[56:59], v[52:55], v[156:159]\n"
  "ds_read_b128 v[168:171], v245 offset:40960\n"
  "ds_read_b128 v[172:175], v247 offset:38912\n"
  "v_mfma_f32_16x16x32_bf16 v[120:123], v[60:63], v[44:47], v[120:123]\n"
  "ds_read_b64_tr_b16 v[180:181], v26 offset:38912\n"
  "v_mfma_f32_16x16x32_bf16 v[140:143], v[60:63], v[48:51], v[140:143]\n"
  "ds_read_b64_tr_b16 v[182:183], v26 offset:39936\n"
  "v_mfma_f32_16x16x32_bf16 v[124:127], v[64:67], v[44:47], v[124:127]\n"
  "ds_read_b64_tr_b16 v[184:185], v27 offset:38912\n"
  "ds_read_b64_tr_b16 v[186:187], v27 offset:39936\n"
  "v_mfma_f32_16x16x32_bf16 v[144:147], v[64:67], v[48:51], v[144:147]\n"
  "s_setprio 2\n"
  "ds_read_b64_tr_b16 v[188:189], v28 offset:38912\n"
  "v_mfma_f32_16x16x32_bf16 v[128:131], v[68:71], v[44:47], v[128:131]\n"
  "ds_read_b64_tr_b16 v[190:191], v28 offset:39936\n"
  "ds_read_b64_tr_b16 v[192:193], v29 offset:38912\n"
  "v_mfma_f32_16x16x32_bf16 v[148:151], v[68:71], v[48:51], v[148:151]\n"
  "ds_read_b64_tr_b16 v[194:195], v29 offset:39936\n"
  "v_mfma_f32_16x16x32_bf16 v[132:135], v[72:75], v[44:47], v[132:135]\n"
  "s_cbranch_vccz P2_C59\n"
  "ds_read_b64_tr_b16 v[196:197], v31 offset:38912\n"
  "ds_read_b64_tr_b16 v[198:199], v31 offset:39936\n"
  "P2_C59:\n"
  "v_mfma_f32_16x16x32_bf16 v[152:155], v[72:75], v[48:51], v[152:155]\n"
  "ds_read_b64_tr_b16 v[212:213], v25 offset:40960\n"
  "ds_read_b64_tr_b16 v[214:215], v25 offset:41984\n"
  "s_cbranch_vccz P2_C60\n"
  "v_mfma_f32_16x16x32_bf16 v[160:163], v[76:79], v[52:55], v[160:163]\n"
  "P2_C60:\n"
  "ds_read_b128 v[200:203], v245 offset:39936\n"
  "v_mfma_f32_16x16x32_bf16 v[116:119], v[92:95], v[80:83], v[116:119]\n"
  "s_setprio 1\n"
  "ds_read_b128 v[204:207], v245 offset:41984\n"
  "ds_read_b128 v[208:211], v247 offset:39936\n"
  "v_mfma_f32_16x16x32_bf16 v[136:139], v[92:95], v[84:87], v[136:139]\n"
  "ds_read_b64_tr_b16 v[216:217], v26 offset:40960\n"
  "v_mfma_f32_16x16x32_bf16 v[156:159], v[92:95], v[88:91], v[156:159]\n"
  "ds_read_b64_tr_b16 v[218:219], v26 offset:41984\n"
  "v_mfma_f32_16x16x32_bf16 v[120:123], v[96:99], v[80:83], v[120:123]\n"
  "ds_read_b64_tr_b16 v[220:221], v27 offset:40960\n"
  "ds_read_b64_tr_b16 v[222:223], v27 offset:41984\n"
  "v_mfma_f32_16x16x32_bf16 v[140:143], v[96:99], v[84:87], v[140:143]\n"
  "ds_read_b64_tr_b16 v[224:225], v28 offset:40960\n"
  "v_mfma_f32_16x16x32_bf16 v[124:127], v[100:103], v[80:83], v[124:127]\n"
  "ds_read_b64_tr_b16 v[226:227], v28 offset:41984\n"
  "ds_read_b64_tr_b16 v[228:229], v29 offset:40960\n"
  "v_mfma_f32_16x16x32_bf16 v[144:147], v[100:103], v[84:87], v[144:147]\n"
  "s_setprio 0\n"
  "ds_read_b64_tr_b16 v[230:231], v29 offset:41984\n"
  "v_mfma_f32_16x16x32_bf16 v[128:131], v[104:107], v[80:83], v[128:131]\n"
  "s_cbranch_vccz P2_C61\n"
  "ds_read_b64_tr_b16 v[232:233], v31 offset:40960\n"
  "ds_read_b64_tr_b16 v[234:235], v31 offset:41984\n"
  "P2_C61:\n"
  "v_mfma_f32_16x16x32_bf16 v[148:151], v[104:107], v[84:87], v[148:151]\n"
  "v_mfma_f32_16x16x32_bf16 v[132:135], v[108:111], v[80:83], v[132:135]\n"
  "v_mfma_f32_16x16x32_bf16 v[152:155], v[108:111], v[84:87], v[152:155]\n"
  "s_cbranch_vccz P2_C62\n"
  "v_mfma_f32_16x16x32_bf16 v[160:163], v[112:115], v[88:91], v[160:163]\n"
  "P2_C62:\n"
  "s_waitcnt lgkmcnt(0)\n"
  "v_mfma_f32_16x16x32_bf16 v[116:119], v[176:179], v[164:167], v[116:119]\n"
  "s_setprio 3\n"
  "v_mfma_f32_16x16x32_bf16 v[136:139], v[176:179], v[168:171], v[136:139]\n"
  "v_mfma_f32_16x16x32_bf16 v[156:159], v[176:179], v[172:175], v[156:159]\n"
  "v_mfma_f32_16x16x32_bf16 v[120:123], v[180:183], v[164:167], v[120:123]\n"
  "v_mfma_f32_16x16x32_bf16 v[140:143], v[180:183], v[168:171], v[140:143]\n"
  "v_mfma_f32_16x16x32_bf16 v[124:127], v[184:187], v[164:167], v[124:127]\n"
  "v_mfma_f32_16x16x32_bf16 v[144:147], v[184:187], v[168:171], v[144:147]\n"
  "s_setprio 2\n"
  "v_mfma_f32_16x16x32_bf16 v[128:131], v[188:191], v[164:167], v[128:131]\n"
  "v_mfma_f32_16x16x32_bf16 v[148:151], v[188:191], v[168:171], v[148:151]\n"
  "v_mfma_f32_16x16x32_bf16 v[132:135], v[192:195], v[164:167], v[132:135]\n"
  "v_mfma_f32_16x16x32_bf16 v[152:155], v[192:195], v[168:171], v[152:155]\n"
  "s_cbranch_vccz P2_C63\n"
  "v_mfma_f32_16x16x32_bf16 v[160:163], v[196:199], v[172:175], v[160:163]\n"
  "P2_C63:\n"
  "v_mfma_f32_16x16x32_bf16 v[116:119], v[212:215], v[200:203], v[116:119]\n"
  "s_setprio 1\n"
  "v_mfma_f32_16x16x32_bf16 v[136:139], v[212:215], v[204:207], v[136:139]\n"
  "v_mfma_f32_16x16x32_bf16 v[156:159], v[212:215], v[208:211], v[156:159]\n"
  "v_mfma_f32_16x16x32_bf16 v[120:123], v[216:219], v[200:203], v[120:123]\n"
  "v_mfma_f32_16x16x32_bf16 v[140:143], v[216:219], v[204:207], v[140:143]\n"
  "v_mfma_f32_16x16x32_bf16 v[124:127], v[220:223], v[200:203], v[124:127]\n"
  "v_mfma_f32_16x16x32_bf16 v[144:147], v[220:223], v[204:207], v[144:147]\n"
  "s_setprio 0\n"
  "v_mfma_f32_16x16x32_bf16 v[128:131], v[224:227], v[200:203], v[128:131]\n"
  "v_mfma_f32_16x16x32_bf16 v[148:151], v[224:227], v[204:207], v[148:151]\n"
  "v_mfma_f32_16x16x32_bf16 v[132:135], v[228:231], v[200:203], v[132:135]\n"
  "v_mfma_f32_16x16x32_bf16 v[152:155], v[228:231], v[204:207], v[152:155]\n"
  "s_cbranch_vccz P2_C64\n"
  "v_mfma_f32_16x16x32_bf16 v[160:163], v[232:235], v[208:211], v[160:163]\n"
  "P2_C64:\n"
  "s_lshl_b32 s38, %[hg], 0\n"
  "s_mul_i32 s57, %[hg], 24576\n"
  "s_mul_i32 s39, %[wave], 3072\n"
  "s_add_u32 s57, s57, s39\n"
  "s_nop 7\n"
  "s_nop 3\n"
  "s_cmp_eq_u32 %[hg], 0\n"
  "s_cbranch_scc1 P2_KEEP0\n"
  "s_add_u32 s38, s57, 0\n"
  "v_cvt_pk_bf16_f32 v164, v116, v117\n"
  "v_cvt_pk_bf16_f32 v165, v118, v119\n"
  "v_cvt_pk_bf16_f32 v166, v120, v121\n"
  "v_cvt_pk_bf16_f32 v167, v122, v123\n"
  "v_cvt_pk_bf16_f32 v168, v124, v125\n"
  "v_cvt_pk_bf16_f32 v169, v126, v127\n"
  "buffer_store_dwordx4 v[164:167], v17, %[srdx2], s38 offen offset:0 sc1\n"
  "v_lshrrev_b32 v3, 1, v17\n"
  "buffer_store_dwordx2 v[168:169], v3, %[srdx2], s38 offen offset:1024 sc1\n"
  "s_branch P2_NEXT0\n"
  "P2_KEEP0:\n"
  "v_mov_b64 v[44:45], v[116:117]\n"
  "v_mov_b64 v[46:47], v[118:119]\n"
  "s_add_u32 s38, s37, 0\n"
  "s_cmp_ge_u32 s38, 5\n"
  "s_cselect_b32 s39, 5, 0\n"
  "s_sub_u32 s38, s38, s39\n"
  "s_lshl_b32 s39, s37, 1\n"
  "s_mul_i32 s40, s36, 5\n"
  "s_add_u32 s38, s38, s40\n"
  "s_lshl_b32 s38, s38, 5\n"
  "s_mul_i32 s39, s39, 40960\n"
  "s_add_u32 s42, s38, s39\n"
  "v_mov_b64 v[48:49], v[120:121]\n"
  "v_mov_b64 v[50:51], v[122:123]\n"
  "s_add_u32 s38, s37, 1\n"
  "s_cmp_ge_u32 s38, 5\n"
  "s_cselect_b32 s39, 5, 0\n"
  "s_sub_u32 s38, s38, s39\n"
  "s_lshl_b32 s39, s37, 1\n"
  "s_mul_i32 s40, s36, 5\n"
  "s_add_u32 s38, s38, s40\n"
  "s_lshl_b32 s38, s38, 5\n"
  "s_mul_i32 s39, s39, 40960\n"
  "s_add_u32 s43, s38, s39\n"
  "v_mov_b64 v[52:53], v[124:125]\n"
  "v_mov_b64 v[54:55], v[126:127]\n"
  "s_add_u32 s38, s37, 2\n"
  "s_cmp_ge_u32 s38, 5\n"
  "s_cselect_b32 s39, 5, 0\n"
  "s_sub_u32 s38, s38, s39\n"
  "s_lshl_b32 s39, s37, 1\n"
  "s_mul_i32 s40, s36, 5\n"
  "s_add_u32 s38, s38, s40\n"
  "s_lshl_b32 s38, s38, 5\n"
  "s_mul_i32 s39, s39, 40960\n"
  "s_add_u32 s44, s38, s39\n"
  "P2_NEXT0:\n"
  "s_nop 7\n"
  "s_nop 3\n"
  "s_cmp_eq_u32 %[hg], 1\n"
  "s_cbranch_scc1 P2_KEEP1\n"
  "s_add_u32 s38, s57, 98304\n"
  "v_cvt_pk_bf16_f32 v172, v128, v129\n"
  "v_cvt_pk_bf16_f32 v173, v130, v131\n"
  "v_cvt_pk_bf16_f32 v174, v132, v133\n"
  "v_cvt_pk_bf16_f32 v175, v134, v135\n"
  "v_cvt_pk_bf16_f32 v176, v136, v137\n"
  "v_cvt_pk_bf16_f32 v177, v138, v139\n"
  "buffer_store_dwordx4 v[172:175], v17, %[srdx2], s38 offen offset:0 sc1\n"
  "v_lshrrev_b32 v3, 1, v17\n"
  "buffer_store_dwordx2 v[176:177], v3, %[srdx2], s38 offen offset:1024 sc1\n"
  "s_branch P2_NEXT1\n"
  "P2_KEEP1:\n"
  "v_mov_b64 v[44:45], v[128:129]\n"
  "v_mov_b64 v[46:47], v[130:131]\n"
  "s_add_u32 s38, s37, 3\n"
  "s_cmp_ge_u32 s38, 5\n"
  "s_cselect_b32 s39, 5, 0\n"
  "s_sub_u32 s38, s38, s39\n"
  "s_lshl_b32 s39, s37, 1\n"
  "s_mul_i32 s40, s36, 5\n"
  "s_add_u32 s38, s38, s40\n"
  "s_lshl_b32 s38, s38, 5\n"
  "s_mul_i32 s39, s39, 40960\n"
  "s_add_u32 s42, s38, s39\n"
  "v_mov_b64 v[48:49], v[132:133]\n"
  "v_mov_b64 v[50:51], v[134:135]\n"
  "s_add_u32 s38, s37, 4\n"
  "s_cmp_ge_u32 s38, 5\n"
  "s_cselect_b32 s39, 5, 0\n"
  "s_sub_u32 s38, s38, s39\n"
  "s_lshl_b32 s39, s37, 1\n"
  "s_mul_i32 s40, s36, 5\n"
  "s_add_u32 s38, s38, s40\n"
  "s_lshl_b32 s38, s38, 5\n"
  "s_mul_i32 s39, s39, 40960\n"
  "s_add_u32 s43, s38, s39\n"
  "v_mov_b64 v[52:53], v[136:137]\n"
  "v_mov_b64 v[54:55], v[138:139]\n"
  "s_add_u32 s38, s37, 0\n"
  "s_cmp_ge_u32 s38, 5\n"
  "s_cselect_b32 s39, 5, 0\n"
  "s_sub_u32 s38, s38, s39\n"
  "s_lshl_b32 s39, s37, 1\n"
  "s_add_u32 s39, s39, 1\n"
  "s_mul_i32 s40, s36, 5\n"
  "s_add_u32 s38, s38, s40\n"
  "s_lshl_b32 s38, s38, 5\n"
  "s_mul_i32 s39, s39, 40960\n"
  "s_add_u32 s44, s38, s39\n"
  "P2_NEXT1:\n"
  "s_nop 7\n"
  "s_nop 3\n"
  "s_cmp_eq_u32 %[hg], 2\n"
  "s_cbranch_scc1 P2_KEEP2\n"
  "s_add_u32 s38, s57, 196608\n"
  "v_cvt_pk_bf16_f32 v180, v140, v141\n"
  "v_cvt_pk_bf16_f32 v181, v142, v143\n"
  "v_cvt_pk_bf16_f32 v182, v144, v145\n"
  "v_cvt_pk_bf16_f32 v183, v146, v147\n"
  "v_cvt_pk_bf16_f32 v184, v148, v149\n"
  "v_cvt_pk_bf16_f32 v185, v150, v151\n"
  "buffer_store_dwordx4 v[180:183], v17, %[srdx2], s38 offen offset:0 sc1\n"
  "v_lshrrev_b32 v3, 1, v17\n"
  "buffer_store_dwordx2 v[184:185], v3, %[srdx2], s38 offen offset:1024 sc1\n"
  "s_branch P2_NEXT2\n"
  "P2_KEEP2:\n"
  "v_mov_b64 v[44:45], v[140:141]\n"
  "v_mov_b64 v[46:47], v[142:143]\n"
  "s_add_u32 s38, s37, 1\n"
  "s_cmp_ge_u32 s38, 5\n"
  "s_cselect_b32 s39, 5, 0\n"
  "s_sub_u32 s38, s38, s39\n"
  "s_lshl_b32 s39, s37, 1\n"
  "s_add_u32 s39, s39, 1\n"
  "s_mul_i32 s40, s36, 5\n"
  "s_add_u32 s38, s38, s40\n"
  "s_lshl_b32 s38, s38, 5\n"
  "s_mul_i32 s39, s39, 40960\n"
  "s_add_u32 s42, s38, s39\n"
  "v_mov_b64 v[48:49], v[144:145]\n"
  "v_mov_b64 v[50:51], v[146:147]\n"
  "s_add_u32 s38, s37, 2\n"
  "s_cmp_ge_u32 s38, 5\n"
  "s_cselect_b32 s39, 5, 0\n"
  "s_sub_u32 s38, s38, s39\n"
  "s_lshl_b32 s39, s37, 1\n"
  "s_add_u32 s39, s39, 1\n"
  "s_mul_i32 s40, s36, 5\n"
  "s_add_u32 s38, s38, s40\n"
  "s_lshl_b32 s38, s38, 5\n"
  "s_mul_i32 s39, s39, 40960\n"
  "s_add_u32 s43, s38, s39\n"
  "v_mov_b64 v[52:53], v[148:149]\n"
  "v_mov_b64 v[54:55], v[150:151]\n"
  "s_add_u32 s38, s37, 3\n"
  "s_cmp_ge_u32 s38, 5\n"
  "s_cselect_b32 s39, 5, 0\n"
  "s_sub_u32 s38, s38, s39\n"
  "s_lshl_b32 s39, s37, 1\n"
  "s_add_u32 s39, s39, 1\n"
  "s_mul_i32 s40, s36, 5\n"
  "s_add_u32 s38, s38, s40\n"
  "s_lshl_b32 s38, s38, 5\n"
  "s_mul_i32 s39, s39, 40960\n"
  "s_add_u32 s44, s38, s39\n"
  "P2_NEXT2:\n"
  "s_nop 7\n"
  "s_nop 3\n"
  "s_cmp_eq_u32 %[hg], 3\n"
  "s_cbranch_scc1 P2_KEEP3\n"
  "s_add_u32 s38, s57, 294912\n"
  "v_cvt_pk_bf16_f32 v188, v152, v153\n"
  "v_cvt_pk_bf16_f32 v189, v154, v155\n"
  "v_cvt_pk_bf16_f32 v190, v156, v157\n"
  "v_cvt_pk_bf16_f32 v191, v158, v159\n"
  "v_cvt_pk_bf16_f32 v192, v160, v161\n"
  "v_cvt_pk_bf16_f32 v193, v162, v163\n"
  "buffer_store_dwordx4 v[188:191], v17, %[srdx2], s38 offen offset:0 sc1\n"
  "v_lshrrev_b32 v3, 1, v17\n"
  "buffer_store_dwordx2 v[192:193], v3, %[srdx2], s38 offen offset:1024 sc1\n"
  "s_branch P2_NEXT3\n"
  "P2_KEEP3:\n"
  "v_mov_b64 v[44:45], v[152:153]\n"
  "v_mov_b64 v[46:47], v[154:155]\n"
  "s_add_u32 s38, s37, 4\n"
  "s_cmp_ge_u32 s38, 5\n"
  "s_cselect_b32 s39, 5, 0\n"
  "s_sub_u32 s38, s38, s39\n"
  "s_lshl_b32 s39, s37, 1\n"
  "s_add_u32 s39, s39, 1\n"
  "s_mul_i32 s40, s36, 5\n"
  "s_add_u32 s38, s38, s40\n"
  "s_lshl_b32 s38, s38, 5\n"
  "s_mul_i32 s39, s39, 40960\n"
  "s_add_u32 s42, s38, s39\n"
  "v_mov_b64 v[48:49], v[156:157]\n"
  "v_mov_b64 v[50:51], v[158:159]\n"
  "s_mov_b32 s38, s37\n"
  "s_mov_b32 s39, 8\n"
  "s_mul_i32 s40, s36, 5\n"
  "s_add_u32 s38, s38, s40\n"
  "s_lshl_b32 s38, s38, 5\n"
  "s_mul_i32 s39, s39, 40960\n"
  "s_add_u32 s43, s38, s39\n"
  "v_mov_b64 v[52:53], v[160:161]\n"
  "v_mov_b64 v[54:55], v[162:163]\n"
  "s_mov_b32 s38, 4\n"
  "s_mov_b32 s39, 8\n"
  "s_mul_i32 s40, s36, 5\n"
  "s_add_u32 s38, s38, s40\n"
  "s_lshl_b32 s38, s38, 5\n"
  "s_mul_i32 s39, s39, 40960\n"
  "s_add_u32 s44, s38, s39\n"
  "s_cmp_eq_u32 s37, s36\n"
  "s_cselect_b32 s44, s44, 0x2000000\n"
  "P2_NEXT3:\n"
  "s_nop 7\n"
  "s_nop 3\n"
  "s_waitcnt vmcnt(0)\n"
  "s_lshl_b32 s38, %[grp], 4\n"
  "s_lshl_b32 s39, %[jx], 2\n"
  "s_add_u32 s38, s38, s39\n"
  "s_lshl_b32 s38, s38, 9\n"
  "s_lshl_b32 s39, %[wave], 6\n"
  "s_add_u32 s38, s38, s39\n"
  "s_add_u32 s38, s38, 34816\n"
  "s_lshl_b32 s39, %[hg], 9\n"
  "s_add_u32 s39, s39, s38\n"
  "v_and_b32 v240, 3, v1\n"
  "v_max_u32 v240, 1, v240\n"
  "v_add_u32 v240, %[hg], v240\n"
  "v_and_b32 v240, 3, v240\n"
  "v_lshlrev_b32 v240, 9, v240\n"
  "v_mov_b32 v241, s34\n"
  "s_mov_b32 s41, 0x20000\n"
  "buffer_load_dword v242, v240, %[srdf], s38 offen offset:0 sc1\n"
  "v_mov_b32 v243, 0\n"
  "s_mov_b64 exec, 1\n"
  "buffer_store_dword v241, v243, %[srdf], s39 offen offset:0 sc1\n"
  "s_mov_b64 exec, -1\n"
  "s_waitcnt vmcnt(1)\n"
  "s_branch P2_F2W_C\n"
  "P2_F2W_L:\n"
  "buffer_load_dword v242, v240, %[srdf], s38 offen offset:0 sc1\n"
  "s_waitcnt vmcnt(0)\n"
  "P2_F2W_C:\n"
  "v_cmp_eq_u32 s[58:59], v242, v241\n"
  "s_nop 4\n"
  "s_cmp_eq_u64 s[58:59], -1\n"
  "s_cbranch_scc1 P2_F2W_S\n"
  "s_sleep 1\n"
  "s_sub_u32 s41, s41, 1\n"
  "s_cmp_lg_u32 s41, 0\n"
  "s_cbranch_scc1 P2_F2W_L\n"
  "P2_F2W_S:\n"
  "s_nop 7\n"
  "s_nop 3\n"
  "s_lshl_b32 s38, %[grp], 7\n"
  "s_cmp_eq_u32 %[wave], 0\n"
  "s_cbranch_scc0 P2_GP_S\n"
  "v_mov_b32 v241, s34\n"
  "v_mov_b32 v240, 0\n"
  "s_mov_b64 exec, 1\n"
  "buffer_store_dword v241, v240, %[srdf], s38 offen offset:0 sc1\n"
  "s_mov_b64 exec, -1\n"
  "P2_GP_S:\n"
  "s_nop 7\n"
  "s_nop 3\n"
  "s_add_u32 s38, %[hg], 1\n"
  "s_and_b32 s38, s38, 3\n"
  "s_mul_i32 s38, s38, 24576\n"
  "s_mul_i32 s39, %[hg], 98304\n"
  "s_add_u32 s38, s38, s39\n"
  "s_mul_i32 s39, %[wave], 3072\n"
  "s_add_u32 s38, s38, s39\n"
  "buffer_load_dwordx4 v[60:63], v17, %[srdx2], s38 offen offset:0 sc1\n"
  "v_lshrrev_b32 v3, 1, v17\n"
  "buffer_load_dwordx2 v[64:65], v3, %[srdx2], s38 offen offset:1024 sc1\n"
  "s_add_u32 s38, %[hg], 2\n"
  "s_and_b32 s38, s38, 3\n"
  "s_mul_i32 s38, s38, 24576\n"
  "s_mul_i32 s39, %[hg], 98304\n"
  "s_add_u32 s38, s38, s39\n"
  "s_mul_i32 s39, %[wave], 3072\n"
  "s_add_u32 s38, s38, s39\n"
  "buffer_load_dwordx4 v[68:71], v17, %[srdx2], s38 offen offset:0 sc1\n"
  "buffer_load_dwordx2 v[72:73], v3, %[srdx2], s38 offen offset:1024 sc1\n"
  "s_add_u32 s38, %[hg], 3\n"
  "s_and_b32 s38, s38, 3\n"
  "s_mul_i32 s38, s38, 24576\n"
  "s_mul_i32 s39, %[hg], 98304\n"
  "s_add_u32 s38, s38, s39\n"
  "s_mul_i32 s39, %[wave], 3072\n"
  "s_add_u32 s38, s38, s39\n"
  "buffer_load_dwordx4 v[76:79], v17, %[srdx2], s38 offen offset:0 sc1\n"
  "buffer_load_dwordx2 v[80:81], v3, %[srdx2], s38 offen offset:1024 sc1\n"
  "s_waitcnt vmcnt(4)\n"
  "v_lshlrev_b32 v5, 16, v60\n"
  "v_and_b32 v7, 0xffff0000, v60\n"
  "v_add_f32 v44, v44, v5\n"
  "v_add_f32 v45, v45, v7\n"
  "v_lshlrev_b32 v5, 16, v61\n"
  "v_and_b32 v7, 0xffff0000, v61\n"
  "v_add_f32 v46, v46, v5\n"
  "v_add_f32 v47, v47, v7\n"
  "v_lshlrev_b32 v5, 16, v62\n"
  "v_and_b32 v7, 0xffff0000, v62\n"
  "v_add_f32 v48, v48, v5\n"
  "v_add_f32 v49, v49, v7\n"
  "v_lshlrev_b32 v5, 16, v63\n"
  "v_and_b32 v7, 0xffff0000, v63\n"
  "v_add_f32 v50, v50, v5\n"
  "v_add_f32 v51, v51, v7\n"
  "v_lshlrev_b32 v5, 16, v64\n"
  "v_and_b32 v7, 0xffff0000, v64\n"
  "v_add_f32 v52, v52, v5\n"
  "v_add_f32 v53, v53, v7\n"
  "v_lshlrev_b32 v5, 16, v65\n"
  "v_and_b32 v7, 0xffff0000, v65\n"
  "v_add_f32 v54, v54, v5\n"
  "v_add_f32 v55, v55, v7\n"
  "s_waitcnt vmcnt(2)\n"
  "v_lshlrev_b32 v5, 16, v68\n"
  "v_and_b32 v7, 0xffff0000, v68\n"
  "v_add_f32 v44, v44, v5\n"
  "v_add_f32 v45, v45, v7\n"
  "v_lshlrev_b32 v5, 16, v69\n"
  "v_and_b32 v7, 0xffff0000, v69\n"
  "v_add_f32 v46, v46, v5\n"
  "v_add_f32 v47, v47, v7\n"
  "v_lshlrev_b32 v5, 16, v70\n"
  "v_and_b32 v7, 0xffff0000, v70\n"
  "v_add_f32 v48, v48, v5\n"
  "v_add_f32 v49, v49, v7\n"
  "v_lshlrev_b32 v5, 16, v71\n"
  "v_and_b32 v7, 0xffff0000, v71\n"
  "v_add_f32 v50, v50, v5\n"
  "v_add_f32 v51, v51, v7\n"
  "v_lshlrev_b32 v5, 16, v72\n"
  "v_and_b32 v7, 0xffff0000, v72\n"
  "v_add_f32 v52, v52, v5\n"
  "v_add_f32 v53, v53, v7\n"
  "v_lshlrev_b32 v5, 16, v73\n"
  "v_and_b32 v7, 0xffff0000, v73\n"
  "v_add_f32 v54, v54, v5\n"
  "v_add_f32 v55, v55, v7\n"
  "s_waitcnt vmcnt(0)\n"
  "v_lshlrev_b32 v5, 16, v76\n"
  "v_and_b32 v7, 0xffff0000, v76\n"
  "v_add_f32 v44, v44, v5\n"
  "v_add_f32 v45, v45, v7\n"
  "v_lshlrev_b32 v5, 16, v77\n"
  "v_and_b32 v7, 0xffff0000, v77\n"
  "v_add_f32 v46, v46, v5\n"
  "v_add_f32 v47, v47, v7\n"
  "v_lshlrev_b32 v5, 16, v78\n"
  "v_and_b32 v7, 0xffff0000, v78\n"
  "v_add_f32 v48, v48, v5\n"
  "v_add_f32 v49, v49, v7\n"
  "v_lshlrev_b32 v5, 16, v79\n"
  "v_and_b32 v7, 0xffff0000, v79\n"
  "v_add_f32 v50, v50, v5\n"
  "v_add_f32 v51, v51, v7\n"
  "v_lshlrev_b32 v5, 16, v80\n"
  "v_and_b32 v7, 0xffff0000, v80\n"
  "v_add_f32 v52, v52, v5\n"
  "v_add_f32 v53, v53, v7\n"
  "v_lshlrev_b32 v5, 16, v81\n"
  "v_and_b32 v7, 0xffff0000, v81\n"
  "v_add_f32 v54, v54, v5\n"
  "v_add_f32 v55, v55, v7\n"
  "v_cvt_pk_bf16_f32 v100, v44, v45\n"
  "v_cvt_pk_bf16_f32 v101, v46, v47\n"
  "buffer_store_dwordx2 v[100:101], v15, %[srdout], s42 offen offset:0\n"
  "v_cvt_pk_bf16_f32 v102, v48, v49\n"
  "v_cvt_pk_bf16_f32 v103, v50, v51\n"
  "buffer_store_dwordx2 v[102:103], v15, %[srdout], s43 offen offset:0\n"
  "v_cvt_pk_bf16_f32 v104, v52, v53\n"
  "v_cvt_pk_bf16_f32 v105, v54, v55\n"
  "buffer_store_dwordx2 v[104:105], v15, %[srdout], s44 offen offset:0\n"
  "s_branch P1_END\n"
  "P1_SLOW:\n"
  "s_mov_b32 s35, 1\n"
  "s_waitcnt vmcnt(0) lgkmcnt(0)\n"
  "s_barrier\n"
  "s_nop 7\n"
  "s_nop 3\n"
  "v_lshrrev_b32 v2, 4, v1\n"
  "v_bfe_u32 v3, v1, 2, 2\n"
  "v_and_b32 v4, 3, v1\n"
  "v_and_b32 v5, 15, v1\n"
  "v_lshlrev_b32 v6, 6, v2\n"
  "v_lshl_add_u32 v6, v3, 8, v6\n"
  "v_lshl_add_u32 v10, v4, 4, v6\n"
  "v_xor_b32 v11, 32, v10\n"
  "v_lshlrev_b32 v6, 11, v2\n"
  "v_lshl_add_u32 v6, v3, 6, v6\n"
  "v_lshrrev_b32 v7, 1, v4\n"
  "v_lshl_add_u32 v6, v7, 4, v6\n"
  "v_and_b32 v7, 1, v4\n"
  "v_lshl_add_u32 v6, v7, 3, v6\n"
  "v_and_b32 v7, 1, v2\n"
  "s_lshl_b32 s39, s36, 13\n"
  "s_add_u32 s39, s39, 0\n"
  "s_add_u32 s39, s39, %[ldsb]\n"
  "s_xor_b32 s40, s37, 0\n"
  "s_lshl_b32 s40, s40, 8\n"
  "s_add_u32 s40, s40, s39\n"
  "v_xor_b32 v8, 0, v7\n"
  "v_lshl_add_u32 v8, v8, 5, v6\n"
  "v_add_u32 v22, s40, v8\n"
  "v_xor_b32 v8, 1, v7\n"
  "v_lshl_add_u32 v8, v8, 5, v6\n"
  "v_add_u32 v23, s40, v8\n"
  "s_xor_b32 s40, s37, 1\n"
  "s_lshl_b32 s40, s40, 8\n"
  "s_add_u32 s40, s40, s39\n"
  "v_xor_b32 v8, 0, v7\n"
  "v_lshl_add_u32 v8, v8, 5, v6\n"
  "v_add_u32 v24, s40, v8\n"
  "v_xor_b32 v8, 1, v7\n"
  "v_lshl_add_u32 v8, v8, 5, v6\n"
  "v_add_u32 v25, s40, v8\n"
  "s_xor_b32 s40, s37, 2\n"
  "s_lshl_b32 s40, s40, 8\n"
  "s_add_u32 s40, s40, s39\n"
  "v_xor_b32 v8, 0, v7\n"
  "v_lshl_add_u32 v8, v8, 5, v6\n"
  "v_add_u32 v26, s40, v8\n"
  "v_xor_b32 v8, 1, v7\n"
  "v_lshl_add_u32 v8, v8, 5, v6\n"
  "v_add_u32 v27, s40, v8\n"
  "s_xor_b32 s40, s37, 3\n"
  "s_lshl_b32 s40, s40, 8\n"
  "s_add_u32 s40, s40, s39\n"
  "v_xor_b32 v8, 0, v7\n"
  "v_lshl_add_u32 v8, v8, 5, v6\n"
  "v_add_u32 v28, s40, v8\n"
  "v_xor_b32 v8, 1, v7\n"
  "v_lshl_add_u32 v8, v8, 5, v6\n"
  "v_add_u32 v29, s40, v8\n"
  "v_lshrrev_b32 v6, 3, v5\n"
  "v_lshlrev_b32 v6, 10, v6\n"
  "v_and_b32 v7, 7, v5\n"
  "v_lshl_add_u32 v6, v7, 6, v6\n"
  "v_and_b32 v7, 1, v3\n"
  "v_lshlrev_b32 v7, 1, v7\n"
  "v_xor_b32 v7, v7, v2\n"
  "v_lshl_add_u32 v6, v7, 4, v6\n"
  "s_lshl_b32 s39, s37, 12\n"
  "s_add_u32 s39, s39, 65536\n"
  "s_add_u32 s39, s39, %[ldsb]\n"
  "v_add_u32 v20, s39, v6\n"
  "s_add_u32 s39, %[ldsb], 81920\n"
  "v_add_u32 v21, s39, v6\n"
  "v_lshlrev_b32 v17, 4, v1\n"
  "s_lshl_b32 s50, s36, 17\n"
  "s_lshl_b32 s39, s37, 12\n"
  "s_add_u32 s50, s50, s39\n"
  "s_lshl_b32 s51, s36, 15\n"
  "s_add_u32 s51, s51, s39\n"
  "s_add_u32 s51, s51, %[ldsb]\n"
  "v_lshrrev_b32 v2, 4, v1\n"
  "v_bfe_u32 v3, v1, 2, 2\n"
  "v_and_b32 v4, 3, v1\n"
  "v_and_b32 v5, 15, v1\n"
  "s_lshl_b32 s38, s36, 15\n"
  "s_add_u32 s38, s38, %[ldsb]\n"
  "v_lshlrev_b32 v6, 10, v3\n"
  "v_lshl_add_u32 v6, v4, 6, v6\n"
  "v_and_b32 v7, 1, v3\n"
  "v_lshlrev_b32 v7, 1, v7\n"
  "v_xor_b32 v7, v7, v2\n"
  "v_lshl_add_u32 v6, v7, 4, v6\n"
  "v_add_u32 v12, s38, v6\n"
  "v_add_u32 v240, 65536, v12\n"
  "v_lshlrev_b32 v6, 10, v2\n"
  "v_lshl_add_u32 v6, v3, 6, v6\n"
  "v_lshrrev_b32 v7, 1, v4\n"
  "v_lshl_add_u32 v6, v7, 4, v6\n"
  "v_and_b32 v7, 1, v4\n"
  "v_lshl_add_u32 v6, v7, 3, v6\n"
  "v_and_b32 v7, 1, v2\n"
  "s_add_u32 s39, s38, 16384\n"
  "v_xor_b32 v5, 0, v7\n"
  "v_lshl_add_u32 v5, v5, 5, v6\n"
  "v_add_u32 v13, s39, v5\n"
  "v_add_u32 v241, 65536, v13\n"
  "v_xor_b32 v5, 1, v7\n"
  "v_lshl_add_u32 v5, v5, 5, v6\n"
  "v_add_u32 v14, s39, v5\n"
  "v_add_u32 v242, 65536, v14\n"
  "s_lshl_b32 s39, s36, 11\n"
  "s_add_u32 s39, s39, 159744\n"
  "s_add_u32 s39, s39, %[ldsb]\n"
  "v_lshlrev_b32 v6, 3, v1\n"
  "v_add_u32 v16, s39, v6\n"
  "s_lshl_b32 s40, s37, 9\n"
  "v_add_u32 v15, s40, v16\n"
  "s_lshl_b32 s39, s36, 12\n"
  "s_add_u32 s39, s39, 151552\n"
  "s_add_u32 s39, s39, %[ldsb]\n"
  "v_add_u32 v246, s39, v17\n"
  "s_xor_b32 s38, s37, 0\n"
  "s_lshl_b32 s38, s38, 8\n"
  "v_add_u32 v248, s38, v12\n"
  "s_xor_b32 s38, s37, 1\n"
  "s_lshl_b32 s38, s38, 8\n"
  "v_add_u32 v249, s38, v12\n"
  "s_xor_b32 s38, s37, 2\n"
  "s_lshl_b32 s38, s38, 8\n"
  "v_add_u32 v250, s38, v12\n"
  "s_xor_b32 s38, s37, 3\n"
  "s_lshl_b32 s38, s38, 8\n"
  "v_add_u32 v251, s38, v12\n"
  "v_mov_b32 v20, 0xff800000\n"
  "v_mov_b32 v24, 0xff800000\n"
  "v_mov_b32 v28, 0xff800000\n"
  "s_mov_b32 s40, s50\n"
  "s_mov_b32 s41, 8\n"
  "s_nop 7\n"
  "s_nop 3\n"
  "P1_SLOOP:\n"
  "s_add_u32 m0, s51, 0\n"
  "s_nop 0\n"
  "buffer_load_dwordx4 v10, %[srdk], s40 offen offset:0 nt lds\n"
  "buffer_load_dwordx4 v11, %[srdk], s40 offen offset:1024 nt lds\n"
  "buffer_load_dwordx4 v10, %[srdk], s40 offen offset:2048 nt lds\n"
  "buffer_load_dwordx4 v11, %[srdk], s40 offen offset:3072 nt lds\n"
  "s_add_u32 s40, s40, 16384\n"
  "s_waitcnt vmcnt(0)\n"
  "s_barrier\n"
  "ds_read_b128 v[200:203], v248 offset:0\n"
  "ds_read_b128 v[216:219], v12 offset:0\n"
  "ds_read_b128 v[204:207], v249 offset:0\n"
  "ds_read_b128 v[220:223], v12 offset:256\n"
  "ds_read_b128 v[208:211], v250 offset:0\n"
  "ds_read_b128 v[224:227], v12 offset:512\n"
  "ds_read_b128 v[212:215], v251 offset:0\n"
  "ds_read_b128 v[228:231], v12 offset:768\n"
  "s_waitcnt lgkmcnt(7)\n"
  "v_mfma_f32_16x16x32_bf16 v[164:167], v[200:203], v[116:119], 0\n"
  "v_mfma_f32_16x16x32_bf16 v[180:183], v[200:203], v[132:135], 0\n"
  "s_waitcnt lgkmcnt(6)\n"
  "v_mfma_f32_16x16x32_bf16 v[196:199], v[216:219], v[148:151], 0\n"
  "s_waitcnt lgkmcnt(5)\n"
  "v_mfma_f32_16x16x32_bf16 v[164:167], v[204:207], v[120:123], v[164:167]\n"
  "v_mfma_f32_16x16x32_bf16 v[180:183], v[204:207], v[136:139], v[180:183]\n"
  "s_waitcnt lgkmcnt(4)\n"
  "v_mfma_f32_16x16x32_bf16 v[196:199], v[220:223], v[152:155], v[196:199]\n"
  "s_waitcnt lgkmcnt(3)\n"
  "v_mfma_f32_16x16x32_bf16 v[164:167], v[208:211], v[124:127], v[164:167]\n"
  "v_mfma_f32_16x16x32_bf16 v[180:183], v[208:211], v[140:143], v[180:183]\n"
  "s_waitcnt lgkmcnt(2)\n"
  "v_mfma_f32_16x16x32_bf16 v[196:199], v[224:227], v[156:159], v[196:199]\n"
  "s_waitcnt lgkmcnt(1)\n"
  "v_mfma_f32_16x16x32_bf16 v[164:167], v[212:215], v[128:131], v[164:167]\n"
  "v_mfma_f32_16x16x32_bf16 v[180:183], v[212:215], v[144:147], v[180:183]\n"
  "s_waitcnt lgkmcnt(0)\n"
  "v_mfma_f32_16x16x32_bf16 v[196:199], v[228:231], v[160:163], v[196:199]\n"
  "s_nop 4\n"
  "v_max3_f32 v20, v20, v164, v165\n"
  "v_max3_f32 v20, v20, v166, v167\n"
  "v_max3_f32 v24, v24, v180, v181\n"
  "v_max3_f32 v24, v24, v182, v183\n"
  "v_max3_f32 v28, v28, v196, v197\n"
  "v_max3_f32 v28, v28, v198, v199\n"
  "ds_read_b128 v[200:203], v248 offset:4096\n"
  "ds_read_b128 v[216:219], v12 offset:4096\n"
  "ds_read_b128 v[204:207], v249 offset:4096\n"
  "ds_read_b128 v[220:223], v12 offset:4352\n"
  "ds_read_b128 v[208:211], v250 offset:4096\n"
  "ds_read_b128 v[224:227], v12 offset:4608\n"
  "ds_read_b128 v[212:215], v251 offset:4096\n"
  "ds_read_b128 v[228:231], v12 offset:4864\n"
  "s_waitcnt lgkmcnt(7)\n"
  "v_mfma_f32_16x16x32_bf16 v[164:167], v[200:203], v[116:119], 0\n"
  "v_mfma_f32_16x16x32_bf16 v[180:183], v[200:203], v[132:135], 0\n"
  "s_waitcnt lgkmcnt(6)\n"
  "v_mfma_f32_16x16x32_bf16 v[196:199], v[216:219], v[148:151], 0\n"
  "s_waitcnt lgkmcnt(5)\n"
  "v_mfma_f32_16x16x32_bf16 v[164:167], v[204:207], v[120:123], v[164:167]\n"
  "v_mfma_f32_16x16x32_bf16 v[180:183], v[204:207], v[136:139], v[180:183]\n"
  "s_waitcnt lgkmcnt(4)\n"
  "v_mfma_f32_16x16x32_bf16 v[196:199], v[220:223], v[152:155], v[196:199]\n"
  "s_waitcnt lgkmcnt(3)\n"
  "v_mfma_f32_16x16x32_bf16 v[164:167], v[208:211], v[124:127], v[164:167]\n"
  "v_mfma_f32_16x16x32_bf16 v[180:183], v[208:211], v[140:143], v[180:183]\n"
  "s_waitcnt lgkmcnt(2)\n"
  "v_mfma_f32_16x16x32_bf16 v[196:199], v[224:227], v[156:159], v[196:199]\n"
  "s_waitcnt lgkmcnt(1)\n"
  "v_mfma_f32_16x16x32_bf16 v[164:167], v[212:215], v[128:131], v[164:167]\n"
  "v_mfma_f32_16x16x32_bf16 v[180:183], v[212:215], v[144:147], v[180:183]\n"
  "s_waitcnt lgkmcnt(0)\n"
  "v_mfma_f32_16x16x32_bf16 v[196:199], v[228:231], v[160:163], v[196:199]\n"
  "s_nop 4\n"
  "v_max3_f32 v20, v20, v164, v165\n"
  "v_max3_f32 v20, v20, v166, v167\n"
  "v_max3_f32 v24, v24, v180, v181\n"
  "v_max3_f32 v24, v24, v182, v183\n"
  "v_max3_f32 v28, v28, v196, v197\n"
  "v_max3_f32 v28, v28, v198, v199\n"
  "ds_read_b128 v[200:203], v248 offset:8192\n"
  "ds_read_b128 v[216:219], v12 offset:8192\n"
  "ds_read_b128 v[204:207], v249 offset:8192\n"
  "ds_read_b128 v[220:223], v12 offset:8448\n"
  "ds_read_b128 v[208:211], v250 offset:8192\n"
  "ds_read_b128 v[224:227], v12 offset:8704\n"
  "ds_read_b128 v[212:215], v251 offset:8192\n"
  "ds_read_b128 v[228:231], v12 offset:8960\n"
  "s_waitcnt lgkmcnt(7)\n"
  "v_mfma_f32_16x16x32_bf16 v[164:167], v[200:203], v[116:119], 0\n"
  "v_mfma_f32_16x16x32_bf16 v[180:183], v[200:203], v[132:135], 0\n"
  "s_waitcnt lgkmcnt(6)\n"
  "v_mfma_f32_16x16x32_bf16 v[196:199], v[216:219], v[148:151], 0\n"
  "s_waitcnt lgkmcnt(5)\n"
  "v_mfma_f32_16x16x32_bf16 v[164:167], v[204:207], v[120:123], v[164:167]\n"
  "v_mfma_f32_16x16x32_bf16 v[180:183], v[204:207], v[136:139], v[180:183]\n"
  "s_waitcnt lgkmcnt(4)\n"
  "v_mfma_f32_16x16x32_bf16 v[196:199], v[220:223], v[152:155], v[196:199]\n"
  "s_waitcnt lgkmcnt(3)\n"
  "v_mfma_f32_16x16x32_bf16 v[164:167], v[208:211], v[124:127], v[164:167]\n"
  "v_mfma_f32_16x16x32_bf16 v[180:183], v[208:211], v[140:143], v[180:183]\n"
  "s_waitcnt lgkmcnt(2)\n"
  "v_mfma_f32_16x16x32_bf16 v[196:199], v[224:227], v[156:159], v[196:199]\n"
  "s_waitcnt lgkmcnt(1)\n"
  "v_mfma_f32_16x16x32_bf16 v[164:167], v[212:215], v[128:131], v[164:167]\n"
  "v_mfma_f32_16x16x32_bf16 v[180:183], v[212:215], v[144:147], v[180:183]\n"
  "s_waitcnt lgkmcnt(0)\n"
  "v_mfma_f32_16x16x32_bf16 v[196:199], v[228:231], v[160:163], v[196:199]\n"
  "s_nop 4\n"
  "v_max3_f32 v20, v20, v164, v165\n"
  "v_max3_f32 v20, v20, v166, v167\n"
  "v_max3_f32 v24, v24, v180, v181\n"
  "v_max3_f32 v24, v24, v182, v183\n"
  "v_max3_f32 v28, v28, v196, v197\n"
  "v_max3_f32 v28, v28, v198, v199\n"
  "ds_read_b128 v[200:203], v248 offset:12288\n"
  "ds_read_b128 v[216:219], v12 offset:12288\n"
  "ds_read_b128 v[204:207], v249 offset:12288\n"
  "ds_read_b128 v[220:223], v12 offset:12544\n"
  "ds_read_b128 v[208:211], v250 offset:12288\n"
  "ds_read_b128 v[224:227], v12 offset:12800\n"
  "ds_read_b128 v[212:215], v251 offset:12288\n"
  "ds_read_b128 v[228:231], v12 offset:13056\n"
  "s_waitcnt lgkmcnt(7)\n"
  "v_mfma_f32_16x16x32_bf16 v[164:167], v[200:203], v[116:119], 0\n"
  "v_mfma_f32_16x16x32_bf16 v[180:183], v[200:203], v[132:135], 0\n"
  "s_waitcnt lgkmcnt(6)\n"
  "v_mfma_f32_16x16x32_bf16 v[196:199], v[216:219], v[148:151], 0\n"
  "s_waitcnt lgkmcnt(5)\n"
  "v_mfma_f32_16x16x32_bf16 v[164:167], v[204:207], v[120:123], v[164:167]\n"
  "v_mfma_f32_16x16x32_bf16 v[180:183], v[204:207], v[136:139], v[180:183]\n"
  "s_waitcnt lgkmcnt(4)\n"
  "v_mfma_f32_16x16x32_bf16 v[196:199], v[220:223], v[152:155], v[196:199]\n"
  "s_waitcnt lgkmcnt(3)\n"
  "v_mfma_f32_16x16x32_bf16 v[164:167], v[208:211], v[124:127], v[164:167]\n"
  "v_mfma_f32_16x16x32_bf16 v[180:183], v[208:211], v[140:143], v[180:183]\n"
  "s_waitcnt lgkmcnt(2)\n"
  "v_mfma_f32_16x16x32_bf16 v[196:199], v[224:227], v[156:159], v[196:199]\n"
  "s_waitcnt lgkmcnt(1)\n"
  "v_mfma_f32_16x16x32_bf16 v[164:167], v[212:215], v[128:131], v[164:167]\n"
  "v_mfma_f32_16x16x32_bf16 v[180:183], v[212:215], v[144:147], v[180:183]\n"
  "s_waitcnt lgkmcnt(0)\n"
  "v_mfma_f32_16x16x32_bf16 v[196:199], v[228:231], v[160:163], v[196:199]\n"
  "s_nop 4\n"
  "v_max3_f32 v20, v20, v164, v165\n"
  "v_max3_f32 v20, v20, v166, v167\n"
  "v_max3_f32 v24, v24, v180, v181\n"
  "v_max3_f32 v24, v24, v182, v183\n"
  "v_max3_f32 v28, v28, v196, v197\n"
  "v_max3_f32 v28, v28, v198, v199\n"
  "s_waitcnt lgkmcnt(0)\n"
  "s_barrier\n"
  "s_nop 7\n"
  "s_nop 3\n"
  "s_sub_u32 s41, s41, 1\n"
  "s_cmp_lg_u32 s41, 0\n"
  "s_cbranch_scc1 P1_SLOOP\n"
  "v_mov_b32 v2, v20\n"
  "s_nop 1\n"
  "v_permlane32_swap_b32 v20, v2\n"
  "v_max_f32 v20, v20, v2\n"
  "v_mov_b32 v2, v20\n"
  "s_nop 1\n"
  "v_permlane16_swap_b32 v20, v2\n"
  "v_max_f32 v20, v20, v2\n"
  "v_sub_f32 v20, 0, v20\n"
  "v_mov_b32 v21, v20\n"
  "v_mov_b32 v22, v20\n"
  "v_mov_b32 v23, v20\n"
  "v_mov_b32 v2, v24\n"
  "s_nop 1\n"
  "v_permlane32_swap_b32 v24, v2\n"
  "v_max_f32 v24, v24, v2\n"
  "v_mov_b32 v2, v24\n"
  "s_nop 1\n"
  "v_permlane16_swap_b32 v24, v2\n"
  "v_max_f32 v24, v24, v2\n"
  "v_sub_f32 v24, 0, v24\n"
  "v_mov_b32 v25, v24\n"
  "v_mov_b32 v26, v24\n"
  "v_mov_b32 v27, v24\n"
  "v_mov_b32 v2, v28\n"
  "s_nop 1\n"
  "v_permlane32_swap_b32 v28, v2\n"
  "v_max_f32 v28, v28, v2\n"
  "v_mov_b32 v2, v28\n"
  "s_nop 1\n"
  "v_permlane16_swap_b32 v28, v2\n"
  "v_max_f32 v28, v28, v2\n"
  "v_sub_f32 v28, 0, v28\n"
  "v_mov_b32 v29, v28\n"
  "v_mov_b32 v30, v28\n"
  "v_mov_b32 v31, v28\n"
  "s_add_u32 m0, s51, 0\n"
  "s_add_u32 s40, s50, 0\n"
  "buffer_load_dwordx4 v10, %[srdk], s40 offen offset:0 nt lds\n"
  "buffer_load_dwordx4 v11, %[srdk], s40 offen offset:1024 nt lds\n"
  "buffer_load_dwordx4 v10, %[srdk], s40 offen offset:2048 nt lds\n"
  "buffer_load_dwordx4 v11, %[srdk], s40 offen offset:3072 nt lds\n"
  "s_add_u32 m0, s51, 16384\n"
  "s_add_u32 s40, s50, 0\n"
  "buffer_load_dwordx4 v10, %[srdv], s40 offen offset:0 nt lds\n"
  "buffer_load_dwordx4 v11, %[srdv], s40 offen offset:1024 nt lds\n"
  "buffer_load_dwordx4 v10, %[srdv], s40 offen offset:2048 nt lds\n"
  "buffer_load_dwordx4 v11, %[srdv], s40 offen offset:3072 nt lds\n"
  "s_add_u32 m0, s51, 65536\n"
  "s_add_u32 s40, s50, 16384\n"
  "buffer_load_dwordx4 v10, %[srdk], s40 offen offset:0 nt lds\n"
  "buffer_load_dwordx4 v11, %[srdk], s40 offen offset:1024 nt lds\n"
  "buffer_load_dwordx4 v10, %[srdk], s40 offen offset:2048 nt lds\n"
  "buffer_load_dwordx4 v11, %[srdk], s40 offen offset:3072 nt lds\n"
  "s_add_u32 m0, s51, 81920\n"
  "s_add_u32 s40, s50, 16384\n"
  "buffer_load_dwordx4 v10, %[srdv], s40 offen offset:0 nt lds\n"
  "buffer_load_dwordx4 v11, %[srdv], s40 offen offset:1024 nt lds\n"
  "buffer_load_dwordx4 v10, %[srdv], s40 offen offset:2048 nt lds\n"
  "buffer_load_dwordx4 v11, %[srdv], s40 offen offset:3072 nt lds\n"
  "v_mov_b64 v[44:45], 0\n"
  "v_mov_b64 v[46:47], 0\n"
  "v_mov_b64 v[48:49], 0\n"
  "v_mov_b64 v[50:51], 0\n"
  "v_mov_b64 v[52:53], 0\n"
  "v_mov_b64 v[54:55], 0\n"
  "v_mov_b64 v[56:57], 0\n"
  "v_mov_b64 v[58:59], 0\n"
  "v_mov_b64 v[60:61], 0\n"
  "v_mov_b64 v[62:63], 0\n"
  "v_mov_b64 v[64:65], 0\n"
  "v_mov_b64 v[66:67], 0\n"
  "v_mov_b64 v[68:69], 0\n"
  "v_mov_b64 v[70:71], 0\n"
  "v_mov_b64 v[72:73], 0\n"
  "v_mov_b64 v[74:75], 0\n"
  "v_mov_b64 v[76:77], 0\n"
  "v_mov_b64 v[78:79], 0\n"
  "v_mov_b64 v[80:81], 0\n"
  "v_mov_b64 v[82:83], 0\n"
  "v_mov_b64 v[84:85], 0\n"
  "v_mov_b64 v[86:87], 0\n"
  "v_mov_b64 v[88:89], 0\n"
  "v_mov_b64 v[90:91], 0\n"
  "v_mov_b64 v[92:93], 0\n"
  "v_mov_b64 v[94:95], 0\n"
  "v_mov_b64 v[96:97], 0\n"
  "v_mov_b64 v[98:99], 0\n"
  "v_mov_b64 v[100:101], 0\n"
  "v_mov_b64 v[102:103], 0\n"
  "v_mov_b64 v[104:105], 0\n"
  "v_mov_b64 v[106:107], 0\n"
  "v_mov_b64 v[108:109], 0\n"
  "v_mov_b64 v[110:111], 0\n"
  "v_mov_b64 v[112:113], 0\n"
  "v_mov_b64 v[114:115], 0\n"
  "v_mov_b64 v[36:37], 0\n"
  "v_mov_b64 v[38:39], 0\n"
  "v_mov_b64 v[40:41], 0\n"
  "v_mov_b64 v[42:43], 0\n"
  "v_mov_b64 v[18:19], 0\n"
  "s_waitcnt vmcnt(8)\n"
  "s_barrier\n"
  "s_nop 7\n"
  "s_nop 3\n"
  "s_cmp_eq_u32 s37, 0\n"
  "s_cbranch_scc1 P1_RST0\n"
  "s_cmp_eq_u32 s37, 1\n"
  "s_cbranch_scc1 P1_RST1\n"
  "s_cmp_eq_u32 s37, 2\n"
  "s_cbranch_scc1 P1_RST2\n"
  "s_branch P1_RST3\n"
  "P1_END:\n"
  :
  : [srdx]"s"(srdx),[srdw]"s"(srdw),[srdk]"s"(srdk),[srdv]"s"(srdv),[srdo]"s"(srdo),[wave]"s"(wave),[ldsb]"s"(ldsb),[jx]"s"(jx),[bb]"s"(b),
    [srdk0]"s"(srdk[0]),[srdk1]"s"(srdk[1]),[srdk2]"s"(srdk[2]),[srdk3]"s"(srdk[3]),[srdv0]"s"(srdv[0]),[srdv1]"s"(srdv[1]),[srdv2]"s"(srdv[2]),[srdv3]"s"(srdv[3]),
    [srdwo]"s"(srdwo),[srdx2]"s"(srdx2),[srdout]"s"(srdout),[srdf]"s"(srdf),[grp]"s"(grp),[hg]"s"(hg)
  : "v1","v2","v3","v4","v5","v6","v7","v8","v9","v10","v11","v12","v13","v14","v15","v16","v17","v18","v19","v20","v21","v22","v23","v24","v25","v26","v27","v28","v29","v30","v31","v32","v33","v34","v35","v36","v37","v38","v39","v40","v41","v42","v43","v44","v45","v46","v47","v48","v49","v50","v51","v52","v53","v54","v55","v56","v57","v58","v59","v60","v61","v62","v63","v64","v65","v66","v67","v68","v69","v70","v71","v72","v73","v74","v75","v76","v77","v78","v79","v80","v81","v82","v83","v84","v85","v86","v87","v88","v89","v90","v91","v92","v93","v94","v95","v96","v97","v98","v99","v100","v101","v102","v103","v104","v105","v106","v107","v108","v109","v110","v111","v112","v113","v114","v115","v116","v117","v118","v119","v120","v121","v122","v123","v124","v125","v126","v127","v128","v129","v130","v131","v132","v133","v134","v135","v136","v137","v138","v139","v140","v141","v142","v143","v144","v145","v146","v147","v148","v149","v150","v151","v152","v153","v154","v155","v156","v157","v158","v159","v160","v161","v162","v163","v164","v165","v166","v167","v168","v169","v170","v171","v172","v173","v174","v175","v176","v177","v178","v179","v180","v181","v182","v183","v184","v185","v186","v187","v188","v189","v190","v191","v192","v193","v194","v195","v196","v197","v198","v199","v200","v201","v202","v203","v204","v205","v206","v207","v208","v209","v210","v211","v212","v213","v214","v215","v216","v217","v218","v219","v220","v221","v222","v223","v224","v225","v226","v227","v228","v229","v230","v231","v232","v233","v234","v235","v236","v237","v238","v239","v240","v241","v242","v243","v244","v245","v246","v247","v248","v249","v250","v251","v252","v253","v254","v255","s34","s35","s36","s37","s38","s39","s40","s41","s42","s43","s44","s45","s46","s47","s48","s49","s50","s51","s52","s53","s54","s55","s56","s57","s58","s59","s60","s61","s62","s63","scc","vcc","memory");
}

extern "C" void kernel_launch(void* const* d_in, const int* in_sizes, int n_in,
                              void* d_out, int out_size, void* d_ws, size_t ws_size,
                              hipStream_t stream) {
  (void)hipFuncSetAttribute((const void*)p1_kernel,hipFuncAttributeMaxDynamicSharedMemorySize,163840);
  p1_kernel<<<256,512,163840,stream>>>((const u16*)d_in[0],(const u16*)d_in[1],(const u16*)d_in[2],(const u16*)d_in[3],(const u16*)d_in[4],
                                       (const u16*)d_in[5],(const u16*)d_in[6],(const u16*)d_in[7],(const u16*)d_in[8],(const u16*)d_in[9],(char*)d_ws,(u16*)d_out);
}
```
